```python
import torch

def _softmax(x, axis=-1):
    return torch.softmax(x, dim=axis)
def _gelu(x):
    return torch.nn.functional.gelu(x, approximate="tanh")
def _silu(x):
    return torch.nn.functional.silu(x)
def _sigmoid(x):
    return torch.sigmoid(x)
def _layernorm(x, axis=-1, eps=1e-5):
    m = x.mean(dim=axis, keepdim=True)
    v = x.var(dim=axis, keepdim=True, unbiased=False)
    return (x - m) / torch.sqrt(v + eps)
def _scatter_add(tgt, idx, src, axis=0):
    return tgt.clone().index_add_(axis, idx, src)
def _segment_sum(src, seg, n):
    out = torch.zeros((n,) + src.shape[1:], dtype=src.dtype)
    return out.index_add_(0, seg, src)
def _pad(x, pw):
    flat = tuple(v for pair in reversed(pw) for v in pair)
    return torch.nn.functional.pad(x, flat)

kernel_name = 'recurrent_scan__ssm_conv_s5__dense_activation_relu__prompt'

def setup_inputs():

    _w = (lambda a: (lambda t: t.to(torch.bfloat16) if t.is_floating_point() else t)(torch.as_tensor(a)))
    def _A_setup():
        B, S, D = 1, 2048, 640
        P = D
        Lambda_re = -0.5 * (lambda _x: torch.exp(torch.as_tensor(_x)))((lambda *s: torch.randn(*s, dtype=torch.bfloat16) * 0.2)(P))
        Lambda_im = (lambda *s: torch.randn(*s, dtype=torch.bfloat16) * 0.4)(P) * 1.0
        B_re = (lambda *s: torch.randn(*s, dtype=torch.bfloat16) * 0.2)(P, D) * (D ** -0.5)
        B_im = (lambda *s: torch.randn(*s, dtype=torch.bfloat16) * 0.2)(P, D) * (D ** -0.5)
        C_re = (lambda *s: torch.randn(*s, dtype=torch.bfloat16) * 0.2)(D, P) * (P ** -0.5)
        C_im = (lambda *s: torch.randn(*s, dtype=torch.bfloat16) * 0.2)(D, P) * (P ** -0.5)
        log_dt = (lambda *s: torch.randn(*s, dtype=torch.bfloat16) * 0.2)(P) * 0.3 - 2.0
        Dskip = (lambda *s: torch.randn(*s, dtype=torch.bfloat16) * 0.05)(D)
        return {
            "x": _w((lambda *s: torch.randn(*s, dtype=torch.bfloat16) * 0.05)(B, S, D)),
            "Lambda_re": _w(Lambda_re),
            "Lambda_im": _w(Lambda_im),
            "B_re": _w(B_re),
            "B_im": _w(B_im),
            "C_re": _w(C_re),
            "C_im": _w(C_im),
            "log_dt": _w(log_dt),
            "Dskip": _w(Dskip),
        }
    _A_in = _A_setup()

    def _B_setup():
        B, S, D, Dff = 1, 2048, 640, 1728
        return {
            "x": _w((lambda *s: torch.randn(*s, dtype=torch.bfloat16) * 0.05)(B, S, D)),
            "w1": _w((lambda *s: torch.randn(*s, dtype=torch.bfloat16) * 0.05)(D, Dff)),
            "w2": _w((lambda *s: torch.randn(*s, dtype=torch.bfloat16) * 0.05)(Dff, D)),
        }
    _B_in = _B_setup()

    return {
        "A_x": _A_in["x"],
        "A_Lambda_re": _A_in["Lambda_re"],
        "A_Lambda_im": _A_in["Lambda_im"],
        "A_B_re": _A_in["B_re"],
        "A_B_im": _A_in["B_im"],
        "A_C_re": _A_in["C_re"],
        "A_C_im": _A_in["C_im"],
        "A_log_dt": _A_in["log_dt"],
        "A_Dskip": _A_in["Dskip"],
        "B_x": _B_in["x"],
        "B_w1": _B_in["w1"],
        "B_w2": _B_in["w2"],
    }

def reference(A_x, A_Lambda_re, A_Lambda_im, A_B_re, A_B_im, A_C_re, A_C_im, A_log_dt, A_Dskip, B_x, B_w1, B_w2):
    def _A_ref(x, Lambda_re, Lambda_im, B_re, B_im, C_re, C_im, log_dt, Dskip):
        Bsz, L, H = x.shape
        Lam = Lambda_re + 1j * Lambda_im
        Bmat = B_re + 1j * B_im
        Cmat = C_re + 1j * C_im
        dt = torch.exp(log_dt)
        Lam_bar = torch.exp(dt * Lam)
        B_bar = ((Lam_bar - 1.0) / Lam)[:, None] * Bmat
        Bu = torch.einsum("bld,pd->blp", x + 0j, B_bar)
        h = torch.zeros_like(Bu[:, 0, :])
        hs = []
        for t in range(L):
            h = Lam_bar[None, :] * h + Bu[:, t, :]
            hs.append(h)
        hseq = torch.stack(hs, 1)
        y = 2.0 * torch.einsum("blp,dp->bld", hseq, Cmat).real + x * Dskip[None, None, :]
        return y

    def _B_ref(x, w1, w2):
        h = torch.einsum("bsd,df->bsf", x, w1)
        zero = torch.zeros_like(h)
        h = torch.maximum(h, zero)
        return torch.einsum("bsf,fd->bsd", h, w2)

    x = A_x
    xn = _layernorm(x, axis=-1)
    s = _A_ref(xn, A_Lambda_re, A_Lambda_im, A_B_re, A_B_im, A_C_re, A_C_im, A_log_dt, A_Dskip)
    g = _silu(_B_ref(xn, B_w1, B_w2))
    return x + s * g
```

```cpp
#include <hip/hip_runtime.h>
#include <hip/hip_bf16.h>
typedef unsigned short u16;
__device__ __forceinline__ float bf2f(u16 v){ return __uint_as_float(((unsigned)v)<<16); }
__device__ __forceinline__ u16 f2bf(float f){ __hip_bfloat16 b = __float2bfloat16(f); return *reinterpret_cast<u16*>(&b); }
__device__ __forceinline__ float rbf(float f){ return bf2f(f2bf(f)); }
constexpr int L=2048, D=640, P=640, F=1728;

__global__ void k_ln(const u16* x, u16* xn){
  int row=blockIdx.x; int t=threadIdx.x;
  float v[10]; float s=0;
  for(int i=0;i<10;i++){ v[i]=bf2f(x[row*D+t+64*i]); s+=v[i]; }
  for(int o=32;o;o>>=1) s+=__shfl_xor(s,o);
  float m=s/D; float q=0;
  for(int i=0;i<10;i++){ float d=v[i]-m; q+=d*d; }
  for(int o=32;o;o>>=1) q+=__shfl_xor(q,o);
  float var=rbf(q/D); float mb=rbf(m);
  float den=rbf(sqrtf(rbf(var+1e-5f)));
  for(int i=0;i<10;i++){ float d=rbf(v[i]-mb); xn[row*D+t+64*i]=f2bf(d/den); }
}
__global__ void k_g1(const u16* xn,const u16* Bre,const u16* Bim,const u16* w1,float* ure,float* uim,u16* h1){
  int n=blockIdx.x*64+threadIdx.x; int l=blockIdx.y;
  __shared__ float xs[D];
  for(int i=threadIdx.x;i<D;i+=64) xs[i]=bf2f(xn[l*D+i]);
  __syncthreads();
  float acc=0;
  if(n<P){ for(int k=0;k<D;k++) acc+=xs[k]*bf2f(Bre[n*D+k]); ure[l*P+n]=acc; }
  else if(n<2*P){ int p=n-P; for(int k=0;k<D;k++) acc+=xs[k]*bf2f(Bim[p*D+k]); uim[l*P+p]=acc; }
  else { int f=n-2*P; for(int k=0;k<D;k++) acc+=xs[k]*bf2f(w1[k*F+f]); h1[l*F+f]=f2bf(fmaxf(rbf(acc),0.f)); }
}
__global__ void k_scan(const u16* Lre,const u16* Lim,const u16* logdt,float* ure,float* uim, int round_h){
  int p=blockIdx.x*64+threadIdx.x;
  float lr=bf2f(Lre[p]), li=bf2f(Lim[p]);
  float dt=rbf(expf(bf2f(logdt[p])));
  float er=expf(dt*lr); float ar=er*cosf(dt*li), ai=er*sinf(dt*li);
  float nr=ar-1.f, ni=ai; float dn=lr*lr+li*li;
  float cr=(nr*lr+ni*li)/dn, ci=(ni*lr-nr*li)/dn;
  float hr=0,hi=0;
  for(int t=0;t<L;t++){
    float r=ure[t*P+p], i=uim[t*P+p];
    float br=cr*r-ci*i, bi=cr*i+ci*r;
    float nhr=ar*hr-ai*hi+br, nhi=ar*hi+ai*hr+bi;
    hr=nhr; hi=nhi;
    ure[t*P+p]=round_h?rbf(hr):hr; uim[t*P+p]=round_h?rbf(hi):hi;
  }
}
__global__ void k_g2(const u16* x,const u16* xn,const float* hre,const float* him,const u16* h1,const u16* Cre,const u16* Cim,const u16* w2,const u16* Dsk,float* out){
  int d=blockIdx.x*64+threadIdx.x; int l=blockIdx.y;
  __shared__ float a[2*P+F];
  for(int i=threadIdx.x;i<P;i+=64){ a[i]=hre[l*P+i]; a[P+i]=him[l*P+i]; }
  for(int i=threadIdx.x;i<F;i+=64) a[2*P+i]=bf2f(h1[l*F+i]);
  __syncthreads();
  float y=0; for(int p=0;p<P;p++) y+=a[p]*bf2f(Cre[d*P+p])-a[P+p]*bf2f(Cim[d*P+p]);
  float g=0; for(int f=0;f<F;f++) g+=a[2*P+f]*bf2f(w2[f*D+d]);
  g=rbf(g); g=rbf(g/(1.f+expf(-g)));
  float s=2.f*y+rbf(bf2f(xn[l*D+d])*bf2f(Dsk[d]));
  out[l*D+d]=bf2f(x[l*D+d])+s*g;
}
extern "C" void kernel_launch(void* const* d_in, const int* in_sizes, int n_in,
                              void* d_out, int out_size, void* d_ws, size_t ws_size,
                              hipStream_t stream) {
  const u16* x=(const u16*)d_in[0]; const u16* Lre=(const u16*)d_in[1]; const u16* Lim=(const u16*)d_in[2];
  const u16* Bre=(const u16*)d_in[3]; const u16* Bim=(const u16*)d_in[4]; const u16* Cre=(const u16*)d_in[5]; const u16* Cim=(const u16*)d_in[6];
  const u16* logdt=(const u16*)d_in[7]; const u16* Dsk=(const u16*)d_in[8]; const u16* w1=(const u16*)d_in[10]; const u16* w2=(const u16*)d_in[11];
  char* ws=(char*)d_ws;
  u16* xn=(u16*)ws; float* ure=(float*)(ws+(4<<20)); float* uim=(float*)(ws+(12<<20)); u16* h1=(u16*)(ws+(20<<20));
  k_ln<<<L,64,0,stream>>>(x,xn);
  k_g1<<<dim3((2*P+F)/64,L),64,0,stream>>>(xn,Bre,Bim,w1,ure,uim,h1);
  k_scan<<<P/64,64,0,stream>>>(Lre,Lim,logdt,ure,uim,1);
  k_g2<<<dim3(D/64,L),64,0,stream>>>(x,xn,ure,uim,h1,Cre,Cim,w2,Dsk,(float*)d_out);
}
```

```cpp
#include <hip/hip_runtime.h>
#include <hip/hip_bf16.h>
typedef unsigned short u16;
__device__ __forceinline__ float bf2f(u16 v){ return __uint_as_float(((unsigned)v)<<16); }
__device__ __forceinline__ u16 f2bf(float f){ __hip_bfloat16 b = __float2bfloat16(f); return *reinterpret_cast<u16*>(&b); }
__device__ __forceinline__ float rbf(float f){ return bf2f(f2bf(f)); }
constexpr int L=2048, D=640, P=640, F=1728;

__global__ void k_ln(const u16* x, u16* xn){
  int row=blockIdx.x; int t=threadIdx.x;
  float v[10]; float s=0;
  for(int i=0;i<10;i++){ v[i]=bf2f(x[row*D+t+64*i]); s+=v[i]; }
  for(int o=32;o;o>>=1) s+=__shfl_xor(s,o);
  float m=s/D; float q=0;
  for(int i=0;i<10;i++){ float d=v[i]-m; q+=d*d; }
  for(int o=32;o;o>>=1) q+=__shfl_xor(q,o);
  float var=rbf(q/D); float mb=rbf(m);
  float den=rbf(sqrtf(rbf(var+1e-5f)));
  for(int i=0;i<10;i++){ float d=rbf(v[i]-mb); xn[row*D+t+64*i]=f2bf(d/den); }
}
__global__ void k_g1(const u16* xn,const u16* Bre,const u16* Bim,const u16* w1,float* ure,float* uim,u16* h1){
  int n=blockIdx.x*64+threadIdx.x; int l=blockIdx.y;
  __shared__ float xs[D];
  for(int i=threadIdx.x;i<D;i+=64) xs[i]=bf2f(xn[l*D+i]);
  __syncthreads();
  float acc=0;
  if(n<P){ for(int k=0;k<D;k++) acc+=xs[k]*bf2f(Bre[n*D+k]); ure[l*P+n]=acc; }
  else if(n<2*P){ int p=n-P; for(int k=0;k<D;k++) acc+=xs[k]*bf2f(Bim[p*D+k]); uim[l*P+p]=acc; }
  else { int f=n-2*P; for(int k=0;k<D;k++) acc+=xs[k]*bf2f(w1[k*F+f]); h1[l*F+f]=f2bf(fmaxf(rbf(acc),0.f)); }
}
__global__ void k_scan(const u16* Lre,const u16* Lim,const u16* logdt,float* ure,float* uim, int round_h){
  int p=blockIdx.x*64+threadIdx.x;
  float lr=bf2f(Lre[p]), li=bf2f(Lim[p]);
  float dt=rbf(expf(bf2f(logdt[p])));
  float er=expf(dt*lr); float ar=er*cosf(dt*li), ai=er*sinf(dt*li);
  float nr=ar-1.f, ni=ai; float dn=lr*lr+li*li;
  float cr=(nr*lr+ni*li)/dn, ci=(ni*lr-nr*li)/dn;
  float hr=0,hi=0;
  for(int t=0;t<L;t++){
    float r=ure[t*P+p], i=uim[t*P+p];
    float br=cr*r-ci*i, bi=cr*i+ci*r;
    float nhr=ar*hr-ai*hi+br, nhi=ar*hi+ai*hr+bi;
    hr=nhr; hi=nhi;
    ure[t*P+p]=round_h?rbf(hr):hr; uim[t*P+p]=round_h?rbf(hi):hi;
  }
}
__global__ void k_g2(const u16* x,const u16* xn,const float* hre,const float* him,const u16* h1,const u16* Cre,const u16* Cim,const u16* w2,const u16* Dsk,float* out){
  int d=blockIdx.x*64+threadIdx.x; int l=blockIdx.y;
  __shared__ float a[2*P+F];
  for(int i=threadIdx.x;i<P;i+=64){ a[i]=hre[l*P+i]; a[P+i]=him[l*P+i]; }
  for(int i=threadIdx.x;i<F;i+=64) a[2*P+i]=bf2f(h1[l*F+i]);
  __syncthreads();
  float y=0; for(int p=0;p<P;p++) y+=a[p]*bf2f(Cre[d*P+p])-a[P+p]*bf2f(Cim[d*P+p]);
  float g=0; for(int f=0;f<F;f++) g+=a[2*P+f]*bf2f(w2[f*D+d]);
  g=rbf(g); g=rbf(g/(1.f+expf(-g)));
  float s=2.f*y+rbf(bf2f(xn[l*D+d])*bf2f(Dsk[d]));
  out[l*D+d]=bf2f(x[l*D+d])+s*g;
}

typedef __attribute__((address_space(3))) void* ldsp;
typedef const __attribute__((address_space(1))) void* gp;
template<int DEPTH>
__global__ __launch_bounds__(512,2) void kprobe(const char* __restrict__ x,const char* __restrict__ Bre,const char* __restrict__ Bim,const char* __restrict__ w1,int G,int mult){
  extern __shared__ __attribute__((aligned(16))) char smem[];
  unsigned long long r0=__builtin_amdgcn_s_memrealtime();
  int wave=__builtin_amdgcn_readfirstlane(threadIdx.x>>6), lane=threadIdx.x&63;
  int xcd=blockIdx.x&7, r=blockIdx.x>>3;
  int J=32/G; int gsets=16/G;
  int c=(xcd%gsets)*G + r/J;
  int j=(xcd/gsets)*J + r%J;
  int i=j>>1, typeB=j&1;
  int nS=typeB?2:3; int s0=5*i+(typeB?3:0); int m0=7*i+(typeB?3:0);
  const char* src[5]; int stepb[5];
  #pragma unroll
  for(int b=0;b<5;b++){
    int q=wave*5+b;
    if(q<16){ int row=q*8+(lane>>3); src[b]=x+(size_t)(c*128+row)*1280+(lane&7)*16; stepb[b]=128; }
    else { int p=(q-16)>>2, sub=(q-16)&3;
      if(p<nS){ int row=sub*8+(lane>>3); int ch=(s0+p)*16+(row&15); src[b]=((row<16)?Bre:Bim)+(size_t)ch*1280+(lane&7)*16; stepb[b]=128; }
      else { int nM=6-nS; int idx=((p-nS)*4+sub)*64+lane; int cpr=nM*4; int krow=idx/cpr, cc=idx-krow*cpr;
        int col16=m0*4+cc; if(col16>=216) col16-=8;
        src[b]=w1+(size_t)krow*3456+col16*16; stepb[b]=64*3456; }
    }
  }
  auto issue=[&](int ch){
    char* lb=smem+(ch%DEPTH)*40960+wave*5120;
    #pragma unroll
    for(int b=0;b<5;b++) __builtin_amdgcn_global_load_lds((gp)(src[b]+(size_t)ch*stepb[b]),(ldsp)(lb+b*1024),16,0,0);
  };
  #pragma unroll
  for(int ch=0;ch<DEPTH-1;ch++) issue(ch);
  #pragma unroll
  for(int ch=0;ch<10;ch++){
    int rem=(ch+DEPTH-1<=10? DEPTH-2 : 10-1-ch);
    if(rem>=3) asm volatile("s_waitcnt vmcnt(15)":::"memory");
    else if(rem==2) asm volatile("s_waitcnt vmcnt(10)":::"memory");
    else if(rem==1) asm volatile("s_waitcnt vmcnt(5)":::"memory");
    else asm volatile("s_waitcnt vmcnt(0)":::"memory");
    __builtin_amdgcn_s_barrier();
    if(ch+DEPTH-1<10) issue(ch+DEPTH-1);
  }
  unsigned long long r2=__builtin_amdgcn_s_memrealtime();
  unsigned long long target=r2+(r2-r0)*mult;
  if(blockIdx.x==100) while(__builtin_amdgcn_s_memrealtime()<target) __builtin_amdgcn_s_sleep(10);
}

extern "C" void kernel_launch(void* const* d_in, const int* in_sizes, int n_in,
                              void* d_out, int out_size, void* d_ws, size_t ws_size,
                              hipStream_t stream) {
  const u16* x=(const u16*)d_in[0]; const u16* Lre=(const u16*)d_in[1]; const u16* Lim=(const u16*)d_in[2];
  const u16* Bre=(const u16*)d_in[3]; const u16* Bim=(const u16*)d_in[4]; const u16* Cre=(const u16*)d_in[5]; const u16* Cim=(const u16*)d_in[6];
  const u16* logdt=(const u16*)d_in[7]; const u16* Dsk=(const u16*)d_in[8]; const u16* w1=(const u16*)d_in[10]; const u16* w2=(const u16*)d_in[11];
  char* ws=(char*)d_ws;
  u16* xn=(u16*)ws; float* ure=(float*)(ws+(4<<20)); float* uim=(float*)(ws+(12<<20)); u16* h1=(u16*)(ws+(20<<20));
  hipFuncSetAttribute((const void*)kprobe<3>,hipFuncAttributeMaxDynamicSharedMemorySize,3*40960);
  kprobe<3><<<256,512,3*40960,stream>>>((const char*)x,(const char*)Bre,(const char*)Bim,(const char*)w1,4,0);
  k_ln<<<L,64,0,stream>>>(x,xn);
  k_g1<<<dim3((2*P+F)/64,L),64,0,stream>>>(xn,Bre,Bim,w1,ure,uim,h1);
  k_scan<<<P/64,64,0,stream>>>(Lre,Lim,logdt,ure,uim,1);
  k_g2<<<dim3(D/64,L),64,0,stream>>>(x,xn,ure,uim,h1,Cre,Cim,w2,Dsk,(float*)d_out);
}
```

```cpp
#include <hip/hip_runtime.h>

#define STAMPJ(id)
namespace s5k {
typedef unsigned short u16;
typedef unsigned int u32;
typedef unsigned long long u64;
typedef short bf16x8 __attribute__((ext_vector_type(8)));
typedef short s16x2 __attribute__((ext_vector_type(2)));
typedef float f32x4 __attribute__((ext_vector_type(4)));
typedef float f32x2 __attribute__((ext_vector_type(2)));
typedef u32 u32x4 __attribute__((ext_vector_type(4)));
typedef u32 u32x2 __attribute__((ext_vector_type(2)));
typedef __bf16 bf16x2 __attribute__((ext_vector_type(2)));
#define LAS __attribute__((address_space(3)))
#define GAS __attribute__((address_space(1)))
typedef LAS char* lptr;
typedef __amdgpu_buffer_rsrc_t rsrc_t;

constexpr u32 WS_STATS = 4096;
constexpr u32 WS_AGG = 65536;
constexpr u32 WS_F1 = 262144, WS_F2 = WS_F1 + 32768, WS_F3 = WS_F2 + 32768;
constexpr u32 WS_PROF = 512 << 10;
constexpr u32 WS_H1 = 1u << 20;
constexpr u32 WS_HS = 9u << 20;
constexpr u32 WS_CT = 16u << 20;
constexpr u32 WS_WT = 18u << 20;
constexpr u32 WS_END = 21u << 20;
constexpr int CHUNK = 40960;
constexpr int LD_STG = 143360, LD_ST2 = LD_STG + 2048, LD_CS = LD_ST2 + 1024, LD_AGW = LD_CS + 1024, LD_CIN = LD_AGW + 1536,
              LD_CNT = LD_CIN + 1536, LD_TRB = LD_CNT + 256, LD_TAB = LD_TRB + 4096, LD_END = LD_TAB + 48 * 160;
constexpr int LDS_BYTES = 163840;
static_assert(LD_END <= LDS_BYTES, "lds");
enum { T_A = 0, T_C = 1, T_8 = 2, T_32 = 6, T_128 = 10, T_512 = 14 };
enum { C_F1 = 0, C_B2 = 1, C_S1 = 2, C_S2 = 3, C_F2 = 4, C_G = 5, C_G2 = 6, C_Y = 7 };
constexpr int SC1 = 16;
#define GMAP 2
constexpr u32 SPIN_CAP = 1u << 16;

struct Args {
  const u16 *x, *lre, *lim, *bre, *bim, *cre, *cim, *ldt, *dsk, *w1, *w2;
  float* out;
  char* ws;
};

__device__ __forceinline__ float u2f(u32 v) { return __builtin_bit_cast(float, v); }
__device__ __forceinline__ u32 f2u(float v) { return __builtin_bit_cast(u32, v); }
__device__ __forceinline__ float bf2f(u32 v) { return __builtin_bit_cast(float, v << 16); }
__device__ __forceinline__ float bfhi(u32 v) { return __builtin_bit_cast(float, v & 0xffff0000u); }
__device__ __forceinline__ u32 pk2(float a, float b) { return __builtin_bit_cast(u32, __builtin_convertvector(f32x2{a, b}, bf16x2)); }
__device__ __forceinline__ float rbf(float a) { return bf2f(pk2(a, a)); }
__device__ __forceinline__ f32x4 unpk4(u32x2 v) { return f32x4{bf2f(v.x), bfhi(v.x), bf2f(v.y), bfhi(v.y)}; }
__device__ __forceinline__ f32x4 rbf4(f32x4 v) { return unpk4(u32x2{pk2(v.x, v.y), pk2(v.z, v.w)}); }
__device__ __forceinline__ u32 relu2(u32 v) {
  return __builtin_bit_cast(u32, __builtin_elementwise_max(__builtin_bit_cast(s16x2, v), s16x2{0, 0}));
}
__device__ __forceinline__ f32x2 cmul(f32x2 a, f32x2 b) {
  float x, y;
  asm("v_mul_f32 %0, %2, %4\n\tv_mul_f32 %1, %2, %5\n\tv_fma_f32 %0, -%3, %5, %0\n\tv_fma_f32 %1, %3, %4, %1"
      : "=&v"(x), "=&v"(y) : "v"(a.x), "v"(a.y), "v"(b.x), "v"(b.y));
  return f32x2{x, y};
}
__device__ __forceinline__ f32x2 cfma(f32x2 a, f32x2 b, f32x2 c) {
  float x, y;
  asm("v_fma_f32 %0, %2, %4, %6\n\tv_fma_f32 %1, %2, %5, %7\n\tv_fma_f32 %0, -%3, %5, %0\n\tv_fma_f32 %1, %3, %4, %1"
      : "=&v"(x), "=&v"(y) : "v"(a.x), "v"(a.y), "v"(b.x), "v"(b.y), "v"(c.x), "v"(c.y));
  return f32x2{x, y};
}
__device__ __forceinline__ rsrc_t mk_rsrc(const void* p, u32 bytes) { return __builtin_amdgcn_make_buffer_rsrc((void*)p, 0, bytes, 0x00020000); }
__device__ __forceinline__ f32x4 mfma(bf16x8 a, bf16x8 b, f32x4 c) { return __builtin_amdgcn_mfma_f32_16x16x32_bf16(a, b, c, 0, 0, 0); }
__device__ __forceinline__ bf16x8 asfrag(u32x4 v) { return __builtin_bit_cast(bf16x8, v); }
__device__ __forceinline__ void dma16(rsrc_t rs, u32 dst, u32 voff, u32 soff) {
  asm volatile("s_mov_b32 m0, %0\n\ts_nop 0\n\tbuffer_load_dwordx4 %1, %2, %3 offen lds" ::"s"(dst), "v"(voff), "s"(rs), "s"(soff) : "memory");
}
__device__ __forceinline__ void dma16_sc1(rsrc_t rs, u32 dst, u32 voff, u32 soff) {
  asm volatile("s_mov_b32 m0, %0\n\ts_nop 0\n\tbuffer_load_dwordx4 %1, %2, %3 offen sc1 lds" ::"s"(dst), "v"(voff), "s"(rs), "s"(soff) : "memory");
}
__device__ __forceinline__ void dma4_sc1(rsrc_t rs, u32 dst, u32 voff, u32 soff) {
  asm volatile("s_mov_b32 m0, %0\n\ts_nop 0\n\tbuffer_load_dword %1, %2, %3 offen sc1 lds" ::"s"(dst), "v"(voff), "s"(rs), "s"(soff) : "memory");
}
__device__ __forceinline__ lptr L(u32 addr) { return (lptr)(size_t)addr; }
__device__ __forceinline__ bf16x8 ld128(u32 p) { return *(LAS bf16x8*)L(p); }
__device__ __forceinline__ f32x2 ldc(u32 p) { return *(LAS f32x2*)L(p); }
__device__ __forceinline__ f32x4 ld4(u32 p) { return *(LAS f32x4*)L(p); }
__device__ __forceinline__ bf16x8 ldtr(u32 p0, u32 p1) {
  typedef short v4i16 __attribute__((ext_vector_type(4)));
  v4i16 a = __builtin_amdgcn_ds_read_tr16_b64_v4i16((LAS v4i16*)L(p0));
  v4i16 b = __builtin_amdgcn_ds_read_tr16_b64_v4i16((LAS v4i16*)L(p1));
  return __builtin_shufflevector(a, b, 0, 1, 2, 3, 4, 5, 6, 7);
}
__device__ __forceinline__ u32x4 gld(rsrc_t rs, u32 voff, u32 soff) { return __builtin_amdgcn_raw_buffer_load_b128(rs, voff, soff, SC1); }
__device__ __forceinline__ void gst(u32x4 v, rsrc_t rs, u32 voff) { __builtin_amdgcn_raw_buffer_store_b128(v, rs, voff, 0, SC1); }
template <int N> __device__ __forceinline__ void wait_vm() { asm volatile("s_waitcnt vmcnt(%0)" ::"i"(N) : "memory"); }
__device__ __forceinline__ void wait_lgkm0() { __builtin_amdgcn_s_waitcnt(0xc07f); asm volatile("" ::: "memory"); }
__device__ __forceinline__ void wg_barrier() {
  asm volatile("" ::: "memory");
  __builtin_amdgcn_s_barrier();
  asm volatile("" ::: "memory");
}
#define PIN(x) asm volatile("" : "+v"(x))
#define SCHED0() __builtin_amdgcn_sched_barrier(0)
__device__ __forceinline__ u32 cnt_add(int idx, int lane) {
  u32 old;
  const u32 addr = LD_CNT + idx * 4 + (lane == 0 ? 0 : 64), one = 1;
  asm volatile("s_waitcnt lgkmcnt(0)\n\ts_mov_b64 exec, 1\n\tds_add_rtn_u32 %0, %1, %2\n\ts_mov_b64 exec, -1\n\ts_waitcnt lgkmcnt(0)"
               : "=&v"(old) : "v"(addr), "v"(one) : "memory");
  return __builtin_amdgcn_readfirstlane(old);
}
__device__ __forceinline__ void cnt_wait(int idx, u32 want) {
  const u32 addr = LD_CNT + idx * 4;
#pragma unroll 1
  for (u32 s = 0; s < (SPIN_CAP << 4); s++) {
    u32 v;
    asm volatile("ds_read_b32 %0, %1\n\ts_waitcnt lgkmcnt(0)" : "=&v"(v) : "v"(addr) : "memory");
    if (__builtin_amdgcn_readfirstlane(v) >= want) break;
    __builtin_amdgcn_s_sleep(1);
  }
}
__device__ __forceinline__ void polln(rsrc_t wsr, u32 off, int mask, u32 ta, u32 tb, int lane) {
  const u32 vo = off + (lane & mask) * 128;
#pragma unroll 1
  for (u32 s = 0; s < SPIN_CAP; s++) {
    u32x2 v = __builtin_amdgcn_raw_buffer_load_b64(wsr, vo, 0, SC1);
    if (__all((v.x == ta) & (v.y == tb))) break;
    __builtin_amdgcn_s_sleep(2);
  }
  __builtin_amdgcn_fence(__ATOMIC_ACQUIRE, "wavefront");
  asm volatile("" ::: "memory");
}
__device__ __forceinline__ void put_flag(rsrc_t wsr, u32 off, u32 ta, u32 tb, int lane) {
  __builtin_amdgcn_raw_buffer_store_b64(u32x2{ta, tb}, wsr, lane == 0 ? off : WS_END, 0, SC1);
}
template <int CTRL> __device__ __forceinline__ float dpp(float x) {
  return __builtin_bit_cast(float, __builtin_amdgcn_mov_dpp(__builtin_bit_cast(int, x), CTRL, 0xf, 0xf, true));
}
__device__ __forceinline__ void rows4(float x, float (&r)[4]) {
  auto s = __builtin_amdgcn_permlane16_swap(f2u(x), f2u(x), false, false);
  const u32 e = s[0], o = s[1];
  auto te = __builtin_amdgcn_permlane32_swap(e, e, false, false);
  auto to = __builtin_amdgcn_permlane32_swap(o, o, false, false);
  const u32 r0 = te[0], r2 = te[1], r1 = to[0], r3 = to[1];
  r[0] = u2f(r0); r[1] = u2f(r1); r[2] = u2f(r2); r[3] = u2f(r3);
}
__device__ __forceinline__ float rows_sum(float x) {
  auto s = __builtin_amdgcn_permlane16_swap(f2u(x), f2u(x), false, false);
  const u32 a = s[0], b = s[1];
  x = u2f(a) + u2f(b);
  auto t = __builtin_amdgcn_permlane32_swap(f2u(x), f2u(x), false, false);
  const u32 c = t[0], d = t[1];
  return u2f(c) + u2f(d);
}
__device__ __forceinline__ float wave_sum(float v) {
  v += dpp<0xB1>(v); v += dpp<0x4E>(v); v += dpp<0x124>(v); v += dpp<0x128>(v);
  return rows_sum(v);
}
__device__ __forceinline__ float fexp(float x) {
  const float hi = x * 1.4426950408889634f;
  const float lo = __builtin_fmaf(x, 1.4426950408889634f, -hi) + x * 1.925963033500011e-8f;
  return __builtin_amdgcn_exp2f(hi) * (1.0f + lo * 0.6931471805599453f);
}
__device__ __forceinline__ void fsincos(float x, float& sn, float& cs) {
  if (__builtin_expect(__any(!(__builtin_fabsf(x) < 2000.f)), 0)) { sincosf(x, &sn, &cs); return; }
  const float n = __builtin_rintf(x * 0.6366197723675814f);
  float r = __builtin_fmaf(n, -1.5703125f, x);
  r = __builtin_fmaf(n, -4.837512969970703125e-4f, r);
  r = __builtin_fmaf(n, -7.54978995489188e-8f, r);
  const float r2 = r * r;
  const float s = __builtin_fmaf(r * r2, __builtin_fmaf(r2, __builtin_fmaf(r2, -1.9515295891e-4f, 8.3321608736e-3f), -1.6666654611e-1f), r);
  const float c = __builtin_fmaf(r2 * r2, __builtin_fmaf(r2, __builtin_fmaf(r2, 2.443315711809948e-5f, -1.388731625493765e-3f), 4.166664568298827e-2f),
                                 __builtin_fmaf(r2, -0.5f, 1.0f));
  const int k = (int)n;
  const float s1 = (k & 1) ? c : s, c1 = (k & 1) ? s : c;
  sn = (k & 2) ? -s1 : s1;
  cs = ((k + 1) & 2) ? -c1 : c1;
}
#define STAMP(id) asm volatile("; MARK " #id)
#define STAMPI(id)

template <bool IS_M, int NP>
__device__ __forceinline__ void phase1(const Args& a, int c, int j, int wave, int lane, u32& ta, u32& tb, u32x4 (&cf)[5][5]) {
  constexpr int NU = 2 * NP;
  constexpr bool TYPE_B = IS_M ? (NP == 4) : (NP == 2);
  constexpr int nS = TYPE_B ? 2 : 3, nM = 6 - nS, MP = nM * 64;
  const int wm = wave & 3;
  const int i2 = j >> 1;
  const int s0 = 5 * i2 + (TYPE_B ? 3 : 0);
  const int m0 = 7 * i2 + ((i2 & 1) ? (TYPE_B ? 3 : 0) : (TYPE_B ? 0 : 4));
  const int li = lane & 15, q = lane >> 4, r8 = lane >> 3, sl = lane & 7;
  const rsrc_t wsr = mk_rsrc(a.ws, WS_END);

  u32x2 ep;
  u32x4 xr0, xr1;
  u32 plr, pli, pdt;
  {
    const GAS char* xrow = (const GAS char*)a.x + (size_t)(c * 128 + j * 8 + wave) * 1280;
    asm volatile("global_load_dwordx2 %0, %1, %2 sc1" : "=v"(ep) : "v"(0), "s"(a.ws) : "memory");
    asm volatile("global_load_dwordx4 %0, %1, %2" : "=v"(xr0) : "v"(lane * 16), "s"(xrow) : "memory");
    asm volatile("global_load_dwordx4 %0, %1, %2 offset:1024" : "=v"(xr1) : "v"(li * 16), "s"(xrow) : "memory");
    if (!IS_M) {
      const int ch = (s0 * 16 + (q < NP ? lane : li)) * 2;
      asm volatile("global_load_ushort %0, %1, %2" : "=v"(plr) : "v"(ch), "s"(a.lre) : "memory");
      asm volatile("global_load_ushort %0, %1, %2" : "=v"(pli) : "v"(ch), "s"(a.lim) : "memory");
      asm volatile("global_load_ushort %0, %1, %2" : "=v"(pdt) : "v"(ch), "s"(a.ldt) : "memory");
    }
  }
  rsrc_t drs[5];
  u32 dvo[5], dso[5], dstep[5], ddst[5];
  {
    const rsrc_t brs = mk_rsrc(wm < 2 ? a.bre : a.bim, 640 * 1280);
    const u32 bvo = ((wm & 1) * 8 + r8) * 1280 + ((sl ^ ((wm & 1) * 4 + (r8 >> 1))) << 4);
#pragma unroll
    for (int b = 0; b < 5; b++) {
      if (!IS_M && b < 4) {
        drs[b] = mk_rsrc(a.x, 2048 * 1280);
        dvo[b] = (8 * (r8 >> 2) + (r8 & 3)) * 1280 + ((sl ^ ((b & 1) * 4 + (r8 >> 1))) << 4);
        dso[b] = (c * 128 + wm * 32 + (b & 1) * 16 + (b >> 1) * 4) * 1280;
        dstep[b] = 128;
        ddst[b] = (wm * 4 + b) * 1024;
      } else if (!IS_M || b < nS - 1) {
        const int p = IS_M ? b + 1 : 0;
        drs[b] = brs;
        dvo[b] = bvo;
        dso[b] = (s0 + p) * 16 * 1280;
        dstep[b] = 128;
        ddst[b] = 16384 + p * 4096 + wm * 1024;
      } else {
        const int t = b - (nS - 1);
        const int idx = t * 64 + lane;
        const int kr = idx / (4 * nM), cpos = idx - kr * (4 * nM);
        const int sw = TYPE_B ? ((kr & 3) | (((kr >> 3) & 1) << 2)) : ((kr >> 3) & 1);
        int col16 = m0 * 4 + (((cpos >> 1) ^ sw) << 1) + (cpos & 1);
        if (TYPE_B && col16 >= 216) col16 -= 8;
        drs[b] = mk_rsrc(a.w1, 640 * 3456);
        dvo[b] = kr * 3456 + col16 * 16;
        dso[b] = wm * 16 * 3456;
        dstep[b] = 64 * 3456;
        ddst[b] = 16384 + nS * 4096 + (wm * nM + t) * 1024;
      }
    }
  }
  int kc = c + j;
  kc -= kc >= 20 ? 20 : kc >= 10 ? 10 : 0;
  kc = kc == 10 ? 0 : kc;
  auto dma1 = [&](int bufoff, int b) { dma16_sc1(drs[b], bufoff + ddst[b], dvo[b], dso[b] + kc * dstep[b]); };
  auto issue = [&](int bufoff) {
#pragma unroll
    for (int b = 0; b < 5; b++) dma1(bufoff, b);
    kc = kc == 9 ? 0 : kc + 1;
  };
  issue(0);
  *(LAS u32*)L(LD_CNT + li * 4) = 0;
  const int rid = c * 2 + (j & 1);
  STAMP(1);

  int pr[NP];
#pragma unroll
  for (int p = 0; p < NP; p++) pr[p] = (p + wm) % NP;
  u32 xb[2][2], wb[2][NU];
  {
    const u32 xo = li * 128 + ((q ^ (li >> 1)) << 4);
    const int qq = li >> 2;
    const u32 rowb = 16384 + nS * 4096 + (8 * q + qq) * MP + (lane & 3) * 8;
    const int f = TYPE_B ? (qq | ((q & 1) << 2)) : (q & 1);
#pragma unroll
    for (int h = 0; h < 2; h++) {
#pragma unroll
      for (int kk = 0; kk < 2; kk++) {
        xb[h][kk] = h * 2 * CHUNK + wm * 4096 + (xo ^ (kk << 6));
        PIN(xb[h][kk]);
      }
#pragma unroll
      for (int u = 0; u < NU; u++) {
        wb[h][u] = h * 2 * CHUNK + (IS_M ? rowb + (((2 * pr[u >> 1] + (u & 1)) ^ f) << 5) : 16384 + pr[u >> 1] * 4096 + (xo ^ ((u & 1) << 6)));
        PIN(wb[h][u]);
      }
    }
  }
  bf16x8 xf[2][2], wf[2][NU];
  auto read1 = [&](int set, int bufoff, int kk, int f) {
    const int h = bufoff == 2 * CHUNK, bo = h ? 0 : bufoff;
    if (f < 2) {
      xf[set][f] = ld128(xb[h][kk] + (bo + f * 2048));
    } else if (f < NU + 2) {
      const int u = f - 2;
      if (!IS_M) wf[set][u] = ld128(wb[h][(u & ~1) + kk] + (bo + (u & 1) * 2048));
      else wf[set][u] = ldtr(wb[h][u] + (bo + kk * 32 * MP), wb[h][u] + (bo + kk * 32 * MP + 4 * MP));
    }
  };
  f32x4 acc[2][NU], csum[2];
#pragma unroll
  for (int m = 0; m < 2; m++) {
#pragma unroll
    for (int u = 0; u < NU; u++) { acc[m][u] = f32x4{0.f, 0.f, 0.f, 0.f}; PIN(acc[m][u]); }
    csum[m] = f32x4{0.f, 0.f, 0.f, 0.f};
    PIN(csum[m]);
  }
  u32x4 ones4 = {0x3f803f80u, 0x3f803f80u, 0x3f803f80u, 0x3f803f80u};
  PIN(ones4);
  constexpr int G = NU + 1;
  constexpr int NDB = IS_M ? 2 : 3;
  auto dpos = [](int d) { return (2 * G * d + (IS_M ? G : 0)) / 5; };
  static_assert((2 * G * (NDB - 1) + (IS_M ? G : 0)) / 5 < G && (2 * G * NDB + (IS_M ? G : 0)) / 5 >= G, "split");
  auto step = [&](int set, int rbuf, int rkk, int dbuf) {
#pragma unroll
    for (int g = 0; g < G; g++) {
      if (g > 0) {
        const int u = g - 1;
#pragma unroll
        for (int m = 0; m < 2; m++)
          acc[m][u] = IS_M ? mfma(wf[set][u], xf[set][m], acc[m][u]) : mfma(xf[set][m], wf[set][u], acc[m][u]);
      } else {
        csum[0] = mfma(asfrag(ones4), wf[set][0], csum[0]);
        csum[1] = mfma(asfrag(ones4), wf[set][1], csum[1]);
      }
      if (rbuf >= 0) {
        if (g == 0) { read1(set ^ 1, rbuf, rkk, 2); read1(set ^ 1, rbuf, rkk, 3); }
        else if (g == 1) { read1(set ^ 1, rbuf, rkk, 0); read1(set ^ 1, rbuf, rkk, 1); }
        else read1(set ^ 1, rbuf, rkk, g + 2);
      } else if (g == 3) {
        LAS float* csp = (LAS float*)L(LD_CS + (IS_M ? 512 : 0) + (wm < NP ? wm : 3) * 128 + (lane & 31) * 4);
        float e0 = csum[0].x, e1 = csum[1].x;
        PIN(e0); PIN(e1);
        if (wm < NP) *csp = (q & 1) ? e1 : e0;
      }
#pragma unroll
      for (int d = 0; d < 5; d++)
        if ((set == 1 ? g : g + G) == dpos(d)) dma1(dbuf, d);
      SCHED0();
    }
  };

  asm volatile("s_waitcnt vmcnt(5)" : "+v"(ep), "+v"(xr0), "+v"(xr1)::"memory");
  if (!IS_M) asm volatile("" : "+v"(plr), "+v"(pli), "+v"(pdt));
  {
    u64 z = ((u64)__builtin_amdgcn_readfirstlane(ep.y) << 32 | __builtin_amdgcn_readfirstlane(ep.x)) + 0x9E3779B97F4A7C15ull;
    z = (z ^ (z >> 30)) * 0xBF58476D1CE4E5B9ull;
    z = (z ^ (z >> 27)) * 0x94D049BB133111EBull;
    z ^= z >> 31;
    ta = (u32)z;
    tb = (u32)(z >> 32);
    float v[16];
#pragma unroll
    for (int e = 0; e < 4; e++) {
      v[2 * e] = bf2f(xr0[e]); v[2 * e + 1] = bfhi(xr0[e]);
      v[8 + 2 * e] = bf2f(xr1[e]); v[8 + 2 * e + 1] = bfhi(xr1[e]);
    }
    float s0_ = 0.f, s1_ = 0.f;
#pragma unroll
    for (int e = 0; e < 8; e++) { s0_ += v[e]; s1_ += v[8 + e]; }
    const float mean = wave_sum(lane < 16 ? s0_ + s1_ : s0_) * (1.0f / 640.0f);
    s0_ = 0.f; s1_ = 0.f;
#pragma unroll
    for (int e = 0; e < 8; e++) {
      const float d0 = v[e] - mean, d1 = v[8 + e] - mean;
      s0_ += d0 * d0; s1_ += d1 * d1;
    }
    const float var = wave_sum(lane < 16 ? s0_ + s1_ : s0_) * (1.0f / 640.0f);
    const float mb = rbf(mean), den = rbf(__builtin_amdgcn_sqrtf(rbf(rbf(var) + 1e-5f)));
    gst(u32x4{f2u(mb), ta, f2u(__builtin_amdgcn_rcpf(den)), tb}, wsr, lane == 0 ? WS_STATS + (c * 128 + j * 8 + wave) * 16 : WS_END);
  }
  if (!IS_M) {
    if (wm == 0) {
      const float lr = bf2f(plr), lim = bf2f(pli);
      const float dt = rbf(fexp(bf2f(pdt)));
      const float er = fexp(dt * lr);
      float sn, cn;
      fsincos(dt * lim, sn, cn);
      const f32x2 a1 = {er * cn, er * sn};
      const float nr = a1.x - 1.0f, dn = 1.0f / (lr * lr + lim * lim);
      const u32 tp = LD_TAB + (lane < 48 ? lane : 47) * 160;
      if (q < NP) {
        *(LAS f32x4*)L(tp) = f32x4{a1.x, a1.y, (nr * lr + a1.y * lim) * dn, (a1.y * lr - nr * lim) * dn};
        const f32x2 a2 = cmul(a1, a1), a4 = cmul(a2, a2);
        f32x2 b = cmul(a4, a4);
#pragma unroll
        for (int i = 0; i < 4; i++) {
          const f32x2 b2 = cmul(b, b), b3 = cmul(b2, b);
          *(LAS f32x4*)L(tp + 16 + i * 32) = f32x4{1.f, 0.f, b.x, b.y};
          *(LAS f32x4*)L(tp + 32 + i * 32) = f32x4{b2.x, b2.y, b3.x, b3.y};
          b = cmul(b2, b2);
        }
      }
    }
  }
  issue(CHUNK);
#pragma unroll
  for (int d = 0; d < NDB; d++) dma1(2 * CHUNK, d);
  u32x4 rt[2];
  const int rks = min(rid * 2 + (wm & 1), 53);
  if (!IS_M) {
#pragma unroll
    for (int i = 0; i < 2; i++) {
      const int f = min(rid * 8 + wm * 2 + i, 199);
      const int wk = f / 5, n = f - wk * 5, w = wk / 10, k = wk - w * 10;
      const GAS char* src = (const GAS char*)(k < 5 ? a.cre : a.cim) + (i2 * 80 + n * 16) * 1280 + (160 * w + 32 * (k < 5 ? k : k - 5)) * 2;
      asm volatile("global_load_dwordx4 %0, %1, %2" : "=v"(rt[i]) : "v"(li * 1280 + q * 16), "s"(src) : "memory");
    }
    wait_vm<6 + NDB + 2>();
  } else {
    wait_vm<6 + NDB>();
  }
  wait_lgkm0();
  wg_barrier();
  STAMP(2);
  if (IS_M) __builtin_amdgcn_s_setprio(1);
#pragma unroll
  for (int f = 0; f < NU + 2; f++) read1(0, 0, 0, f);
  int it = 0;
  auto body = [&](int prv, int cur, int nxt) {
    step(0, cur, 1, prv);
    kc = kc == 9 ? 0 : kc + 1;
    if (nxt < 0) {
      const int r = wave * 16 + li;
      u32x4 g = *(LAS u32x4*)L(LD_STG + r * 16);
      if (!__all((g.y == ta) & (g.w == tb))) {
#pragma unroll 1
        for (u32 s = 0; s < SPIN_CAP; s++) {
          g = gld(wsr, WS_STATS + (c * 128 + r) * 16, 0);
          if (__all((g.y == ta) & (g.w == tb))) break;
        }
      }
      const int t5 = r & 31;
      *(LAS f32x2*)L(LD_ST2 + ((r & ~31) + (((t5 >> 2) & 1) << 4) + ((t5 >> 3) << 2) + (t5 & 3)) * 8) = f32x2{u2f(g.x), u2f(g.z)};
    } else if (!IS_M && it == 0) wait_vm<7>();
    else if (IS_M && it == 1 && wm < 2) wait_vm<10>();
    else wait_vm<5>();
    wait_lgkm0();
    wg_barrier();
    STAMPI(3 + it);
    if (IS_M && it == 0 && wm < 2) {
      const rsrc_t w2r = mk_rsrc(a.w2, 1728 * 1280);
#pragma unroll
      for (int b = 0; b < 5; b++) {
        const int idx = b * 64 + lane, kr = idx / 10, cpos = idx - kr * 10;
        dma16(w2r, 3 * CHUNK + wm * 5120 + b * 1024, kr * 1280 + i2 * 160 + cpos * 16, rks * (32 * 1280));
      }
    }
    if (it == 2) {
      if (!IS_M) {
        asm volatile("" : "+v"(rt[0]), "+v"(rt[1]));
#pragma unroll
        for (int i = 0; i < 2; i++) gst(rt[i], wsr, WS_CT + (i2 * 200 + min(rid * 8 + wm * 2 + i, 199)) * 1024 + lane * 16);
      } else if (wm < 2) {
        const u32 p = 3 * CHUNK + wm * 5120 + (4 * q + (li >> 2)) * 160 + (lane & 3) * 8;
#pragma unroll
        for (int n = 0; n < 5; n++)
          gst(__builtin_bit_cast(u32x4, ldtr(p + n * 32, p + n * 32 + 2560)), wsr, WS_WT + ((i2 * 54 + rks) * 5 + n) * 1024 + lane * 16);
      }
    }
    if (IS_M && it == 3 && wm == 3) put_flag(wsr, WS_F3 + (i2 * 32 + rid) * 128, ta, tb, lane);
    if (it >= 7) {
      if (it == 7) {
        const u32 f = *(LAS u32*)L(LD_TRB + wave * 256 + lane * 4);
        if (!__all(f == ((lane & 1) ? tb : ta))) polln(wsr, WS_F3 + i2 * 32 * 128, 31, ta, tb, lane);
#pragma unroll
        for (int b = 0; b < 5; b++) {
          drs[b] = wsr; dvo[b] = lane * 16; dso[b] = WS_WT + i2 * 54 * 5120 + b * 1024; dstep[b] = 5120; ddst[b] = wave * 5120 + b * 1024;
        }
      }
      kc = wm * 14 + (cur / CHUNK) * 2 + (IS_M ? 1 : 0);
    }
    SCHED0();
    step(1, nxt, 0, cur);
    if (it == 5) {
      dma4_sc1(wsr, LD_TRB + wave * 256, (lane >> 1) * 128 + (lane & 1) * 4, WS_F3 + i2 * 32 * 128);
      dma4_sc1(wsr, LD_STG + wave * 256, lane * 4, WS_STATS + c * 2048 + wave * 256);
    }
    it++;
  };
#pragma unroll 1
  for (int r = 0; r < 3; r++) {
    body(2 * CHUNK, 0, CHUNK);
    body(0, CHUNK, 2 * CHUNK);
    body(CHUNK, 2 * CHUNK, 0);
  }
  body(2 * CHUNK, 0, -1);
#pragma unroll
  for (int d = NDB; d < 5; d++) dma1(0, d);
  if (IS_M) __builtin_amdgcn_s_setprio(0);
  else __builtin_amdgcn_s_setprio(2);
  STAMP(3);
  float mb[2];
  f32x4 st[4];
  f32x2 ca[NP], cc[NP], a8r[NP];
  u32 tp[NP];
  if (IS_M) {
#pragma unroll
    for (int m = 0; m < 2; m++) mb[m] = *(LAS float*)L(LD_ST2 + (wm * 32 + m * 16 + li) * 8);
  } else {
#pragma unroll
    for (int i = 0; i < 4; i++) st[i] = ld4(LD_ST2 + (wm * 32 + (i >> 1) * 16 + 4 * q + 2 * (i & 1)) * 8);
#pragma unroll
    for (int p = 0; p < NP; p++) {
      tp[p] = LD_TAB + (pr[p] * 16 + li) * 160;
      const f32x4 t0 = ld4(tp[p]);
      ca[p] = f32x2{t0.x, t0.y};
      cc[p] = f32x2{t0.z, t0.w};
      a8r[p] = ldc(tp[p] + (T_8 + 3 - q) * 8);
    }
  }
  wait_lgkm0();
  wg_barrier();
  STAMP(4);

  if (IS_M) {
#pragma unroll
    for (int p = 0; p < NP; p++) {
      const f32x4 c0 = ld4(LD_CS + 512 + pr[p] * 128 + q * 16), c1 = ld4(LD_CS + 512 + pr[p] * 128 + 64 + q * 16);
      const u32 off = (m0 + pr[p] < 54) ? WS_H1 + ((c * 8 + 2 * wm) * 54 + m0 + pr[p]) * 1024 : WS_END;
#pragma unroll
      for (int m = 0; m < 2; m++) {
        const f32x4 v0 = acc[m][2 * p] - mb[m] * c0, v1 = acc[m][2 * p + 1] - mb[m] * c1;
        gst(u32x4{relu2(pk2(v0.x, v0.y)), relu2(pk2(v0.z, v0.w)), relu2(pk2(v1.x, v1.y)), relu2(pk2(v1.z, v1.w))}, wsr,
            off + m * 54 * 1024 + lane * 16);
      }
    }
  } else {
    f32x2 u[NP][8];
    f32x2 seg[NP];
    float Sr[NP], Si[NP];
#pragma unroll
    for (int p = 0; p < NP; p++) {
      Sr[p] = *(LAS float*)L(LD_CS + pr[p] * 128 + li * 4);
      Si[p] = *(LAS float*)L(LD_CS + pr[p] * 128 + 64 + li * 4);
    }
#pragma unroll
    for (int p = 0; p < NP; p++) {
      f32x2 h;
#pragma unroll
      for (int t = 0; t < 8; t++) {
        const float mu = st[t >> 1][(t & 1) * 2], rs = st[t >> 1][(t & 1) * 2 + 1];
        u[p][t] = f32x2{(acc[t >> 2][2 * p][t & 3] - mu * Sr[p]) * rs, (acc[t >> 2][2 * p + 1][t & 3] - mu * Si[p]) * rs};
        h = t ? cfma(ca[p], h, u[p][t]) : u[p][0];
      }
      seg[p] = h;
      h = cmul(a8r[p], h);
      *(LAS f32x2*)L(LD_AGW + ((wm * 3 + pr[p]) * 16 + li) * 8) = f32x2{rows_sum(h.x), rows_sum(h.y)};
    }
    if (cnt_add(C_S1, lane) == 3) {
      const int ch = lane < 48 ? lane : 47;
      const f32x2 a32 = ldc(LD_TAB + ch * 160 + (T_32 + 1) * 8);
      f32x2 h = ldc(LD_AGW + ch * 8);
#pragma unroll
      for (int w = 1; w < 4; w++) h = cfma(a32, h, ldc(LD_AGW + (w * 48 + ch) * 8));
      gst(u32x4{f2u(h.x), ta, f2u(h.y), tb}, wsr, q < NP ? WS_AGG + (c * 640 + s0 * 16 + lane) * 16 : WS_END);
    }
    STAMP(5);
    {
      const u32 co = WS_CT + (i2 * 200 + wm * 50) * 1024 + lane * 16;
#pragma unroll
      for (int k = 0; k < 5; k++)
#pragma unroll
        for (int n = 0; n < 5; n++) cf[k][n] = gld(wsr, co, (k * 5 + n) * 1024);
      SCHED0();
      wait_vm<25>();
      cnt_add(C_B2, lane);
    }
    cnt_wait(C_S1, 4);
    f32x2 aq[NP], pre[NP];
    u32 hso[NP];
#pragma unroll
    for (int p = 0; p < NP; p++) {
      const f32x2 a32 = ldc(tp[p] + (T_32 + 1) * 8), a8q = ldc(tp[p] + (T_8 + q) * 8), aw = ldc(tp[p] + (T_32 + wm) * 8);
      f32x2 h = {0.f, 0.f};
#pragma unroll
      for (int w = 0; w < 3; w++) {
        const f32x2 g = ldc(LD_AGW + ((w * 3 + pr[p]) * 16 + li) * 8);
        if (w < wm) h = cfma(a32, h, g);
      }
#pragma unroll
      for (int t = 0; t < 8; t++) u[p][t] = cmul(cc[p], u[p][t]);
      {
        const f32x2 a8 = ldc(tp[p] + (T_8 + 1) * 8);
        float gr[4], gi[4];
        rows4(seg[p].x, gr); rows4(seg[p].y, gi);
#pragma unroll
        for (int w = 0; w < 3; w++) {
          const f32x2 t = cfma(a8, h, f32x2{gr[w], gi[w]});
          h.x = w < q ? t.x : h.x;
          h.y = w < q ? t.y : h.y;
          PIN(h);
        }
      }
      pre[p] = cmul(cc[p], h);
      aq[p] = cmul(cc[p], cmul(a8q, aw));
      const int cu = s0 + pr[p];
      hso[p] = WS_HS + ((c * 8 + 2 * wm) * 40 + (cu / 10) * 10 + ((cu % 10) >> 1) + (lane >> 5) * 5) * 1024 + (cu & 1) * 512 + (lane & 31) * 16;
      asm volatile("" : "+v"(pre[p]), "+v"(aq[p]), "+v"(hso[p]), "+v"(u[p][0]), "+v"(u[p][1]), "+v"(u[p][2]), "+v"(u[p][3]), "+v"(u[p][4]),
                   "+v"(u[p][5]), "+v"(u[p][6]), "+v"(u[p][7]));
    }
    u32 tw = LD_TRB + wm * 1024 + (li >> 3) * 256 + q * 64 + (li & 7) * 2, tr = LD_TRB + wm * 1024 + lane * 16;
    PIN(tw); PIN(tr);
    cnt_wait(C_S2, 4);
    STAMP(6);
#pragma unroll
    for (int p = 0; p < NP; p++) {
      const u32 ci = LD_CIN + (pr[p] * 16 + li) * 8;
      f32x2 h = (ldc(ci) + ldc(ci + 384)) + (ldc(ci + 768) + ldc(ci + 1152));
      h = cfma(aq[p], h, pre[p]);
#pragma unroll
      for (int m = 0; m < 2; m++) {
#pragma unroll
        for (int r = 0; r < 4; r++) {
          h = cfma(ca[p], h, u[p][4 * m + r]);
          const u32 pk = pk2(h.x, -h.y);
          *(LAS u16*)L(tw + r * 16) = (u16)pk;
          *(LAS u16*)L(tw + r * 16 + 512) = (u16)(pk >> 16);
        }
        asm volatile("" ::: "memory");
        gst(*(LAS u32x4*)L(tr), wsr, hso[p] + m * 40 * 1024);
        asm volatile("" ::: "memory");
      }
    }
    if (c == 15 && j == 0 && wm == 0) {
      const u64 e = ((u64)ep.y << 32 | ep.x) + 1;
      __builtin_amdgcn_raw_buffer_store_b64(u32x2{(u32)e, (u32)(e >> 32)}, wsr, lane == 0 ? 0 : WS_END, 0, SC1);
    }
  }
}

__device__ __forceinline__ void phase2_m(const Args& a, int c, int j, int wave, int lane, u32 ta, u32 tb) {
  const int wm = wave & 3, li = lane & 15, q = lane >> 4;
  const rsrc_t wsr = mk_rsrc(a.ws, WS_END);
  const int rh = j & 1, i2 = j >> 1, dcol0 = i2 * 80;
  const u32 wo = WS_WT + (i2 * 54 + wm * 14) * 5120;
#pragma unroll
  for (int b = 0; b < 5; b++) dma16_sc1(wsr, (24 + wm) * 5120 + b * 1024, lane * 16, wo + (30 + b) * 1024);
  const int cp = 4 * wm + q, e = c - 1 - cp, s0 = 5 * i2 + (rh ? 3 : 0), np = rh ? 2 : 3;
  f32x2 pw[3];
#pragma unroll
  for (int p = 0; p < 3; p++) {
    const u32 tp = LD_TAB + ((p < np ? p : 0) * 16 + li) * 160;
    pw[p] = cmul(ldc(tp + (T_128 + (e & 3)) * 8), ldc(tp + (T_512 + ((e >> 2) & 3)) * 8));
    if (e < 0) pw[p] = f32x2{0.f, 0.f};
  }
  wait_vm<5>();
  if (cnt_add(C_F1, lane) == 3) put_flag(wsr, WS_F1 + (c * 16 + j) * 128, ta, tb, lane);
  STAMP(5);
  {
    u32x4 g[3];
    const u32 go = WS_AGG + (cp * 640 + s0 * 16 + li) * 16;
#pragma unroll 1
    for (u32 s = 0; s < SPIN_CAP; s++) {
      bool ok = true;
#pragma unroll
      for (int p = 0; p < 3; p++) g[p] = gld(wsr, go, (p < np ? p : 0) * 256);
#pragma unroll
      for (int p = 0; p < 3; p++) ok = ok & (g[p].y == ta) & (g[p].w == tb);
      if (__all(ok | (e < 0))) break;
      __builtin_amdgcn_s_sleep(1);
    }
#pragma unroll
    for (int p = 0; p < 3; p++) {
      const f32x2 h = cmul(pw[p], e < 0 ? f32x2{0.f, 0.f} : f32x2{u2f(g[p].x), u2f(g[p].z)});
      *(LAS f32x2*)L(LD_CIN + ((wm * 3 + p) * 16 + li) * 8) = f32x2{rows_sum(h.x), rows_sum(h.y)};
    }
    cnt_add(C_S2, lane);
    cnt_add(C_B2, lane);
  }
  STAMP(6);
  u32x4 w[3][5];
  auto ldw = [&](int r, int k) {
#pragma unroll
    for (int n = 0; n < 5; n++) w[r][n] = gld(wsr, lane * 16, wo + (((k < 12 || wm < 3) ? k : 0) * 5 + n) * 1024);
  };
  polln(wsr, WS_F1 + (c * 16 + 4 * wm) * 128, 3, ta, tb, lane);
  STAMP(7);
  f32x4 g[4][5];
  {
    u32 wbase[3];
#pragma unroll
    for (int h = 0; h < 3; h++) {
      wbase[h] = wm * 5120 + h * 61440 + lane * 16;
      PIN(wbase[h]);
    }
    auto rd = [&](int r, int slot) {
#pragma unroll
      for (int n = 0; n < 5; n++) w[r][n] = __builtin_bit_cast(u32x4, ld128(wbase[slot / 3] + ((slot % 3) * 20480 + n * 1024)));
    };
    auto rs = [](int s) { return s < 7 ? (s & 1) : 2 - (s - 7) % 3; };
    u32x4 hf[4][4];
    const u32 hb = WS_H1 + ((c * 8 + 4 * rh) * 54 + wm * 14) * 1024 + lane * 16;
    auto lda = [&](int s) {
#pragma unroll
      for (int m = 0; m < 4; m++) hf[s & 3][m] = gld(wsr, hb, (m * 54 + ((s < 12 || wm < 3) ? s : 0)) * 1024);
    };
#pragma unroll
    for (int s = 0; s < 4; s++) lda(s);
    ldw(2, 7);
    SCHED0();
#pragma unroll
    for (int m = 0; m < 4; m++)
#pragma unroll
      for (int n = 0; n < 5; n++) g[m][n] = f32x4{0.f, 0.f, 0.f, 0.f};
    cnt_wait(C_B2, 8);
    STAMPJ(1);
    rd(0, 0);
#pragma unroll
    for (int s = 0; s < 14; s++) {
      if (s < 12 || wm < 3) {
        if (s + 1 < 7) rd(rs(s + 1), s + 1);
#pragma unroll
        for (int n = 0; n < 5; n++)
#pragma unroll
          for (int m = 0; m < 4; m++) g[m][n] = mfma(asfrag(w[rs(s)][n]), asfrag(hf[s & 3][m]), g[m][n]);
        SCHED0();
        if (s < 11) STAMPJ(2 + s);
        if (s + 4 < 14) lda(s + 4);
        if (s >= 5 && s + 3 < 14) ldw(rs(s), s + 3);
        SCHED0();
      }
    }
  }
  STAMP(8);
  const int tt = 4 * rh + wm;
  const int tok = c * 128 + (tt >> 1) * 32 + 8 * (li >> 2) + 4 * (tt & 1) + (li & 3);
  u32x2 xv[5], dv[5];
#pragma unroll
  for (int n = 0; n < 5; n++) {
    xv[n] = *(const GAS u32x2*)((const GAS u16*)a.x + tok * 640 + dcol0 + 16 * n + 4 * q);
    dv[n] = *(const GAS u32x2*)((const GAS u16*)a.dsk + dcol0 + 16 * n + 4 * q);
  }
#pragma unroll
  for (int m = 0; m < 4; m++)
#pragma unroll
    for (int n = 0; n < 5; n++) *(LAS f32x4*)L((m * 4 + wm) * 5120 + n * 1024 + lane * 16) = g[m][n];
  cnt_add(C_G, lane);
  f32x4 gate[5], skip[5], xs[5];
  const f32x2 st = ldc(LD_ST2 + (tt * 16 + li) * 8);
#pragma unroll
  for (int n = 0; n < 5; n++) {
    xs[n] = unpk4(xv[n]);
    skip[n] = rbf4(rbf4(rbf4(xs[n] - st.x) * st.y) * unpk4(dv[n]));
    PIN(skip[n]);
  }
  cnt_wait(C_G, 4);
  STAMP(9);
#pragma unroll
  for (int n = 0; n < 5; n++) {
    const u32 gp = wm * 20480 + n * 1024 + lane * 16;
    gate[n] = (ld4(gp) + ld4(gp + 5120)) + (ld4(gp + 10240) + ld4(gp + 15360));
    PIN(gate[n]);
  }
  cnt_add(C_G2, lane);
#pragma unroll
  for (int n = 0; n < 5; n++) {
    const f32x4 gb = rbf4(gate[n] * st.y);
    f32x4 sg;
#pragma unroll
    for (int e2 = 0; e2 < 4; e2++) sg[e2] = gb[e2] * __builtin_amdgcn_rcpf(1.0f + __builtin_amdgcn_exp2f(gb[e2] * -1.4426950408889634f));
    gate[n] = rbf4(sg);
    PIN(gate[n]);
  }
  STAMP(11);
  cnt_wait(C_Y, 4);
  STAMP(12);
#pragma unroll
  for (int n = 0; n < 5; n++) {
    const u32 yp = (wm * 5 + n) * 1024 + lane * 16;
    const f32x4 y = (ld4(yp) + ld4(yp + 20480)) + (ld4(yp + 40960) + ld4(yp + 61440));
    *(GAS f32x4*)((GAS float*)a.out + tok * 640 + dcol0 + 16 * n + 4 * q) = xs[n] + (2.0f * y + skip[n]) * gate[n];
  }
  STAMP(13);
}

__device__ __forceinline__ void phase2_s(const Args& a, int c, int j, int wave, int lane, u32 ta, u32 tb, u32x4 (&cf)[5][5]) {
  const int wm = wave & 3;
  const rsrc_t wsr = mk_rsrc(a.ws, WS_END);
  const int rh = j & 1;
  STAMP(7);
  wait_vm<0>();
  if (cnt_add(C_F2, lane) == 3) put_flag(wsr, WS_F2 + (c * 16 + j) * 128, ta, tb, lane);
  STAMP(8);
  f32x4 y[4][5];
  {
    const u32 co = WS_CT + ((j >> 1) * 200 + wm * 50 + 25) * 1024 + lane * 16;
    polln(wsr, WS_F2 + (c * 16 + 4 * wm) * 128, 3, ta, tb, lane);
    STAMP(9);
    STAMPJ(1);
    constexpr int AW = 3;
    u32x4 hf[AW][4];
    const u32 hb = WS_HS + ((c * 8 + 4 * rh) * 40 + wm * 10) * 1024 + lane * 16;
#pragma unroll
    for (int k = 0; k < AW; k++)
#pragma unroll
      for (int m = 0; m < 4; m++) hf[k][m] = gld(wsr, hb, (m * 40 + k) * 1024);
    SCHED0();
#pragma unroll
    for (int m = 0; m < 4; m++)
#pragma unroll
      for (int n = 0; n < 5; n++) y[m][n] = f32x4{0.f, 0.f, 0.f, 0.f};
#pragma unroll
    for (int k = 0; k < 10; k++) {
#pragma unroll
      for (int n = 0; n < 5; n++)
#pragma unroll
        for (int m = 0; m < 4; m++) y[m][n] = mfma(asfrag(cf[k % 5][n]), asfrag(hf[k % AW][m]), y[m][n]);
      SCHED0();
      STAMPJ(2 + k);
      if (k + AW < 10) {
#pragma unroll
        for (int m = 0; m < 4; m++) hf[k % AW][m] = gld(wsr, hb, (m * 40 + k + AW) * 1024);
      }
      if (k < 5) {
#pragma unroll
        for (int n = 0; n < 5; n++) cf[k][n] = gld(wsr, co, (k * 5 + n) * 1024);
      }
      SCHED0();
    }
  }
  STAMP(10);
  cnt_wait(C_G2, 4);
#pragma unroll
  for (int m = 0; m < 4; m++)
#pragma unroll
    for (int n = 0; n < 5; n++) *(LAS f32x4*)L(wm * 20480 + (m * 5 + n) * 1024 + lane * 16) = y[m][n];
  cnt_add(C_Y, lane);
  STAMP(11);
}

__global__ __launch_bounds__(512, 2) void s5_fused(Args a) {
  extern __shared__ __attribute__((aligned(16))) char smem[];
  const int b = blockIdx.x, xcd = b & 7, r = b >> 3;
  const int c = xcd * 2 + (r >> 4), j = r & 15;
  const int wave = __builtin_amdgcn_readfirstlane(threadIdx.x >> 6), lane = threadIdx.x & 63;
  if ((u32)(size_t)(lptr)smem != 0) return;
  STAMP(0);
  u32 ta, tb;
  u32x4 cf[5][5];
  if (wave >= 4) {
    if (j & 1) phase1<true, 4>(a, c, j, wave, lane, ta, tb, cf);
    else phase1<true, 3>(a, c, j, wave, lane, ta, tb, cf);
    phase2_m(a, c, j, wave, lane, ta, tb);
  } else {
    if (j & 1) phase1<false, 2>(a, c, j, wave, lane, ta, tb, cf);
    else phase1<false, 3>(a, c, j, wave, lane, ta, tb, cf);
    phase2_s(a, c, j, wave, lane, ta, tb, cf);
  }
}
}

extern "C" void kernel_launch(void* const* d_in, const int* in_sizes, int n_in, void* d_out, int out_size, void* d_ws,
                              size_t ws_size, hipStream_t stream) {
  using namespace s5k;
  Args a;
  a.x = (const u16*)d_in[0];
  a.lre = (const u16*)d_in[1];
  a.lim = (const u16*)d_in[2];
  a.bre = (const u16*)d_in[3];
  a.bim = (const u16*)d_in[4];
  a.cre = (const u16*)d_in[5];
  a.cim = (const u16*)d_in[6];
  a.ldt = (const u16*)d_in[7];
  a.dsk = (const u16*)d_in[8];
  a.w1 = (const u16*)d_in[10];
  a.w2 = (const u16*)d_in[11];
  a.out = (float*)d_out;
  a.ws = (char*)d_ws;
  (void)hipFuncSetAttribute((const void*)s5_fused, hipFuncAttributeMaxDynamicSharedMemorySize, LDS_BYTES);
  s5_fused<<<256, 512, LDS_BYTES, stream>>>(a);
}
```

```cpp
#define HPROBE 2
#define HWAVE 4
#include <hip/hip_runtime.h>

#define STAMPJ(id)
namespace s5k {
typedef unsigned short u16;
typedef unsigned int u32;
typedef unsigned long long u64;
typedef short bf16x8 __attribute__((ext_vector_type(8)));
typedef short s16x2 __attribute__((ext_vector_type(2)));
typedef float f32x4 __attribute__((ext_vector_type(4)));
typedef float f32x2 __attribute__((ext_vector_type(2)));
typedef u32 u32x4 __attribute__((ext_vector_type(4)));
typedef u32 u32x2 __attribute__((ext_vector_type(2)));
typedef __bf16 bf16x2 __attribute__((ext_vector_type(2)));
#define LAS __attribute__((address_space(3)))
#define GAS __attribute__((address_space(1)))
typedef LAS char* lptr;
typedef __amdgpu_buffer_rsrc_t rsrc_t;

constexpr u32 WS_STATS = 4096;
constexpr u32 WS_AGG = 65536;
constexpr u32 WS_F1 = 262144, WS_F2 = WS_F1 + 32768, WS_F3 = WS_F2 + 32768;
constexpr u32 WS_PROF = 512 << 10;
constexpr u32 WS_H1 = 1u << 20;
constexpr u32 WS_HS = 9u << 20;
constexpr u32 WS_CT = 16u << 20;
constexpr u32 WS_WT = 18u << 20;
constexpr u32 WS_END = 21u << 20;
constexpr int CHUNK = 40960;
constexpr int LD_STG = 143360, LD_ST2 = LD_STG + 2048, LD_CS = LD_ST2 + 1024, LD_AGW = LD_CS + 1024, LD_CIN = LD_AGW + 1536,
              LD_CNT = LD_CIN + 1536, LD_TRB = LD_CNT + 256, LD_TAB = LD_TRB + 4096, LD_END = LD_TAB + 48 * 160;
constexpr int LDS_BYTES = 163840;
static_assert(LD_END <= LDS_BYTES, "lds");
enum { T_A = 0, T_C = 1, T_8 = 2, T_32 = 6, T_128 = 10, T_512 = 14 };
enum { C_F1 = 0, C_B2 = 1, C_S1 = 2, C_S2 = 3, C_F2 = 4, C_G = 5, C_G2 = 6, C_Y = 7 };
constexpr int SC1 = 16;
#define GMAP 2
constexpr u32 SPIN_CAP = 1u << 16;

struct Args {
  const u16 *x, *lre, *lim, *bre, *bim, *cre, *cim, *ldt, *dsk, *w1, *w2;
  float* out;
  char* ws;
};

__device__ __forceinline__ float u2f(u32 v) { return __builtin_bit_cast(float, v); }
__device__ __forceinline__ u32 f2u(float v) { return __builtin_bit_cast(u32, v); }
__device__ __forceinline__ float bf2f(u32 v) { return __builtin_bit_cast(float, v << 16); }
__device__ __forceinline__ float bfhi(u32 v) { return __builtin_bit_cast(float, v & 0xffff0000u); }
__device__ __forceinline__ u32 pk2(float a, float b) { return __builtin_bit_cast(u32, __builtin_convertvector(f32x2{a, b}, bf16x2)); }
__device__ __forceinline__ float rbf(float a) { return bf2f(pk2(a, a)); }
__device__ __forceinline__ f32x4 unpk4(u32x2 v) { return f32x4{bf2f(v.x), bfhi(v.x), bf2f(v.y), bfhi(v.y)}; }
__device__ __forceinline__ f32x4 rbf4(f32x4 v) { return unpk4(u32x2{pk2(v.x, v.y), pk2(v.z, v.w)}); }
__device__ __forceinline__ u32 relu2(u32 v) {
  return __builtin_bit_cast(u32, __builtin_elementwise_max(__builtin_bit_cast(s16x2, v), s16x2{0, 0}));
}
__device__ __forceinline__ f32x2 cmul(f32x2 a, f32x2 b) {
  float x, y;
  asm("v_mul_f32 %0, %2, %4\n\tv_mul_f32 %1, %2, %5\n\tv_fma_f32 %0, -%3, %5, %0\n\tv_fma_f32 %1, %3, %4, %1"
      : "=&v"(x), "=&v"(y) : "v"(a.x), "v"(a.y), "v"(b.x), "v"(b.y));
  return f32x2{x, y};
}
__device__ __forceinline__ f32x2 cfma(f32x2 a, f32x2 b, f32x2 c) {
  float x, y;
  asm("v_fma_f32 %0, %2, %4, %6\n\tv_fma_f32 %1, %2, %5, %7\n\tv_fma_f32 %0, -%3, %5, %0\n\tv_fma_f32 %1, %3, %4, %1"
      : "=&v"(x), "=&v"(y) : "v"(a.x), "v"(a.y), "v"(b.x), "v"(b.y), "v"(c.x), "v"(c.y));
  return f32x2{x, y};
}
__device__ __forceinline__ rsrc_t mk_rsrc(const void* p, u32 bytes) { return __builtin_amdgcn_make_buffer_rsrc((void*)p, 0, bytes, 0x00020000); }
__device__ __forceinline__ f32x4 mfma(bf16x8 a, bf16x8 b, f32x4 c) { return __builtin_amdgcn_mfma_f32_16x16x32_bf16(a, b, c, 0, 0, 0); }
__device__ __forceinline__ bf16x8 asfrag(u32x4 v) { return __builtin_bit_cast(bf16x8, v); }
__device__ __forceinline__ void dma16(rsrc_t rs, u32 dst, u32 voff, u32 soff) {
  asm volatile("s_mov_b32 m0, %0\n\ts_nop 0\n\tbuffer_load_dwordx4 %1, %2, %3 offen lds" ::"s"(dst), "v"(voff), "s"(rs), "s"(soff) : "memory");
}
__device__ __forceinline__ void dma16_sc1(rsrc_t rs, u32 dst, u32 voff, u32 soff) {
  asm volatile("s_mov_b32 m0, %0\n\ts_nop 0\n\tbuffer_load_dwordx4 %1, %2, %3 offen sc1 lds" ::"s"(dst), "v"(voff), "s"(rs), "s"(soff) : "memory");
}
__device__ __forceinline__ void dma4_sc1(rsrc_t rs, u32 dst, u32 voff, u32 soff) {
  asm volatile("s_mov_b32 m0, %0\n\ts_nop 0\n\tbuffer_load_dword %1, %2, %3 offen sc1 lds" ::"s"(dst), "v"(voff), "s"(rs), "s"(soff) : "memory");
}
__device__ __forceinline__ lptr L(u32 addr) { return (lptr)(size_t)addr; }
__device__ __forceinline__ bf16x8 ld128(u32 p) { return *(LAS bf16x8*)L(p); }
__device__ __forceinline__ f32x2 ldc(u32 p) { return *(LAS f32x2*)L(p); }
__device__ __forceinline__ f32x4 ld4(u32 p) { return *(LAS f32x4*)L(p); }
__device__ __forceinline__ bf16x8 ldtr(u32 p0, u32 p1) {
  typedef short v4i16 __attribute__((ext_vector_type(4)));
  v4i16 a = __builtin_amdgcn_ds_read_tr16_b64_v4i16((LAS v4i16*)L(p0));
  v4i16 b = __builtin_amdgcn_ds_read_tr16_b64_v4i16((LAS v4i16*)L(p1));
  return __builtin_shufflevector(a, b, 0, 1, 2, 3, 4, 5, 6, 7);
}
__device__ __forceinline__ u32x4 gld(rsrc_t rs, u32 voff, u32 soff) { return __builtin_amdgcn_raw_buffer_load_b128(rs, voff, soff, SC1); }
__device__ __forceinline__ void gst(u32x4 v, rsrc_t rs, u32 voff) { __builtin_amdgcn_raw_buffer_store_b128(v, rs, voff, 0, SC1); }
template <int N> __device__ __forceinline__ void wait_vm() { asm volatile("s_waitcnt vmcnt(%0)" ::"i"(N) : "memory"); }
__device__ __forceinline__ void wait_lgkm0() { __builtin_amdgcn_s_waitcnt(0xc07f); asm volatile("" ::: "memory"); }
__device__ __forceinline__ void wg_barrier() {
  asm volatile("" ::: "memory");
  __builtin_amdgcn_s_barrier();
  asm volatile("" ::: "memory");
}
#define PIN(x) asm volatile("" : "+v"(x))
#define SCHED0() __builtin_amdgcn_sched_barrier(0)
__device__ __forceinline__ u32 cnt_add(int idx, int lane) {
  u32 old;
  const u32 addr = LD_CNT + idx * 4 + (lane == 0 ? 0 : 64), one = 1;
  asm volatile("s_waitcnt lgkmcnt(0)\n\ts_mov_b64 exec, 1\n\tds_add_rtn_u32 %0, %1, %2\n\ts_mov_b64 exec, -1\n\ts_waitcnt lgkmcnt(0)"
               : "=&v"(old) : "v"(addr), "v"(one) : "memory");
  return __builtin_amdgcn_readfirstlane(old);
}
__device__ __forceinline__ void cnt_wait(int idx, u32 want) {
  const u32 addr = LD_CNT + idx * 4;
#pragma unroll 1
  for (u32 s = 0; s < (SPIN_CAP << 4); s++) {
    u32 v;
    asm volatile("ds_read_b32 %0, %1\n\ts_waitcnt lgkmcnt(0)" : "=&v"(v) : "v"(addr) : "memory");
    if (__builtin_amdgcn_readfirstlane(v) >= want) break;
    __builtin_amdgcn_s_sleep(1);
  }
}
__device__ __forceinline__ void polln(rsrc_t wsr, u32 off, int mask, u32 ta, u32 tb, int lane) {
  const u32 vo = off + (lane & mask) * 128;
#pragma unroll 1
  for (u32 s = 0; s < SPIN_CAP; s++) {
    u32x2 v = __builtin_amdgcn_raw_buffer_load_b64(wsr, vo, 0, SC1);
    if (__all((v.x == ta) & (v.y == tb))) break;
    __builtin_amdgcn_s_sleep(2);
  }
  __builtin_amdgcn_fence(__ATOMIC_ACQUIRE, "wavefront");
  asm volatile("" ::: "memory");
}
__device__ __forceinline__ void put_flag(rsrc_t wsr, u32 off, u32 ta, u32 tb, int lane) {
  __builtin_amdgcn_raw_buffer_store_b64(u32x2{ta, tb}, wsr, lane == 0 ? off : WS_END, 0, SC1);
}
template <int CTRL> __device__ __forceinline__ float dpp(float x) {
  return __builtin_bit_cast(float, __builtin_amdgcn_mov_dpp(__builtin_bit_cast(int, x), CTRL, 0xf, 0xf, true));
}
__device__ __forceinline__ void rows4(float x, float (&r)[4]) {
  auto s = __builtin_amdgcn_permlane16_swap(f2u(x), f2u(x), false, false);
  const u32 e = s[0], o = s[1];
  auto te = __builtin_amdgcn_permlane32_swap(e, e, false, false);
  auto to = __builtin_amdgcn_permlane32_swap(o, o, false, false);
  const u32 r0 = te[0], r2 = te[1], r1 = to[0], r3 = to[1];
  r[0] = u2f(r0); r[1] = u2f(r1); r[2] = u2f(r2); r[3] = u2f(r3);
}
__device__ __forceinline__ float rows_sum(float x) {
  auto s = __builtin_amdgcn_permlane16_swap(f2u(x), f2u(x), false, false);
  const u32 a = s[0], b = s[1];
  x = u2f(a) + u2f(b);
  auto t = __builtin_amdgcn_permlane32_swap(f2u(x), f2u(x), false, false);
  const u32 c = t[0], d = t[1];
  return u2f(c) + u2f(d);
}
__device__ __forceinline__ float wave_sum(float v) {
  v += dpp<0xB1>(v); v += dpp<0x4E>(v); v += dpp<0x124>(v); v += dpp<0x128>(v);
  return rows_sum(v);
}
__device__ __forceinline__ float fexp(float x) {
  const float hi = x * 1.4426950408889634f;
  const float lo = __builtin_fmaf(x, 1.4426950408889634f, -hi) + x * 1.925963033500011e-8f;
  return __builtin_amdgcn_exp2f(hi) * (1.0f + lo * 0.6931471805599453f);
}
__device__ __forceinline__ void fsincos(float x, float& sn, float& cs) {
  if (__builtin_expect(__any(!(__builtin_fabsf(x) < 2000.f)), 0)) { sincosf(x, &sn, &cs); return; }
  const float n = __builtin_rintf(x * 0.6366197723675814f);
  float r = __builtin_fmaf(n, -1.5703125f, x);
  r = __builtin_fmaf(n, -4.837512969970703125e-4f, r);
  r = __builtin_fmaf(n, -7.54978995489188e-8f, r);
  const float r2 = r * r;
  const float s = __builtin_fmaf(r * r2, __builtin_fmaf(r2, __builtin_fmaf(r2, -1.9515295891e-4f, 8.3321608736e-3f), -1.6666654611e-1f), r);
  const float c = __builtin_fmaf(r2 * r2, __builtin_fmaf(r2, __builtin_fmaf(r2, 2.443315711809948e-5f, -1.388731625493765e-3f), 4.166664568298827e-2f),
                                 __builtin_fmaf(r2, -0.5f, 1.0f));
  const int k = (int)n;
  const float s1 = (k & 1) ? c : s, c1 = (k & 1) ? s : c;
  sn = (k & 2) ? -s1 : s1;
  cs = ((k + 1) & 2) ? -c1 : c1;
}
#define STAMPI(id)
#define STAMP(id)                                                                                       \
  do {                                                                                                  \
    if ((id) == 0 || (id) == HPROBE) {                                                                  \
      const u64 t_ = __builtin_amdgcn_s_memrealtime();                                                  \
      *(LAS u32*)L(163000 + wave * 8 + ((id) ? 4 : 0)) = (u32)t_;                                       \
    }                                                                                                   \
    if ((id) == (HWAVE >= 4 ? 13 : 11) && wave == HWAVE && blockIdx.x == 100) {                          \
      wait_lgkm0();                                                                                     \
      const u32 d_ = *(LAS u32*)L(163000 + wave * 8 + 4) - *(LAS u32*)L(163000 + wave * 8);             \
      const u64 e_ = __builtin_amdgcn_s_memrealtime() + (u64)__builtin_amdgcn_readfirstlane(d_) * 20;   \
      while (__builtin_amdgcn_s_memrealtime() < e_) __builtin_amdgcn_s_sleep(8);                        \
    }                                                                                                   \
  } while (0)

template <bool IS_M, int NP>
__device__ __forceinline__ void phase1(const Args& a, int c, int j, int wave, int lane, u32& ta, u32& tb, u32x4 (&cf)[5][5]) {
  constexpr int NU = 2 * NP;
  constexpr bool TYPE_B = IS_M ? (NP == 4) : (NP == 2);
  constexpr int nS = TYPE_B ? 2 : 3, nM = 6 - nS, MP = nM * 64;
  const int wm = wave & 3;
  const int i2 = j >> 1;
  const int s0 = 5 * i2 + (TYPE_B ? 3 : 0);
  const int m0 = 7 * i2 + ((i2 & 1) ? (TYPE_B ? 3 : 0) : (TYPE_B ? 0 : 4));
  const int li = lane & 15, q = lane >> 4, r8 = lane >> 3, sl = lane & 7;
  const rsrc_t wsr = mk_rsrc(a.ws, WS_END);

  u32x2 ep;
  u32x4 xr0, xr1;
  u32 plr, pli, pdt;
  {
    const GAS char* xrow = (const GAS char*)a.x + (size_t)(c * 128 + j * 8 + wave) * 1280;
    asm volatile("global_load_dwordx2 %0, %1, %2 sc1" : "=v"(ep) : "v"(0), "s"(a.ws) : "memory");
    asm volatile("global_load_dwordx4 %0, %1, %2" : "=v"(xr0) : "v"(lane * 16), "s"(xrow) : "memory");
    asm volatile("global_load_dwordx4 %0, %1, %2 offset:1024" : "=v"(xr1) : "v"(li * 16), "s"(xrow) : "memory");
    if (!IS_M) {
      const int ch = (s0 * 16 + (q < NP ? lane : li)) * 2;
      asm volatile("global_load_ushort %0, %1, %2" : "=v"(plr) : "v"(ch), "s"(a.lre) : "memory");
      asm volatile("global_load_ushort %0, %1, %2" : "=v"(pli) : "v"(ch), "s"(a.lim) : "memory");
      asm volatile("global_load_ushort %0, %1, %2" : "=v"(pdt) : "v"(ch), "s"(a.ldt) : "memory");
    }
  }
  rsrc_t drs[5];
  u32 dvo[5], dso[5], dstep[5], ddst[5];
  {
    const rsrc_t brs = mk_rsrc(wm < 2 ? a.bre : a.bim, 640 * 1280);
    const u32 bvo = ((wm & 1) * 8 + r8) * 1280 + ((sl ^ ((wm & 1) * 4 + (r8 >> 1))) << 4);
#pragma unroll
    for (int b = 0; b < 5; b++) {
      if (!IS_M && b < 4) {
        drs[b] = mk_rsrc(a.x, 2048 * 1280);
        dvo[b] = (8 * (r8 >> 2) + (r8 & 3)) * 1280 + ((sl ^ ((b & 1) * 4 + (r8 >> 1))) << 4);
        dso[b] = (c * 128 + wm * 32 + (b & 1) * 16 + (b >> 1) * 4) * 1280;
        dstep[b] = 128;
        ddst[b] = (wm * 4 + b) * 1024;
      } else if (!IS_M || b < nS - 1) {
        const int p = IS_M ? b + 1 : 0;
        drs[b] = brs;
        dvo[b] = bvo;
        dso[b] = (s0 + p) * 16 * 1280;
        dstep[b] = 128;
        ddst[b] = 16384 + p * 4096 + wm * 1024;
      } else {
        const int t = b - (nS - 1);
        const int idx = t * 64 + lane;
        const int kr = idx / (4 * nM), cpos = idx - kr * (4 * nM);
        const int sw = TYPE_B ? ((kr & 3) | (((kr >> 3) & 1) << 2)) : ((kr >> 3) & 1);
        int col16 = m0 * 4 + (((cpos >> 1) ^ sw) << 1) + (cpos & 1);
        if (TYPE_B && col16 >= 216) col16 -= 8;
        drs[b] = mk_rsrc(a.w1, 640 * 3456);
        dvo[b] = kr * 3456 + col16 * 16;
        dso[b] = wm * 16 * 3456;
        dstep[b] = 64 * 3456;
        ddst[b] = 16384 + nS * 4096 + (wm * nM + t) * 1024;
      }
    }
  }
  int kc = c + j;
  kc -= kc >= 20 ? 20 : kc >= 10 ? 10 : 0;
  kc = kc == 10 ? 0 : kc;
  auto dma1 = [&](int bufoff, int b) { dma16_sc1(drs[b], bufoff + ddst[b], dvo[b], dso[b] + kc * dstep[b]); };
  auto issue = [&](int bufoff) {
#pragma unroll
    for (int b = 0; b < 5; b++) dma1(bufoff, b);
    kc = kc == 9 ? 0 : kc + 1;
  };
  issue(0);
  *(LAS u32*)L(LD_CNT + li * 4) = 0;
  const int rid = c * 2 + (j & 1);
  STAMP(1);

  int pr[NP];
#pragma unroll
  for (int p = 0; p < NP; p++) pr[p] = (p + wm) % NP;
  u32 xb[2][2], wb[2][NU];
  {
    const u32 xo = li * 128 + ((q ^ (li >> 1)) << 4);
    const int qq = li >> 2;
    const u32 rowb = 16384 + nS * 4096 + (8 * q + qq) * MP + (lane & 3) * 8;
    const int f = TYPE_B ? (qq | ((q & 1) << 2)) : (q & 1);
#pragma unroll
    for (int h = 0; h < 2; h++) {
#pragma unroll
      for (int kk = 0; kk < 2; kk++) {
        xb[h][kk] = h * 2 * CHUNK + wm * 4096 + (xo ^ (kk << 6));
        PIN(xb[h][kk]);
      }
#pragma unroll
      for (int u = 0; u < NU; u++) {
        wb[h][u] = h * 2 * CHUNK + (IS_M ? rowb + (((2 * pr[u >> 1] + (u & 1)) ^ f) << 5) : 16384 + pr[u >> 1] * 4096 + (xo ^ ((u & 1) << 6)));
        PIN(wb[h][u]);
      }
    }
  }
  bf16x8 xf[2][2], wf[2][NU];
  auto read1 = [&](int set, int bufoff, int kk, int f) {
    const int h = bufoff == 2 * CHUNK, bo = h ? 0 : bufoff;
    if (f < 2) {
      xf[set][f] = ld128(xb[h][kk] + (bo + f * 2048));
    } else if (f < NU + 2) {
      const int u = f - 2;
      if (!IS_M) wf[set][u] = ld128(wb[h][(u & ~1) + kk] + (bo + (u & 1) * 2048));
      else wf[set][u] = ldtr(wb[h][u] + (bo + kk * 32 * MP), wb[h][u] + (bo + kk * 32 * MP + 4 * MP));
    }
  };
  f32x4 acc[2][NU], csum[2];
#pragma unroll
  for (int m = 0; m < 2; m++) {
#pragma unroll
    for (int u = 0; u < NU; u++) { acc[m][u] = f32x4{0.f, 0.f, 0.f, 0.f}; PIN(acc[m][u]); }
    csum[m] = f32x4{0.f, 0.f, 0.f, 0.f};
    PIN(csum[m]);
  }
  u32x4 ones4 = {0x3f803f80u, 0x3f803f80u, 0x3f803f80u, 0x3f803f80u};
  PIN(ones4);
  constexpr int G = NU + 1;
  constexpr int NDB = IS_M ? 2 : 3;
  auto dpos = [](int d) { return (2 * G * d + (IS_M ? G : 0)) / 5; };
  static_assert((2 * G * (NDB - 1) + (IS_M ? G : 0)) / 5 < G && (2 * G * NDB + (IS_M ? G : 0)) / 5 >= G, "split");
  auto step = [&](int set, int rbuf, int rkk, int dbuf) {
#pragma unroll
    for (int g = 0; g < G; g++) {
      if (g > 0) {
        const int u = g - 1;
#pragma unroll
        for (int m = 0; m < 2; m++)
          acc[m][u] = IS_M ? mfma(wf[set][u], xf[set][m], acc[m][u]) : mfma(xf[set][m], wf[set][u], acc[m][u]);
      } else {
        csum[0] = mfma(asfrag(ones4), wf[set][0], csum[0]);
        csum[1] = mfma(asfrag(ones4), wf[set][1], csum[1]);
      }
      if (rbuf >= 0) {
        if (g == 0) { read1(set ^ 1, rbuf, rkk, 2); read1(set ^ 1, rbuf, rkk, 3); }
        else if (g == 1) { read1(set ^ 1, rbuf, rkk, 0); read1(set ^ 1, rbuf, rkk, 1); }
        else read1(set ^ 1, rbuf, rkk, g + 2);
      } else if (g == 3) {
        LAS float* csp = (LAS float*)L(LD_CS + (IS_M ? 512 : 0) + (wm < NP ? wm : 3) * 128 + (lane & 31) * 4);
        float e0 = csum[0].x, e1 = csum[1].x;
        PIN(e0); PIN(e1);
        if (wm < NP) *csp = (q & 1) ? e1 : e0;
      }
#pragma unroll
      for (int d = 0; d < 5; d++)
        if ((set == 1 ? g : g + G) == dpos(d)) dma1(dbuf, d);
      SCHED0();
    }
  };

  asm volatile("s_waitcnt vmcnt(5)" : "+v"(ep), "+v"(xr0), "+v"(xr1)::"memory");
  if (!IS_M) asm volatile("" : "+v"(plr), "+v"(pli), "+v"(pdt));
  {
    u64 z = ((u64)__builtin_amdgcn_readfirstlane(ep.y) << 32 | __builtin_amdgcn_readfirstlane(ep.x)) + 0x9E3779B97F4A7C15ull;
    z = (z ^ (z >> 30)) * 0xBF58476D1CE4E5B9ull;
    z = (z ^ (z >> 27)) * 0x94D049BB133111EBull;
    z ^= z >> 31;
    ta = (u32)z;
    tb = (u32)(z >> 32);
    float v[16];
#pragma unroll
    for (int e = 0; e < 4; e++) {
      v[2 * e] = bf2f(xr0[e]); v[2 * e + 1] = bfhi(xr0[e]);
      v[8 + 2 * e] = bf2f(xr1[e]); v[8 + 2 * e + 1] = bfhi(xr1[e]);
    }
    float s0_ = 0.f, s1_ = 0.f;
#pragma unroll
    for (int e = 0; e < 8; e++) { s0_ += v[e]; s1_ += v[8 + e]; }
    const float mean = wave_sum(lane < 16 ? s0_ + s1_ : s0_) * (1.0f / 640.0f);
    s0_ = 0.f; s1_ = 0.f;
#pragma unroll
    for (int e = 0; e < 8; e++) {
      const float d0 = v[e] - mean, d1 = v[8 + e] - mean;
      s0_ += d0 * d0; s1_ += d1 * d1;
    }
    const float var = wave_sum(lane < 16 ? s0_ + s1_ : s0_) * (1.0f / 640.0f);
    const float mb = rbf(mean), den = rbf(__builtin_amdgcn_sqrtf(rbf(rbf(var) + 1e-5f)));
    gst(u32x4{f2u(mb), ta, f2u(__builtin_amdgcn_rcpf(den)), tb}, wsr, lane == 0 ? WS_STATS + (c * 128 + j * 8 + wave) * 16 : WS_END);
  }
  if (!IS_M) {
    if (wm == 0) {
      const float lr = bf2f(plr), lim = bf2f(pli);
      const float dt = rbf(fexp(bf2f(pdt)));
      const float er = fexp(dt * lr);
      float sn, cn;
      fsincos(dt * lim, sn, cn);
      const f32x2 a1 = {er * cn, er * sn};
      const float nr = a1.x - 1.0f, dn = 1.0f / (lr * lr + lim * lim);
      const u32 tp = LD_TAB + (lane < 48 ? lane : 47) * 160;
      if (q < NP) {
        *(LAS f32x4*)L(tp) = f32x4{a1.x, a1.y, (nr * lr + a1.y * lim) * dn, (a1.y * lr - nr * lim) * dn};
        const f32x2 a2 = cmul(a1, a1), a4 = cmul(a2, a2);
        f32x2 b = cmul(a4, a4);
#pragma unroll
        for (int i = 0; i < 4; i++) {
          const f32x2 b2 = cmul(b, b), b3 = cmul(b2, b);
          *(LAS f32x4*)L(tp + 16 + i * 32) = f32x4{1.f, 0.f, b.x, b.y};
          *(LAS f32x4*)L(tp + 32 + i * 32) = f32x4{b2.x, b2.y, b3.x, b3.y};
          b = cmul(b2, b2);
        }
      }
    }
  }
  issue(CHUNK);
#pragma unroll
  for (int d = 0; d < NDB; d++) dma1(2 * CHUNK, d);
  u32x4 rt[2];
  const int rks = min(rid * 2 + (wm & 1), 53);
  if (!IS_M) {
#pragma unroll
    for (int i = 0; i < 2; i++) {
      const int f = min(rid * 8 + wm * 2 + i, 199);
      const int wk = f / 5, n = f - wk * 5, w = wk / 10, k = wk - w * 10;
      const GAS char* src = (const GAS char*)(k < 5 ? a.cre : a.cim) + (i2 * 80 + n * 16) * 1280 + (160 * w + 32 * (k < 5 ? k : k - 5)) * 2;
      asm volatile("global_load_dwordx4 %0, %1, %2" : "=v"(rt[i]) : "v"(li * 1280 + q * 16), "s"(src) : "memory");
    }
    wait_vm<6 + NDB + 2>();
  } else {
    wait_vm<6 + NDB>();
  }
  wait_lgkm0();
  wg_barrier();
  STAMP(2);
  if (IS_M) __builtin_amdgcn_s_setprio(1);
#pragma unroll
  for (int f = 0; f < NU + 2; f++) read1(0, 0, 0, f);
  int it = 0;
  auto body = [&](int prv, int cur, int nxt) {
    step(0, cur, 1, prv);
    kc = kc == 9 ? 0 : kc + 1;
    if (nxt < 0) {
      const int r = wave * 16 + li;
      u32x4 g = *(LAS u32x4*)L(LD_STG + r * 16);
      if (!__all((g.y == ta) & (g.w == tb))) {
#pragma unroll 1
        for (u32 s = 0; s < SPIN_CAP; s++) {
          g = gld(wsr, WS_STATS + (c * 128 + r) * 16, 0);
          if (__all((g.y == ta) & (g.w == tb))) break;
        }
      }
      const int t5 = r & 31;
      *(LAS f32x2*)L(LD_ST2 + ((r & ~31) + (((t5 >> 2) & 1) << 4) + ((t5 >> 3) << 2) + (t5 & 3)) * 8) = f32x2{u2f(g.x), u2f(g.z)};
    } else if (!IS_M && it == 0) wait_vm<7>();
    else if (IS_M && it == 1 && wm < 2) wait_vm<10>();
    else wait_vm<5>();
    wait_lgkm0();
    wg_barrier();
    STAMPI(3 + it);
    if (IS_M && it == 0 && wm < 2) {
      const rsrc_t w2r = mk_rsrc(a.w2, 1728 * 1280);
#pragma unroll
      for (int b = 0; b < 5; b++) {
        const int idx = b * 64 + lane, kr = idx / 10, cpos = idx - kr * 10;
        dma16(w2r, 3 * CHUNK + wm * 5120 + b * 1024, kr * 1280 + i2 * 160 + cpos * 16, rks * (32 * 1280));
      }
    }
    if (it == 2) {
      if (!IS_M) {
        asm volatile("" : "+v"(rt[0]), "+v"(rt[1]));
#pragma unroll
        for (int i = 0; i < 2; i++) gst(rt[i], wsr, WS_CT + (i2 * 200 + min(rid * 8 + wm * 2 + i, 199)) * 1024 + lane * 16);
      } else if (wm < 2) {
        const u32 p = 3 * CHUNK + wm * 5120 + (4 * q + (li >> 2)) * 160 + (lane & 3) * 8;
#pragma unroll
        for (int n = 0; n < 5; n++)
          gst(__builtin_bit_cast(u32x4, ldtr(p + n * 32, p + n * 32 + 2560)), wsr, WS_WT + ((i2 * 54 + rks) * 5 + n) * 1024 + lane * 16);
      }
    }
    if (IS_M && it == 3 && wm == 3) put_flag(wsr, WS_F3 + (i2 * 32 + rid) * 128, ta, tb, lane);
    if (it >= 7) {
      if (it == 7) {
        const u32 f = *(LAS u32*)L(LD_TRB + wave * 256 + lane * 4);
        if (!__all(f == ((lane & 1) ? tb : ta))) polln(wsr, WS_F3 + i2 * 32 * 128, 31, ta, tb, lane);
#pragma unroll
        for (int b = 0; b < 5; b++) {
          drs[b] = wsr; dvo[b] = lane * 16; dso[b] = WS_WT + i2 * 54 * 5120 + b * 1024; dstep[b] = 5120; ddst[b] = wave * 5120 + b * 1024;
        }
      }
      kc = wm * 14 + (cur / CHUNK) * 2 + (IS_M ? 1 : 0);
    }
    SCHED0();
    step(1, nxt, 0, cur);
    if (it == 5) {
      dma4_sc1(wsr, LD_TRB + wave * 256, (lane >> 1) * 128 + (lane & 1) * 4, WS_F3 + i2 * 32 * 128);
      dma4_sc1(wsr, LD_STG + wave * 256, lane * 4, WS_STATS + c * 2048 + wave * 256);
    }
    it++;
  };
#pragma unroll 1
  for (int r = 0; r < 3; r++) {
    body(2 * CHUNK, 0, CHUNK);
    body(0, CHUNK, 2 * CHUNK);
    body(CHUNK, 2 * CHUNK, 0);
  }
  body(2 * CHUNK, 0, -1);
#pragma unroll
  for (int d = NDB; d < 5; d++) dma1(0, d);
  if (IS_M) __builtin_amdgcn_s_setprio(0);
  else __builtin_amdgcn_s_setprio(2);
  STAMP(3);
  float mb[2];
  f32x4 st[4];
  f32x2 ca[NP], cc[NP], a8r[NP];
  u32 tp[NP];
  if (IS_M) {
#pragma unroll
    for (int m = 0; m < 2; m++) mb[m] = *(LAS float*)L(LD_ST2 + (wm * 32 + m * 16 + li) * 8);
  } else {
#pragma unroll
    for (int i = 0; i < 4; i++) st[i] = ld4(LD_ST2 + (wm * 32 + (i >> 1) * 16 + 4 * q + 2 * (i & 1)) * 8);
#pragma unroll
    for (int p = 0; p < NP; p++) {
      tp[p] = LD_TAB + (pr[p] * 16 + li) * 160;
      const f32x4 t0 = ld4(tp[p]);
      ca[p] = f32x2{t0.x, t0.y};
      cc[p] = f32x2{t0.z, t0.w};
      a8r[p] = ldc(tp[p] + (T_8 + 3 - q) * 8);
    }
  }
  wait_lgkm0();
  wg_barrier();
  STAMP(4);

  if (IS_M) {
#pragma unroll
    for (int p = 0; p < NP; p++) {
      const f32x4 c0 = ld4(LD_CS + 512 + pr[p] * 128 + q * 16), c1 = ld4(LD_CS + 512 + pr[p] * 128 + 64 + q * 16);
      const u32 off = (m0 + pr[p] < 54) ? WS_H1 + ((c * 8 + 2 * wm) * 54 + m0 + pr[p]) * 1024 : WS_END;
#pragma unroll
      for (int m = 0; m < 2; m++) {
        const f32x4 v0 = acc[m][2 * p] - mb[m] * c0, v1 = acc[m][2 * p + 1] - mb[m] * c1;
        gst(u32x4{relu2(pk2(v0.x, v0.y)), relu2(pk2(v0.z, v0.w)), relu2(pk2(v1.x, v1.y)), relu2(pk2(v1.z, v1.w))}, wsr,
            off + m * 54 * 1024 + lane * 16);
      }
    }
  } else {
    f32x2 u[NP][8];
    f32x2 seg[NP];
    float Sr[NP], Si[NP];
#pragma unroll
    for (int p = 0; p < NP; p++) {
      Sr[p] = *(LAS float*)L(LD_CS + pr[p] * 128 + li * 4);
      Si[p] = *(LAS float*)L(LD_CS + pr[p] * 128 + 64 + li * 4);
    }
#pragma unroll
    for (int p = 0; p < NP; p++) {
      f32x2 h;
#pragma unroll
      for (int t = 0; t < 8; t++) {
        const float mu = st[t >> 1][(t & 1) * 2], rs = st[t >> 1][(t & 1) * 2 + 1];
        u[p][t] = f32x2{(acc[t >> 2][2 * p][t & 3] - mu * Sr[p]) * rs, (acc[t >> 2][2 * p + 1][t & 3] - mu * Si[p]) * rs};
        h = t ? cfma(ca[p], h, u[p][t]) : u[p][0];
      }
      seg[p] = h;
      h = cmul(a8r[p], h);
      *(LAS f32x2*)L(LD_AGW + ((wm * 3 + pr[p]) * 16 + li) * 8) = f32x2{rows_sum(h.x), rows_sum(h.y)};
    }
    if (cnt_add(C_S1, lane) == 3) {
      const int ch = lane < 48 ? lane : 47;
      const f32x2 a32 = ldc(LD_TAB + ch * 160 + (T_32 + 1) * 8);
      f32x2 h = ldc(LD_AGW + ch * 8);
#pragma unroll
      for (int w = 1; w < 4; w++) h = cfma(a32, h, ldc(LD_AGW + (w * 48 + ch) * 8));
      gst(u32x4{f2u(h.x), ta, f2u(h.y), tb}, wsr, q < NP ? WS_AGG + (c * 640 + s0 * 16 + lane) * 16 : WS_END);
    }
    STAMP(5);
    {
      const u32 co = WS_CT + (i2 * 200 + wm * 50) * 1024 + lane * 16;
#pragma unroll
      for (int k = 0; k < 5; k++)
#pragma unroll
        for (int n = 0; n < 5; n++) cf[k][n] = gld(wsr, co, (k * 5 + n) * 1024);
      SCHED0();
      wait_vm<25>();
      cnt_add(C_B2, lane);
    }
    cnt_wait(C_S1, 4);
    f32x2 aq[NP], pre[NP];
    u32 hso[NP];
#pragma unroll
    for (int p = 0; p < NP; p++) {
      const f32x2 a32 = ldc(tp[p] + (T_32 + 1) * 8), a8q = ldc(tp[p] + (T_8 + q) * 8), aw = ldc(tp[p] + (T_32 + wm) * 8);
      f32x2 h = {0.f, 0.f};
#pragma unroll
      for (int w = 0; w < 3; w++) {
        const f32x2 g = ldc(LD_AGW + ((w * 3 + pr[p]) * 16 + li) * 8);
        if (w < wm) h = cfma(a32, h, g);
      }
#pragma unroll
      for (int t = 0; t < 8; t++) u[p][t] = cmul(cc[p], u[p][t]);
      {
        const f32x2 a8 = ldc(tp[p] + (T_8 + 1) * 8);
        float gr[4], gi[4];
        rows4(seg[p].x, gr); rows4(seg[p].y, gi);
#pragma unroll
        for (int w = 0; w < 3; w++) {
          const f32x2 t = cfma(a8, h, f32x2{gr[w], gi[w]});
          h.x = w < q ? t.x : h.x;
          h.y = w < q ? t.y : h.y;
          PIN(h);
        }
      }
      pre[p] = cmul(cc[p], h);
      aq[p] = cmul(cc[p], cmul(a8q, aw));
      const int cu = s0 + pr[p];
      hso[p] = WS_HS + ((c * 8 + 2 * wm) * 40 + (cu / 10) * 10 + ((cu % 10) >> 1) + (lane >> 5) * 5) * 1024 + (cu & 1) * 512 + (lane & 31) * 16;
      asm volatile("" : "+v"(pre[p]), "+v"(aq[p]), "+v"(hso[p]), "+v"(u[p][0]), "+v"(u[p][1]), "+v"(u[p][2]), "+v"(u[p][3]), "+v"(u[p][4]),
                   "+v"(u[p][5]), "+v"(u[p][6]), "+v"(u[p][7]));
    }
    u32 tw = LD_TRB + wm * 1024 + (li >> 3) * 256 + q * 64 + (li & 7) * 2, tr = LD_TRB + wm * 1024 + lane * 16;
    PIN(tw); PIN(tr);
    cnt_wait(C_S2, 4);
    STAMP(6);
#pragma unroll
    for (int p = 0; p < NP; p++) {
      const u32 ci = LD_CIN + (pr[p] * 16 + li) * 8;
      f32x2 h = (ldc(ci) + ldc(ci + 384)) + (ldc(ci + 768) + ldc(ci + 1152));
      h = cfma(aq[p], h, pre[p]);
#pragma unroll
      for (int m = 0; m < 2; m++) {
#pragma unroll
        for (int r = 0; r < 4; r++) {
          h = cfma(ca[p], h, u[p][4 * m + r]);
          const u32 pk = pk2(h.x, -h.y);
          *(LAS u16*)L(tw + r * 16) = (u16)pk;
          *(LAS u16*)L(tw + r * 16 + 512) = (u16)(pk >> 16);
        }
        asm volatile("" ::: "memory");
        gst(*(LAS u32x4*)L(tr), wsr, hso[p] + m * 40 * 1024);
        asm volatile("" ::: "memory");
      }
    }
    if (c == 15 && j == 0 && wm == 0) {
      const u64 e = ((u64)ep.y << 32 | ep.x) + 1;
      __builtin_amdgcn_raw_buffer_store_b64(u32x2{(u32)e, (u32)(e >> 32)}, wsr, lane == 0 ? 0 : WS_END, 0, SC1);
    }
  }
}

__device__ __forceinline__ void phase2_m(const Args& a, int c, int j, int wave, int lane, u32 ta, u32 tb) {
  const int wm = wave & 3, li = lane & 15, q = lane >> 4;
  const rsrc_t wsr = mk_rsrc(a.ws, WS_END);
  const int rh = j & 1, i2 = j >> 1, dcol0 = i2 * 80;
  const u32 wo = WS_WT + (i2 * 54 + wm * 14) * 5120;
#pragma unroll
  for (int b = 0; b < 5; b++) dma16_sc1(wsr, (24 + wm) * 5120 + b * 1024, lane * 16, wo + (30 + b) * 1024);
  const int cp = 4 * wm + q, e = c - 1 - cp, s0 = 5 * i2 + (rh ? 3 : 0), np = rh ? 2 : 3;
  f32x2 pw[3];
#pragma unroll
  for (int p = 0; p < 3; p++) {
    const u32 tp = LD_TAB + ((p < np ? p : 0) * 16 + li) * 160;
    pw[p] = cmul(ldc(tp + (T_128 + (e & 3)) * 8), ldc(tp + (T_512 + ((e >> 2) & 3)) * 8));
    if (e < 0) pw[p] = f32x2{0.f, 0.f};
  }
  wait_vm<5>();
  if (cnt_add(C_F1, lane) == 3) put_flag(wsr, WS_F1 + (c * 16 + j) * 128, ta, tb, lane);
  STAMP(5);
  {
    u32x4 g[3];
    const u32 go = WS_AGG + (cp * 640 + s0 * 16 + li) * 16;
#pragma unroll 1
    for (u32 s = 0; s < SPIN_CAP; s++) {
      bool ok = true;
#pragma unroll
      for (int p = 0; p < 3; p++) g[p] = gld(wsr, go, (p < np ? p : 0) * 256);
#pragma unroll
      for (int p = 0; p < 3; p++) ok = ok & (g[p].y == ta) & (g[p].w == tb);
      if (__all(ok | (e < 0))) break;
      __builtin_amdgcn_s_sleep(1);
    }
#pragma unroll
    for (int p = 0; p < 3; p++) {
      const f32x2 h = cmul(pw[p], e < 0 ? f32x2{0.f, 0.f} : f32x2{u2f(g[p].x), u2f(g[p].z)});
      *(LAS f32x2*)L(LD_CIN + ((wm * 3 + p) * 16 + li) * 8) = f32x2{rows_sum(h.x), rows_sum(h.y)};
    }
    cnt_add(C_S2, lane);
    cnt_add(C_B2, lane);
  }
  STAMP(6);
  u32x4 w[3][5];
  auto ldw = [&](int r, int k) {
#pragma unroll
    for (int n = 0; n < 5; n++) w[r][n] = gld(wsr, lane * 16, wo + (((k < 12 || wm < 3) ? k : 0) * 5 + n) * 1024);
  };
  polln(wsr, WS_F1 + (c * 16 + 4 * wm) * 128, 3, ta, tb, lane);
  STAMP(7);
  f32x4 g[4][5];
  {
    u32 wbase[3];
#pragma unroll
    for (int h = 0; h < 3; h++) {
      wbase[h] = wm * 5120 + h * 61440 + lane * 16;
      PIN(wbase[h]);
    }
    auto rd = [&](int r, int slot) {
#pragma unroll
      for (int n = 0; n < 5; n++) w[r][n] = __builtin_bit_cast(u32x4, ld128(wbase[slot / 3] + ((slot % 3) * 20480 + n * 1024)));
    };
    auto rs = [](int s) { return s < 7 ? (s & 1) : 2 - (s - 7) % 3; };
    u32x4 hf[4][4];
    const u32 hb = WS_H1 + ((c * 8 + 4 * rh) * 54 + wm * 14) * 1024 + lane * 16;
    auto lda = [&](int s) {
#pragma unroll
      for (int m = 0; m < 4; m++) hf[s & 3][m] = gld(wsr, hb, (m * 54 + ((s < 12 || wm < 3) ? s : 0)) * 1024);
    };
#pragma unroll
    for (int s = 0; s < 4; s++) lda(s);
    ldw(2, 7);
    SCHED0();
#pragma unroll
    for (int m = 0; m < 4; m++)
#pragma unroll
      for (int n = 0; n < 5; n++) g[m][n] = f32x4{0.f, 0.f, 0.f, 0.f};
    cnt_wait(C_B2, 8);
    STAMPJ(1);
    rd(0, 0);
#pragma unroll
    for (int s = 0; s < 14; s++) {
      if (s < 12 || wm < 3) {
        if (s + 1 < 7) rd(rs(s + 1), s + 1);
#pragma unroll
        for (int n = 0; n < 5; n++)
#pragma unroll
          for (int m = 0; m < 4; m++) g[m][n] = mfma(asfrag(w[rs(s)][n]), asfrag(hf[s & 3][m]), g[m][n]);
        SCHED0();
        if (s < 11) STAMPJ(2 + s);
        if (s + 4 < 14) lda(s + 4);
        if (s >= 5 && s + 3 < 14) ldw(rs(s), s + 3);
        SCHED0();
      }
    }
  }
  STAMP(8);
  const int tt = 4 * rh + wm;
  const int tok = c * 128 + (tt >> 1) * 32 + 8 * (li >> 2) + 4 * (tt & 1) + (li & 3);
  u32x2 xv[5], dv[5];
#pragma unroll
  for (int n = 0; n < 5; n++) {
    xv[n] = *(const GAS u32x2*)((const GAS u16*)a.x + tok * 640 + dcol0 + 16 * n + 4 * q);
    dv[n] = *(const GAS u32x2*)((const GAS u16*)a.dsk + dcol0 + 16 * n + 4 * q);
  }
#pragma unroll
  for (int m = 0; m < 4; m++)
#pragma unroll
    for (int n = 0; n < 5; n++) *(LAS f32x4*)L((m * 4 + wm) * 5120 + n * 1024 + lane * 16) = g[m][n];
  cnt_add(C_G, lane);
  f32x4 gate[5], skip[5], xs[5];
  const f32x2 st = ldc(LD_ST2 + (tt * 16 + li) * 8);
#pragma unroll
  for (int n = 0; n < 5; n++) {
    xs[n] = unpk4(xv[n]);
    skip[n] = rbf4(rbf4(rbf4(xs[n] - st.x) * st.y) * unpk4(dv[n]));
    PIN(skip[n]);
  }
  cnt_wait(C_G, 4);
  STAMP(9);
#pragma unroll
  for (int n = 0; n < 5; n++) {
    const u32 gp = wm * 20480 + n * 1024 + lane * 16;
    gate[n] = (ld4(gp) + ld4(gp + 5120)) + (ld4(gp + 10240) + ld4(gp + 15360));
    PIN(gate[n]);
  }
  cnt_add(C_G2, lane);
#pragma unroll
  for (int n = 0; n < 5; n++) {
    const f32x4 gb = rbf4(gate[n] * st.y);
    f32x4 sg;
#pragma unroll
    for (int e2 = 0; e2 < 4; e2++) sg[e2] = gb[e2] * __builtin_amdgcn_rcpf(1.0f + __builtin_amdgcn_exp2f(gb[e2] * -1.4426950408889634f));
    gate[n] = rbf4(sg);
    PIN(gate[n]);
  }
  STAMP(11);
  cnt_wait(C_Y, 4);
  STAMP(12);
#pragma unroll
  for (int n = 0; n < 5; n++) {
    const u32 yp = (wm * 5 + n) * 1024 + lane * 16;
    const f32x4 y = (ld4(yp) + ld4(yp + 20480)) + (ld4(yp + 40960) + ld4(yp + 61440));
    *(GAS f32x4*)((GAS float*)a.out + tok * 640 + dcol0 + 16 * n + 4 * q) = xs[n] + (2.0f * y + skip[n]) * gate[n];
  }
  STAMP(13);
}

__device__ __forceinline__ void phase2_s(const Args& a, int c, int j, int wave, int lane, u32 ta, u32 tb, u32x4 (&cf)[5][5]) {
  const int wm = wave & 3;
  const rsrc_t wsr = mk_rsrc(a.ws, WS_END);
  const int rh = j & 1;
  STAMP(7);
  wait_vm<0>();
  if (cnt_add(C_F2, lane) == 3) put_flag(wsr, WS_F2 + (c * 16 + j) * 128, ta, tb, lane);
  STAMP(8);
  f32x4 y[4][5];
  {
    const u32 co = WS_CT + ((j >> 1) * 200 + wm * 50 + 25) * 1024 + lane * 16;
    polln(wsr, WS_F2 + (c * 16 + 4 * wm) * 128, 3, ta, tb, lane);
    STAMP(9);
    STAMPJ(1);
    constexpr int AW = 3;
    u32x4 hf[AW][4];
    const u32 hb = WS_HS + ((c * 8 + 4 * rh) * 40 + wm * 10) * 1024 + lane * 16;
#pragma unroll
    for (int k = 0; k < AW; k++)
#pragma unroll
      for (int m = 0; m < 4; m++) hf[k][m] = gld(wsr, hb, (m * 40 + k) * 1024);
    SCHED0();
#pragma unroll
    for (int m = 0; m < 4; m++)
#pragma unroll
      for (int n = 0; n < 5; n++) y[m][n] = f32x4{0.f, 0.f, 0.f, 0.f};
#pragma unroll
    for (int k = 0; k < 10; k++) {
#pragma unroll
      for (int n = 0; n < 5; n++)
#pragma unroll
        for (int m = 0; m < 4; m++) y[m][n] = mfma(asfrag(cf[k % 5][n]), asfrag(hf[k % AW][m]), y[m][n]);
      SCHED0();
      STAMPJ(2 + k);
      if (k + AW < 10) {
#pragma unroll
        for (int m = 0; m < 4; m++) hf[k % AW][m] = gld(wsr, hb, (m * 40 + k + AW) * 1024);
      }
      if (k < 5) {
#pragma unroll
        for (int n = 0; n < 5; n++) cf[k][n] = gld(wsr, co, (k * 5 + n) * 1024);
      }
      SCHED0();
    }
  }
  STAMP(10);
  cnt_wait(C_G2, 4);
#pragma unroll
  for (int m = 0; m < 4; m++)
#pragma unroll
    for (int n = 0; n < 5; n++) *(LAS f32x4*)L(wm * 20480 + (m * 5 + n) * 1024 + lane * 16) = y[m][n];
  cnt_add(C_Y, lane);
  STAMP(11);
}

__global__ __launch_bounds__(512, 2) void s5_fused(Args a) {
  extern __shared__ __attribute__((aligned(16))) char smem[];
  const int b = blockIdx.x, xcd = b & 7, r = b >> 3;
  const int c = xcd * 2 + (r >> 4), j = r & 15;
  const int wave = __builtin_amdgcn_readfirstlane(threadIdx.x >> 6), lane = threadIdx.x & 63;
  if ((u32)(size_t)(lptr)smem != 0) return;
  STAMP(0);
  u32 ta, tb;
  u32x4 cf[5][5];
  if (wave >= 4) {
    if (j & 1) phase1<true, 4>(a, c, j, wave, lane, ta, tb, cf);
    else phase1<true, 3>(a, c, j, wave, lane, ta, tb, cf);
    phase2_m(a, c, j, wave, lane, ta, tb);
  } else {
    if (j & 1) phase1<false, 2>(a, c, j, wave, lane, ta, tb, cf);
    else phase1<false, 3>(a, c, j, wave, lane, ta, tb, cf);
    phase2_s(a, c, j, wave, lane, ta, tb, cf);
  }
}
}

extern "C" void kernel_launch(void* const* d_in, const int* in_sizes, int n_in, void* d_out, int out_size, void* d_ws,
                              size_t ws_size, hipStream_t stream) {
  using namespace s5k;
  Args a;
  a.x = (const u16*)d_in[0];
  a.lre = (const u16*)d_in[1];
  a.lim = (const u16*)d_in[2];
  a.bre = (const u16*)d_in[3];
  a.bim = (const u16*)d_in[4];
  a.cre = (const u16*)d_in[5];
  a.cim = (const u16*)d_in[6];
  a.ldt = (const u16*)d_in[7];
  a.dsk = (const u16*)d_in[8];
  a.w1 = (const u16*)d_in[10];
  a.w2 = (const u16*)d_in[11];
  a.out = (float*)d_out;
  a.ws = (char*)d_ws;
  (void)hipFuncSetAttribute((const void*)s5_fused, hipFuncAttributeMaxDynamicSharedMemorySize, LDS_BYTES);
  s5_fused<<<256, 512, LDS_BYTES, stream>>>(a);
}
```

```cpp
#define OAUX 16
#include <hip/hip_runtime.h>

#define STAMPJ(id)
namespace s5k {
typedef unsigned short u16;
typedef unsigned int u32;
typedef unsigned long long u64;
typedef short bf16x8 __attribute__((ext_vector_type(8)));
typedef short s16x2 __attribute__((ext_vector_type(2)));
typedef float f32x4 __attribute__((ext_vector_type(4)));
typedef float f32x2 __attribute__((ext_vector_type(2)));
typedef u32 u32x4 __attribute__((ext_vector_type(4)));
typedef u32 u32x2 __attribute__((ext_vector_type(2)));
typedef __bf16 bf16x2 __attribute__((ext_vector_type(2)));
#define LAS __attribute__((address_space(3)))
#define GAS __attribute__((address_space(1)))
typedef LAS char* lptr;
typedef __amdgpu_buffer_rsrc_t rsrc_t;

constexpr u32 WS_STATS = 4096;
constexpr u32 WS_AGG = 65536;
constexpr u32 WS_F1 = 262144, WS_F2 = WS_F1 + 32768, WS_F3 = WS_F2 + 32768;
constexpr u32 WS_PROF = 512 << 10;
constexpr u32 WS_H1 = 1u << 20;
constexpr u32 WS_HS = 9u << 20;
constexpr u32 WS_CT = 16u << 20;
constexpr u32 WS_WT = 18u << 20;
constexpr u32 WS_END = 21u << 20;
constexpr int CHUNK = 40960;
constexpr int LD_STG = 143360, LD_ST2 = LD_STG + 2048, LD_CS = LD_ST2 + 1024, LD_AGW = LD_CS + 1024, LD_CIN = LD_AGW + 1536,
              LD_CNT = LD_CIN + 1536, LD_TRB = LD_CNT + 256, LD_TAB = LD_TRB + 4096, LD_END = LD_TAB + 48 * 160;
constexpr int LDS_BYTES = 163840;
static_assert(LD_END <= LDS_BYTES, "lds");
enum { T_A = 0, T_C = 1, T_8 = 2, T_32 = 6, T_128 = 10, T_512 = 14 };
enum { C_F1 = 0, C_B2 = 1, C_S1 = 2, C_S2 = 3, C_F2 = 4, C_G = 5, C_G2 = 6, C_Y = 7 };
constexpr int SC1 = 16;
#define GMAP 2
constexpr u32 SPIN_CAP = 1u << 16;

struct Args {
  const u16 *x, *lre, *lim, *bre, *bim, *cre, *cim, *ldt, *dsk, *w1, *w2;
  float* out;
  char* ws;
};

__device__ __forceinline__ float u2f(u32 v) { return __builtin_bit_cast(float, v); }
__device__ __forceinline__ u32 f2u(float v) { return __builtin_bit_cast(u32, v); }
__device__ __forceinline__ float bf2f(u32 v) { return __builtin_bit_cast(float, v << 16); }
__device__ __forceinline__ float bfhi(u32 v) { return __builtin_bit_cast(float, v & 0xffff0000u); }
__device__ __forceinline__ u32 pk2(float a, float b) { return __builtin_bit_cast(u32, __builtin_convertvector(f32x2{a, b}, bf16x2)); }
__device__ __forceinline__ float rbf(float a) { return bf2f(pk2(a, a)); }
__device__ __forceinline__ f32x4 unpk4(u32x2 v) { return f32x4{bf2f(v.x), bfhi(v.x), bf2f(v.y), bfhi(v.y)}; }
__device__ __forceinline__ f32x4 rbf4(f32x4 v) { return unpk4(u32x2{pk2(v.x, v.y), pk2(v.z, v.w)}); }
__device__ __forceinline__ u32 relu2(u32 v) {
  return __builtin_bit_cast(u32, __builtin_elementwise_max(__builtin_bit_cast(s16x2, v), s16x2{0, 0}));
}
__device__ __forceinline__ f32x2 cmul(f32x2 a, f32x2 b) {
  float x, y;
  asm("v_mul_f32 %0, %2, %4\n\tv_mul_f32 %1, %2, %5\n\tv_fma_f32 %0, -%3, %5, %0\n\tv_fma_f32 %1, %3, %4, %1"
      : "=&v"(x), "=&v"(y) : "v"(a.x), "v"(a.y), "v"(b.x), "v"(b.y));
  return f32x2{x, y};
}
__device__ __forceinline__ f32x2 cfma(f32x2 a, f32x2 b, f32x2 c) {
  float x, y;
  asm("v_fma_f32 %0, %2, %4, %6\n\tv_fma_f32 %1, %2, %5, %7\n\tv_fma_f32 %0, -%3, %5, %0\n\tv_fma_f32 %1, %3, %4, %1"
      : "=&v"(x), "=&v"(y) : "v"(a.x), "v"(a.y), "v"(b.x), "v"(b.y), "v"(c.x), "v"(c.y));
  return f32x2{x, y};
}
__device__ __forceinline__ rsrc_t mk_rsrc(const void* p, u32 bytes) { return __builtin_amdgcn_make_buffer_rsrc((void*)p, 0, bytes, 0x00020000); }
__device__ __forceinline__ f32x4 mfma(bf16x8 a, bf16x8 b, f32x4 c) { return __builtin_amdgcn_mfma_f32_16x16x32_bf16(a, b, c, 0, 0, 0); }
__device__ __forceinline__ bf16x8 asfrag(u32x4 v) { return __builtin_bit_cast(bf16x8, v); }
__device__ __forceinline__ void dma16(rsrc_t rs, u32 dst, u32 voff, u32 soff) {
  asm volatile("s_mov_b32 m0, %0\n\ts_nop 0\n\tbuffer_load_dwordx4 %1, %2, %3 offen lds" ::"s"(dst), "v"(voff), "s"(rs), "s"(soff) : "memory");
}
__device__ __forceinline__ void dma16_sc1(rsrc_t rs, u32 dst, u32 voff, u32 soff) {
  asm volatile("s_mov_b32 m0, %0\n\ts_nop 0\n\tbuffer_load_dwordx4 %1, %2, %3 offen sc1 lds" ::"s"(dst), "v"(voff), "s"(rs), "s"(soff) : "memory");
}
__device__ __forceinline__ void dma4_sc1(rsrc_t rs, u32 dst, u32 voff, u32 soff) {
  asm volatile("s_mov_b32 m0, %0\n\ts_nop 0\n\tbuffer_load_dword %1, %2, %3 offen sc1 lds" ::"s"(dst), "v"(voff), "s"(rs), "s"(soff) : "memory");
}
__device__ __forceinline__ lptr L(u32 addr) { return (lptr)(size_t)addr; }
__device__ __forceinline__ bf16x8 ld128(u32 p) { return *(LAS bf16x8*)L(p); }
__device__ __forceinline__ f32x2 ldc(u32 p) { return *(LAS f32x2*)L(p); }
__device__ __forceinline__ f32x4 ld4(u32 p) { return *(LAS f32x4*)L(p); }
__device__ __forceinline__ bf16x8 ldtr(u32 p0, u32 p1) {
  typedef short v4i16 __attribute__((ext_vector_type(4)));
  v4i16 a = __builtin_amdgcn_ds_read_tr16_b64_v4i16((LAS v4i16*)L(p0));
  v4i16 b = __builtin_amdgcn_ds_read_tr16_b64_v4i16((LAS v4i16*)L(p1));
  return __builtin_shufflevector(a, b, 0, 1, 2, 3, 4, 5, 6, 7);
}
__device__ __forceinline__ u32x4 gld(rsrc_t rs, u32 voff, u32 soff) { return __builtin_amdgcn_raw_buffer_load_b128(rs, voff, soff, SC1); }
__device__ __forceinline__ void gst(u32x4 v, rsrc_t rs, u32 voff) { __builtin_amdgcn_raw_buffer_store_b128(v, rs, voff, 0, SC1); }
template <int N> __device__ __forceinline__ void wait_vm() { asm volatile("s_waitcnt vmcnt(%0)" ::"i"(N) : "memory"); }
__device__ __forceinline__ void wait_lgkm0() { __builtin_amdgcn_s_waitcnt(0xc07f); asm volatile("" ::: "memory"); }
__device__ __forceinline__ void wg_barrier() {
  asm volatile("" ::: "memory");
  __builtin_amdgcn_s_barrier();
  asm volatile("" ::: "memory");
}
#define PIN(x) asm volatile("" : "+v"(x))
#define SCHED0() __builtin_amdgcn_sched_barrier(0)
__device__ __forceinline__ u32 cnt_add(int idx, int lane) {
  u32 old;
  const u32 addr = LD_CNT + idx * 4 + (lane == 0 ? 0 : 64), one = 1;
  asm volatile("s_waitcnt lgkmcnt(0)\n\ts_mov_b64 exec, 1\n\tds_add_rtn_u32 %0, %1, %2\n\ts_mov_b64 exec, -1\n\ts_waitcnt lgkmcnt(0)"
               : "=&v"(old) : "v"(addr), "v"(one) : "memory");
  return __builtin_amdgcn_readfirstlane(old);
}
__device__ __forceinline__ void cnt_wait(int idx, u32 want) {
  const u32 addr = LD_CNT + idx * 4;
#pragma unroll 1
  for (u32 s = 0; s < (SPIN_CAP << 4); s++) {
    u32 v;
    asm volatile("ds_read_b32 %0, %1\n\ts_waitcnt lgkmcnt(0)" : "=&v"(v) : "v"(addr) : "memory");
    if (__builtin_amdgcn_readfirstlane(v) >= want) break;
    __builtin_amdgcn_s_sleep(1);
  }
}
__device__ __forceinline__ void polln(rsrc_t wsr, u32 off, int mask, u32 ta, u32 tb, int lane) {
  const u32 vo = off + (lane & mask) * 128;
#pragma unroll 1
  for (u32 s = 0; s < SPIN_CAP; s++) {
    u32x2 v = __builtin_amdgcn_raw_buffer_load_b64(wsr, vo, 0, SC1);
    if (__all((v.x == ta) & (v.y == tb))) break;
    __builtin_amdgcn_s_sleep(2);
  }
  __builtin_amdgcn_fence(__ATOMIC_ACQUIRE, "wavefront");
  asm volatile("" ::: "memory");
}
__device__ __forceinline__ void put_flag(rsrc_t wsr, u32 off, u32 ta, u32 tb, int lane) {
  __builtin_amdgcn_raw_buffer_store_b64(u32x2{ta, tb}, wsr, lane == 0 ? off : WS_END, 0, SC1);
}
template <int CTRL> __device__ __forceinline__ float dpp(float x) {
  return __builtin_bit_cast(float, __builtin_amdgcn_mov_dpp(__builtin_bit_cast(int, x), CTRL, 0xf, 0xf, true));
}
__device__ __forceinline__ void rows4(float x, float (&r)[4]) {
  auto s = __builtin_amdgcn_permlane16_swap(f2u(x), f2u(x), false, false);
  const u32 e = s[0], o = s[1];
  auto te = __builtin_amdgcn_permlane32_swap(e, e, false, false);
  auto to = __builtin_amdgcn_permlane32_swap(o, o, false, false);
  const u32 r0 = te[0], r2 = te[1], r1 = to[0], r3 = to[1];
  r[0] = u2f(r0); r[1] = u2f(r1); r[2] = u2f(r2); r[3] = u2f(r3);
}
__device__ __forceinline__ float rows_sum(float x) {
  auto s = __builtin_amdgcn_permlane16_swap(f2u(x), f2u(x), false, false);
  const u32 a = s[0], b = s[1];
  x = u2f(a) + u2f(b);
  auto t = __builtin_amdgcn_permlane32_swap(f2u(x), f2u(x), false, false);
  const u32 c = t[0], d = t[1];
  return u2f(c) + u2f(d);
}
__device__ __forceinline__ float wave_sum(float v) {
  v += dpp<0xB1>(v); v += dpp<0x4E>(v); v += dpp<0x124>(v); v += dpp<0x128>(v);
  return rows_sum(v);
}
__device__ __forceinline__ float fexp(float x) {
  const float hi = x * 1.4426950408889634f;
  const float lo = __builtin_fmaf(x, 1.4426950408889634f, -hi) + x * 1.925963033500011e-8f;
  return __builtin_amdgcn_exp2f(hi) * (1.0f + lo * 0.6931471805599453f);
}
__device__ __forceinline__ void fsincos(float x, float& sn, float& cs) {
  if (__builtin_expect(__any(!(__builtin_fabsf(x) < 2000.f)), 0)) { sincosf(x, &sn, &cs); return; }
  const float n = __builtin_rintf(x * 0.6366197723675814f);
  float r = __builtin_fmaf(n, -1.5703125f, x);
  r = __builtin_fmaf(n, -4.837512969970703125e-4f, r);
  r = __builtin_fmaf(n, -7.54978995489188e-8f, r);
  const float r2 = r * r;
  const float s = __builtin_fmaf(r * r2, __builtin_fmaf(r2, __builtin_fmaf(r2, -1.9515295891e-4f, 8.3321608736e-3f), -1.6666654611e-1f), r);
  const float c = __builtin_fmaf(r2 * r2, __builtin_fmaf(r2, __builtin_fmaf(r2, 2.443315711809948e-5f, -1.388731625493765e-3f), 4.166664568298827e-2f),
                                 __builtin_fmaf(r2, -0.5f, 1.0f));
  const int k = (int)n;
  const float s1 = (k & 1) ? c : s, c1 = (k & 1) ? s : c;
  sn = (k & 2) ? -s1 : s1;
  cs = ((k + 1) & 2) ? -c1 : c1;
}
#define STAMP(id) asm volatile("; MARK " #id)
#define STAMPI(id)

template <bool IS_M, int NP>
__device__ __forceinline__ void phase1(const Args& a, int c, int j, int wave, int lane, u32& ta, u32& tb, u32x4 (&cf)[5][5]) {
  constexpr int NU = 2 * NP;
  constexpr bool TYPE_B = IS_M ? (NP == 4) : (NP == 2);
  constexpr int nS = TYPE_B ? 2 : 3, nM = 6 - nS, MP = nM * 64;
  const int wm = wave & 3;
  const int i2 = j >> 1;
  const int s0 = 5 * i2 + (TYPE_B ? 3 : 0);
  const int m0 = 7 * i2 + ((i2 & 1) ? (TYPE_B ? 3 : 0) : (TYPE_B ? 0 : 4));
  const int li = lane & 15, q = lane >> 4, r8 = lane >> 3, sl = lane & 7;
  const rsrc_t wsr = mk_rsrc(a.ws, WS_END);

  u32x2 ep;
  u32x4 xr0, xr1;
  u32 plr, pli, pdt;
  {
    const GAS char* xrow = (const GAS char*)a.x + (size_t)(c * 128 + j * 8 + wave) * 1280;
    asm volatile("global_load_dwordx2 %0, %1, %2 sc1" : "=v"(ep) : "v"(0), "s"(a.ws) : "memory");
    asm volatile("global_load_dwordx4 %0, %1, %2" : "=v"(xr0) : "v"(lane * 16), "s"(xrow) : "memory");
    asm volatile("global_load_dwordx4 %0, %1, %2 offset:1024" : "=v"(xr1) : "v"(li * 16), "s"(xrow) : "memory");
    if (!IS_M) {
      const int ch = (s0 * 16 + (q < NP ? lane : li)) * 2;
      asm volatile("global_load_ushort %0, %1, %2" : "=v"(plr) : "v"(ch), "s"(a.lre) : "memory");
      asm volatile("global_load_ushort %0, %1, %2" : "=v"(pli) : "v"(ch), "s"(a.lim) : "memory");
      asm volatile("global_load_ushort %0, %1, %2" : "=v"(pdt) : "v"(ch), "s"(a.ldt) : "memory");
    }
  }
  rsrc_t drs[5];
  u32 dvo[5], dso[5], dstep[5], ddst[5];
  {
    const rsrc_t brs = mk_rsrc(wm < 2 ? a.bre : a.bim, 640 * 1280);
    const u32 bvo = ((wm & 1) * 8 + r8) * 1280 + ((sl ^ ((wm & 1) * 4 + (r8 >> 1))) << 4);
#pragma unroll
    for (int b = 0; b < 5; b++) {
      if (!IS_M && b < 4) {
        drs[b] = mk_rsrc(a.x, 2048 * 1280);
        dvo[b] = (8 * (r8 >> 2) + (r8 & 3)) * 1280 + ((sl ^ ((b & 1) * 4 + (r8 >> 1))) << 4);
        dso[b] = (c * 128 + wm * 32 + (b & 1) * 16 + (b >> 1) * 4) * 1280;
        dstep[b] = 128;
        ddst[b] = (wm * 4 + b) * 1024;
      } else if (!IS_M || b < nS - 1) {
        const int p = IS_M ? b + 1 : 0;
        drs[b] = brs;
        dvo[b] = bvo;
        dso[b] = (s0 + p) * 16 * 1280;
        dstep[b] = 128;
        ddst[b] = 16384 + p * 4096 + wm * 1024;
      } else {
        const int t = b - (nS - 1);
        const int idx = t * 64 + lane;
        const int kr = idx / (4 * nM), cpos = idx - kr * (4 * nM);
        const int sw = TYPE_B ? ((kr & 3) | (((kr >> 3) & 1) << 2)) : ((kr >> 3) & 1);
        int col16 = m0 * 4 + (((cpos >> 1) ^ sw) << 1) + (cpos & 1);
        if (TYPE_B && col16 >= 216) col16 -= 8;
        drs[b] = mk_rsrc(a.w1, 640 * 3456);
        dvo[b] = kr * 3456 + col16 * 16;
        dso[b] = wm * 16 * 3456;
        dstep[b] = 64 * 3456;
        ddst[b] = 16384 + nS * 4096 + (wm * nM + t) * 1024;
      }
    }
  }
  int kc = c + j;
  kc -= kc >= 20 ? 20 : kc >= 10 ? 10 : 0;
  kc = kc == 10 ? 0 : kc;
  auto dma1 = [&](int bufoff, int b) { dma16_sc1(drs[b], bufoff + ddst[b], dvo[b], dso[b] + kc * dstep[b]); };
  auto issue = [&](int bufoff) {
#pragma unroll
    for (int b = 0; b < 5; b++) dma1(bufoff, b);
    kc = kc == 9 ? 0 : kc + 1;
  };
  issue(0);
  *(LAS u32*)L(LD_CNT + li * 4) = 0;
  const int rid = c * 2 + (j & 1);
  STAMP(1);

  int pr[NP];
#pragma unroll
  for (int p = 0; p < NP; p++) pr[p] = (p + wm) % NP;
  u32 xb[2][2], wb[2][NU];
  {
    const u32 xo = li * 128 + ((q ^ (li >> 1)) << 4);
    const int qq = li >> 2;
    const u32 rowb = 16384 + nS * 4096 + (8 * q + qq) * MP + (lane & 3) * 8;
    const int f = TYPE_B ? (qq | ((q & 1) << 2)) : (q & 1);
#pragma unroll
    for (int h = 0; h < 2; h++) {
#pragma unroll
      for (int kk = 0; kk < 2; kk++) {
        xb[h][kk] = h * 2 * CHUNK + wm * 4096 + (xo ^ (kk << 6));
        PIN(xb[h][kk]);
      }
#pragma unroll
      for (int u = 0; u < NU; u++) {
        wb[h][u] = h * 2 * CHUNK + (IS_M ? rowb + (((2 * pr[u >> 1] + (u & 1)) ^ f) << 5) : 16384 + pr[u >> 1] * 4096 + (xo ^ ((u & 1) << 6)));
        PIN(wb[h][u]);
      }
    }
  }
  bf16x8 xf[2][2], wf[2][NU];
  auto read1 = [&](int set, int bufoff, int kk, int f) {
    const int h = bufoff == 2 * CHUNK, bo = h ? 0 : bufoff;
    if (f < 2) {
      xf[set][f] = ld128(xb[h][kk] + (bo + f * 2048));
    } else if (f < NU + 2) {
      const int u = f - 2;
      if (!IS_M) wf[set][u] = ld128(wb[h][(u & ~1) + kk] + (bo + (u & 1) * 2048));
      else wf[set][u] = ldtr(wb[h][u] + (bo + kk * 32 * MP), wb[h][u] + (bo + kk * 32 * MP + 4 * MP));
    }
  };
  f32x4 acc[2][NU], csum[2];
#pragma unroll
  for (int m = 0; m < 2; m++) {
#pragma unroll
    for (int u = 0; u < NU; u++) { acc[m][u] = f32x4{0.f, 0.f, 0.f, 0.f}; PIN(acc[m][u]); }
    csum[m] = f32x4{0.f, 0.f, 0.f, 0.f};
    PIN(csum[m]);
  }
  u32x4 ones4 = {0x3f803f80u, 0x3f803f80u, 0x3f803f80u, 0x3f803f80u};
  PIN(ones4);
  constexpr int G = NU + 1;
  constexpr int NDB = IS_M ? 2 : 3;
  auto dpos = [](int d) { return (2 * G * d + (IS_M ? G : 0)) / 5; };
  static_assert((2 * G * (NDB - 1) + (IS_M ? G : 0)) / 5 < G && (2 * G * NDB + (IS_M ? G : 0)) / 5 >= G, "split");
  auto step = [&](int set, int rbuf, int rkk, int dbuf) {
#pragma unroll
    for (int g = 0; g < G; g++) {
      if (g > 0) {
        const int u = g - 1;
#pragma unroll
        for (int m = 0; m < 2; m++)
          acc[m][u] = IS_M ? mfma(wf[set][u], xf[set][m], acc[m][u]) : mfma(xf[set][m], wf[set][u], acc[m][u]);
      } else {
        csum[0] = mfma(asfrag(ones4), wf[set][0], csum[0]);
        csum[1] = mfma(asfrag(ones4), wf[set][1], csum[1]);
      }
      if (rbuf >= 0) {
        if (g == 0) { read1(set ^ 1, rbuf, rkk, 2); read1(set ^ 1, rbuf, rkk, 3); }
        else if (g == 1) { read1(set ^ 1, rbuf, rkk, 0); read1(set ^ 1, rbuf, rkk, 1); }
        else read1(set ^ 1, rbuf, rkk, g + 2);
      } else if (g == 3) {
        LAS float* csp = (LAS float*)L(LD_CS + (IS_M ? 512 : 0) + (wm < NP ? wm : 3) * 128 + (lane & 31) * 4);
        float e0 = csum[0].x, e1 = csum[1].x;
        PIN(e0); PIN(e1);
        if (wm < NP) *csp = (q & 1) ? e1 : e0;
      }
#pragma unroll
      for (int d = 0; d < 5; d++)
        if ((set == 1 ? g : g + G) == dpos(d)) dma1(dbuf, d);
      SCHED0();
    }
  };

  asm volatile("s_waitcnt vmcnt(5)" : "+v"(ep), "+v"(xr0), "+v"(xr1)::"memory");
  if (!IS_M) asm volatile("" : "+v"(plr), "+v"(pli), "+v"(pdt));
  {
    u64 z = ((u64)__builtin_amdgcn_readfirstlane(ep.y) << 32 | __builtin_amdgcn_readfirstlane(ep.x)) + 0x9E3779B97F4A7C15ull;
    z = (z ^ (z >> 30)) * 0xBF58476D1CE4E5B9ull;
    z = (z ^ (z >> 27)) * 0x94D049BB133111EBull;
    z ^= z >> 31;
    ta = (u32)z;
    tb = (u32)(z >> 32);
    float v[16];
#pragma unroll
    for (int e = 0; e < 4; e++) {
      v[2 * e] = bf2f(xr0[e]); v[2 * e + 1] = bfhi(xr0[e]);
      v[8 + 2 * e] = bf2f(xr1[e]); v[8 + 2 * e + 1] = bfhi(xr1[e]);
    }
    float s0_ = 0.f, s1_ = 0.f;
#pragma unroll
    for (int e = 0; e < 8; e++) { s0_ += v[e]; s1_ += v[8 + e]; }
    const float mean = wave_sum(lane < 16 ? s0_ + s1_ : s0_) * (1.0f / 640.0f);
    s0_ = 0.f; s1_ = 0.f;
#pragma unroll
    for (int e = 0; e < 8; e++) {
      const float d0 = v[e] - mean, d1 = v[8 + e] - mean;
      s0_ += d0 * d0; s1_ += d1 * d1;
    }
    const float var = wave_sum(lane < 16 ? s0_ + s1_ : s0_) * (1.0f / 640.0f);
    const float mb = rbf(mean), den = rbf(__builtin_amdgcn_sqrtf(rbf(rbf(var) + 1e-5f)));
    gst(u32x4{f2u(mb), ta, f2u(__builtin_amdgcn_rcpf(den)), tb}, wsr, lane == 0 ? WS_STATS + (c * 128 + j * 8 + wave) * 16 : WS_END);
  }
  if (!IS_M) {
    if (wm == 0) {
      const float lr = bf2f(plr), lim = bf2f(pli);
      const float dt = rbf(fexp(bf2f(pdt)));
      const float er = fexp(dt * lr);
      float sn, cn;
      fsincos(dt * lim, sn, cn);
      const f32x2 a1 = {er * cn, er * sn};
      const float nr = a1.x - 1.0f, dn = 1.0f / (lr * lr + lim * lim);
      const u32 tp = LD_TAB + (lane < 48 ? lane : 47) * 160;
      if (q < NP) {
        *(LAS f32x4*)L(tp) = f32x4{a1.x, a1.y, (nr * lr + a1.y * lim) * dn, (a1.y * lr - nr * lim) * dn};
        const f32x2 a2 = cmul(a1, a1), a4 = cmul(a2, a2);
        f32x2 b = cmul(a4, a4);
#pragma unroll
        for (int i = 0; i < 4; i++) {
          const f32x2 b2 = cmul(b, b), b3 = cmul(b2, b);
          *(LAS f32x4*)L(tp + 16 + i * 32) = f32x4{1.f, 0.f, b.x, b.y};
          *(LAS f32x4*)L(tp + 32 + i * 32) = f32x4{b2.x, b2.y, b3.x, b3.y};
          b = cmul(b2, b2);
        }
      }
    }
  }
  issue(CHUNK);
#pragma unroll
  for (int d = 0; d < NDB; d++) dma1(2 * CHUNK, d);
  u32x4 rt[2];
  const int rks = min(rid * 2 + (wm & 1), 53);
  if (!IS_M) {
#pragma unroll
    for (int i = 0; i < 2; i++) {
      const int f = min(rid * 8 + wm * 2 + i, 199);
      const int wk = f / 5, n = f - wk * 5, w = wk / 10, k = wk - w * 10;
      const GAS char* src = (const GAS char*)(k < 5 ? a.cre : a.cim) + (i2 * 80 + n * 16) * 1280 + (160 * w + 32 * (k < 5 ? k : k - 5)) * 2;
      asm volatile("global_load_dwordx4 %0, %1, %2" : "=v"(rt[i]) : "v"(li * 1280 + q * 16), "s"(src) : "memory");
    }
    wait_vm<6 + NDB + 2>();
  } else {
    wait_vm<6 + NDB>();
  }
  wait_lgkm0();
  wg_barrier();
  STAMP(2);
  if (IS_M) __builtin_amdgcn_s_setprio(1);
#pragma unroll
  for (int f = 0; f < NU + 2; f++) read1(0, 0, 0, f);
  int it = 0;
  auto body = [&](int prv, int cur, int nxt) {
    step(0, cur, 1, prv);
    kc = kc == 9 ? 0 : kc + 1;
    if (nxt < 0) {
      const int r = wave * 16 + li;
      u32x4 g = *(LAS u32x4*)L(LD_STG + r * 16);
      if (!__all((g.y == ta) & (g.w == tb))) {
#pragma unroll 1
        for (u32 s = 0; s < SPIN_CAP; s++) {
          g = gld(wsr, WS_STATS + (c * 128 + r) * 16, 0);
          if (__all((g.y == ta) & (g.w == tb))) break;
        }
      }
      const int t5 = r & 31;
      *(LAS f32x2*)L(LD_ST2 + ((r & ~31) + (((t5 >> 2) & 1) << 4) + ((t5 >> 3) << 2) + (t5 & 3)) * 8) = f32x2{u2f(g.x), u2f(g.z)};
    } else if (!IS_M && it == 0) wait_vm<7>();
    else if (IS_M && it == 1 && wm < 2) wait_vm<10>();
    else wait_vm<5>();
    wait_lgkm0();
    wg_barrier();
    STAMPI(3 + it);
    if (IS_M && it == 0 && wm < 2) {
      const rsrc_t w2r = mk_rsrc(a.w2, 1728 * 1280);
#pragma unroll
      for (int b = 0; b < 5; b++) {
        const int idx = b * 64 + lane, kr = idx / 10, cpos = idx - kr * 10;
        dma16(w2r, 3 * CHUNK + wm * 5120 + b * 1024, kr * 1280 + i2 * 160 + cpos * 16, rks * (32 * 1280));
      }
    }
    if (it == 2) {
      if (!IS_M) {
        asm volatile("" : "+v"(rt[0]), "+v"(rt[1]));
#pragma unroll
        for (int i = 0; i < 2; i++) gst(rt[i], wsr, WS_CT + (i2 * 200 + min(rid * 8 + wm * 2 + i, 199)) * 1024 + lane * 16);
      } else if (wm < 2) {
        const u32 p = 3 * CHUNK + wm * 5120 + (4 * q + (li >> 2)) * 160 + (lane & 3) * 8;
#pragma unroll
        for (int n = 0; n < 5; n++)
          gst(__builtin_bit_cast(u32x4, ldtr(p + n * 32, p + n * 32 + 2560)), wsr, WS_WT + ((i2 * 54 + rks) * 5 + n) * 1024 + lane * 16);
      }
    }
    if (IS_M && it == 3 && wm == 3) put_flag(wsr, WS_F3 + (i2 * 32 + rid) * 128, ta, tb, lane);
    if (it >= 7) {
      if (it == 7) {
        const u32 f = *(LAS u32*)L(LD_TRB + wave * 256 + lane * 4);
        if (!__all(f == ((lane & 1) ? tb : ta))) polln(wsr, WS_F3 + i2 * 32 * 128, 31, ta, tb, lane);
#pragma unroll
        for (int b = 0; b < 5; b++) {
          drs[b] = wsr; dvo[b] = lane * 16; dso[b] = WS_WT + i2 * 54 * 5120 + b * 1024; dstep[b] = 5120; ddst[b] = wave * 5120 + b * 1024;
        }
      }
      kc = wm * 14 + (cur / CHUNK) * 2 + (IS_M ? 1 : 0);
    }
    SCHED0();
    step(1, nxt, 0, cur);
    if (it == 5) {
      dma4_sc1(wsr, LD_TRB + wave * 256, (lane >> 1) * 128 + (lane & 1) * 4, WS_F3 + i2 * 32 * 128);
      dma4_sc1(wsr, LD_STG + wave * 256, lane * 4, WS_STATS + c * 2048 + wave * 256);
    }
    it++;
  };
#pragma unroll 1
  for (int r = 0; r < 3; r++) {
    body(2 * CHUNK, 0, CHUNK);
    body(0, CHUNK, 2 * CHUNK);
    body(CHUNK, 2 * CHUNK, 0);
  }
  body(2 * CHUNK, 0, -1);
#pragma unroll
  for (int d = NDB; d < 5; d++) dma1(0, d);
  if (IS_M) __builtin_amdgcn_s_setprio(0);
  else __builtin_amdgcn_s_setprio(2);
  STAMP(3);
  float mb[2];
  f32x4 st[4];
  f32x2 ca[NP], cc[NP], a8r[NP];
  u32 tp[NP];
  if (IS_M) {
#pragma unroll
    for (int m = 0; m < 2; m++) mb[m] = *(LAS float*)L(LD_ST2 + (wm * 32 + m * 16 + li) * 8);
  } else {
#pragma unroll
    for (int i = 0; i < 4; i++) st[i] = ld4(LD_ST2 + (wm * 32 + (i >> 1) * 16 + 4 * q + 2 * (i & 1)) * 8);
#pragma unroll
    for (int p = 0; p < NP; p++) {
      tp[p] = LD_TAB + (pr[p] * 16 + li) * 160;
      const f32x4 t0 = ld4(tp[p]);
      ca[p] = f32x2{t0.x, t0.y};
      cc[p] = f32x2{t0.z, t0.w};
      a8r[p] = ldc(tp[p] + (T_8 + 3 - q) * 8);
    }
  }
  wait_lgkm0();
  wg_barrier();
  STAMP(4);

  if (IS_M) {
#pragma unroll
    for (int p = 0; p < NP; p++) {
      const f32x4 c0 = ld4(LD_CS + 512 + pr[p] * 128 + q * 16), c1 = ld4(LD_CS + 512 + pr[p] * 128 + 64 + q * 16);
      const u32 off = (m0 + pr[p] < 54) ? WS_H1 + ((c * 8 + 2 * wm) * 54 + m0 + pr[p]) * 1024 : WS_END;
#pragma unroll
      for (int m = 0; m < 2; m++) {
        const f32x4 v0 = acc[m][2 * p] - mb[m] * c0, v1 = acc[m][2 * p + 1] - mb[m] * c1;
        gst(u32x4{relu2(pk2(v0.x, v0.y)), relu2(pk2(v0.z, v0.w)), relu2(pk2(v1.x, v1.y)), relu2(pk2(v1.z, v1.w))}, wsr,
            off + m * 54 * 1024 + lane * 16);
      }
    }
  } else {
    f32x2 u[NP][8];
    f32x2 seg[NP];
    float Sr[NP], Si[NP];
#pragma unroll
    for (int p = 0; p < NP; p++) {
      Sr[p] = *(LAS float*)L(LD_CS + pr[p] * 128 + li * 4);
      Si[p] = *(LAS float*)L(LD_CS + pr[p] * 128 + 64 + li * 4);
    }
#pragma unroll
    for (int p = 0; p < NP; p++) {
      f32x2 h;
#pragma unroll
      for (int t = 0; t < 8; t++) {
        const float mu = st[t >> 1][(t & 1) * 2], rs = st[t >> 1][(t & 1) * 2 + 1];
        u[p][t] = f32x2{(acc[t >> 2][2 * p][t & 3] - mu * Sr[p]) * rs, (acc[t >> 2][2 * p + 1][t & 3] - mu * Si[p]) * rs};
        h = t ? cfma(ca[p], h, u[p][t]) : u[p][0];
      }
      seg[p] = h;
      h = cmul(a8r[p], h);
      *(LAS f32x2*)L(LD_AGW + ((wm * 3 + pr[p]) * 16 + li) * 8) = f32x2{rows_sum(h.x), rows_sum(h.y)};
    }
    if (cnt_add(C_S1, lane) == 3) {
      const int ch = lane < 48 ? lane : 47;
      const f32x2 a32 = ldc(LD_TAB + ch * 160 + (T_32 + 1) * 8);
      f32x2 h = ldc(LD_AGW + ch * 8);
#pragma unroll
      for (int w = 1; w < 4; w++) h = cfma(a32, h, ldc(LD_AGW + (w * 48 + ch) * 8));
      gst(u32x4{f2u(h.x), ta, f2u(h.y), tb}, wsr, q < NP ? WS_AGG + (c * 640 + s0 * 16 + lane) * 16 : WS_END);
    }
    STAMP(5);
    {
      const u32 co = WS_CT + (i2 * 200 + wm * 50) * 1024 + lane * 16;
#pragma unroll
      for (int k = 0; k < 5; k++)
#pragma unroll
        for (int n = 0; n < 5; n++) cf[k][n] = gld(wsr, co, (k * 5 + n) * 1024);
      SCHED0();
      wait_vm<25>();
      cnt_add(C_B2, lane);
    }
    cnt_wait(C_S1, 4);
    f32x2 aq[NP], pre[NP];
    u32 hso[NP];
#pragma unroll
    for (int p = 0; p < NP; p++) {
      const f32x2 a32 = ldc(tp[p] + (T_32 + 1) * 8), a8q = ldc(tp[p] + (T_8 + q) * 8), aw = ldc(tp[p] + (T_32 + wm) * 8);
      f32x2 h = {0.f, 0.f};
#pragma unroll
      for (int w = 0; w < 3; w++) {
        const f32x2 g = ldc(LD_AGW + ((w * 3 + pr[p]) * 16 + li) * 8);
        if (w < wm) h = cfma(a32, h, g);
      }
#pragma unroll
      for (int t = 0; t < 8; t++) u[p][t] = cmul(cc[p], u[p][t]);
      {
        const f32x2 a8 = ldc(tp[p] + (T_8 + 1) * 8);
        float gr[4], gi[4];
        rows4(seg[p].x, gr); rows4(seg[p].y, gi);
#pragma unroll
        for (int w = 0; w < 3; w++) {
          const f32x2 t = cfma(a8, h, f32x2{gr[w], gi[w]});
          h.x = w < q ? t.x : h.x;
          h.y = w < q ? t.y : h.y;
          PIN(h);
        }
      }
      pre[p] = cmul(cc[p], h);
      aq[p] = cmul(cc[p], cmul(a8q, aw));
      const int cu = s0 + pr[p];
      hso[p] = WS_HS + ((c * 8 + 2 * wm) * 40 + (cu / 10) * 10 + ((cu % 10) >> 1) + (lane >> 5) * 5) * 1024 + (cu & 1) * 512 + (lane & 31) * 16;
      asm volatile("" : "+v"(pre[p]), "+v"(aq[p]), "+v"(hso[p]), "+v"(u[p][0]), "+v"(u[p][1]), "+v"(u[p][2]), "+v"(u[p][3]), "+v"(u[p][4]),
                   "+v"(u[p][5]), "+v"(u[p][6]), "+v"(u[p][7]));
    }
    u32 tw = LD_TRB + wm * 1024 + (li >> 3) * 256 + q * 64 + (li & 7) * 2, tr = LD_TRB + wm * 1024 + lane * 16;
    PIN(tw); PIN(tr);
    cnt_wait(C_S2, 4);
    STAMP(6);
#pragma unroll
    for (int p = 0; p < NP; p++) {
      const u32 ci = LD_CIN + (pr[p] * 16 + li) * 8;
      f32x2 h = (ldc(ci) + ldc(ci + 384)) + (ldc(ci + 768) + ldc(ci + 1152));
      h = cfma(aq[p], h, pre[p]);
#pragma unroll
      for (int m = 0; m < 2; m++) {
#pragma unroll
        for (int r = 0; r < 4; r++) {
          h = cfma(ca[p], h, u[p][4 * m + r]);
          const u32 pk = pk2(h.x, -h.y);
          *(LAS u16*)L(tw + r * 16) = (u16)pk;
          *(LAS u16*)L(tw + r * 16 + 512) = (u16)(pk >> 16);
        }
        asm volatile("" ::: "memory");
        gst(*(LAS u32x4*)L(tr), wsr, hso[p] + m * 40 * 1024);
        asm volatile("" ::: "memory");
      }
    }
    if (c == 15 && j == 0 && wm == 0) {
      const u64 e = ((u64)ep.y << 32 | ep.x) + 1;
      __builtin_amdgcn_raw_buffer_store_b64(u32x2{(u32)e, (u32)(e >> 32)}, wsr, lane == 0 ? 0 : WS_END, 0, SC1);
    }
  }
}

__device__ __forceinline__ void phase2_m(const Args& a, int c, int j, int wave, int lane, u32 ta, u32 tb) {
  const int wm = wave & 3, li = lane & 15, q = lane >> 4;
  const rsrc_t wsr = mk_rsrc(a.ws, WS_END);
  const int rh = j & 1, i2 = j >> 1, dcol0 = i2 * 80;
  const u32 wo = WS_WT + (i2 * 54 + wm * 14) * 5120;
#pragma unroll
  for (int b = 0; b < 5; b++) dma16_sc1(wsr, (24 + wm) * 5120 + b * 1024, lane * 16, wo + (30 + b) * 1024);
  const int cp = 4 * wm + q, e = c - 1 - cp, s0 = 5 * i2 + (rh ? 3 : 0), np = rh ? 2 : 3;
  f32x2 pw[3];
#pragma unroll
  for (int p = 0; p < 3; p++) {
    const u32 tp = LD_TAB + ((p < np ? p : 0) * 16 + li) * 160;
    pw[p] = cmul(ldc(tp + (T_128 + (e & 3)) * 8), ldc(tp + (T_512 + ((e >> 2) & 3)) * 8));
    if (e < 0) pw[p] = f32x2{0.f, 0.f};
  }
  wait_vm<5>();
  if (cnt_add(C_F1, lane) == 3) put_flag(wsr, WS_F1 + (c * 16 + j) * 128, ta, tb, lane);
  STAMP(5);
  {
    u32x4 g[3];
    const u32 go = WS_AGG + (cp * 640 + s0 * 16 + li) * 16;
#pragma unroll 1
    for (u32 s = 0; s < SPIN_CAP; s++) {
      bool ok = true;
#pragma unroll
      for (int p = 0; p < 3; p++) g[p] = gld(wsr, go, (p < np ? p : 0) * 256);
#pragma unroll
      for (int p = 0; p < 3; p++) ok = ok & (g[p].y == ta) & (g[p].w == tb);
      if (__all(ok | (e < 0))) break;
      __builtin_amdgcn_s_sleep(1);
    }
#pragma unroll
    for (int p = 0; p < 3; p++) {
      const f32x2 h = cmul(pw[p], e < 0 ? f32x2{0.f, 0.f} : f32x2{u2f(g[p].x), u2f(g[p].z)});
      *(LAS f32x2*)L(LD_CIN + ((wm * 3 + p) * 16 + li) * 8) = f32x2{rows_sum(h.x), rows_sum(h.y)};
    }
    cnt_add(C_S2, lane);
    cnt_add(C_B2, lane);
  }
  STAMP(6);
  u32x4 w[3][5];
  auto ldw = [&](int r, int k) {
#pragma unroll
    for (int n = 0; n < 5; n++) w[r][n] = gld(wsr, lane * 16, wo + (((k < 12 || wm < 3) ? k : 0) * 5 + n) * 1024);
  };
  polln(wsr, WS_F1 + (c * 16 + 4 * wm) * 128, 3, ta, tb, lane);
  STAMP(7);
  f32x4 g[4][5];
  {
    u32 wbase[3];
#pragma unroll
    for (int h = 0; h < 3; h++) {
      wbase[h] = wm * 5120 + h * 61440 + lane * 16;
      PIN(wbase[h]);
    }
    auto rd = [&](int r, int slot) {
#pragma unroll
      for (int n = 0; n < 5; n++) w[r][n] = __builtin_bit_cast(u32x4, ld128(wbase[slot / 3] + ((slot % 3) * 20480 + n * 1024)));
    };
    auto rs = [](int s) { return s < 7 ? (s & 1) : 2 - (s - 7) % 3; };
    u32x4 hf[4][4];
    const u32 hb = WS_H1 + ((c * 8 + 4 * rh) * 54 + wm * 14) * 1024 + lane * 16;
    auto lda = [&](int s) {
#pragma unroll
      for (int m = 0; m < 4; m++) hf[s & 3][m] = gld(wsr, hb, (m * 54 + ((s < 12 || wm < 3) ? s : 0)) * 1024);
    };
#pragma unroll
    for (int s = 0; s < 4; s++) lda(s);
    ldw(2, 7);
    SCHED0();
#pragma unroll
    for (int m = 0; m < 4; m++)
#pragma unroll
      for (int n = 0; n < 5; n++) g[m][n] = f32x4{0.f, 0.f, 0.f, 0.f};
    cnt_wait(C_B2, 8);
    STAMPJ(1);
    rd(0, 0);
#pragma unroll
    for (int s = 0; s < 14; s++) {
      if (s < 12 || wm < 3) {
        if (s + 1 < 7) rd(rs(s + 1), s + 1);
#pragma unroll
        for (int n = 0; n < 5; n++)
#pragma unroll
          for (int m = 0; m < 4; m++) g[m][n] = mfma(asfrag(w[rs(s)][n]), asfrag(hf[s & 3][m]), g[m][n]);
        SCHED0();
        if (s < 11) STAMPJ(2 + s);
        if (s + 4 < 14) lda(s + 4);
        if (s >= 5 && s + 3 < 14) ldw(rs(s), s + 3);
        SCHED0();
      }
    }
  }
  STAMP(8);
  const int tt = 4 * rh + wm;
  const int tok = c * 128 + (tt >> 1) * 32 + 8 * (li >> 2) + 4 * (tt & 1) + (li & 3);
  u32x2 xv[5], dv[5];
#pragma unroll
  for (int n = 0; n < 5; n++) {
    xv[n] = *(const GAS u32x2*)((const GAS u16*)a.x + tok * 640 + dcol0 + 16 * n + 4 * q);
    dv[n] = *(const GAS u32x2*)((const GAS u16*)a.dsk + dcol0 + 16 * n + 4 * q);
  }
#pragma unroll
  for (int m = 0; m < 4; m++)
#pragma unroll
    for (int n = 0; n < 5; n++) *(LAS f32x4*)L((m * 4 + wm) * 5120 + n * 1024 + lane * 16) = g[m][n];
  cnt_add(C_G, lane);
  f32x4 gate[5], skip[5], xs[5];
  const f32x2 st = ldc(LD_ST2 + (tt * 16 + li) * 8);
#pragma unroll
  for (int n = 0; n < 5; n++) {
    xs[n] = unpk4(xv[n]);
    skip[n] = rbf4(rbf4(rbf4(xs[n] - st.x) * st.y) * unpk4(dv[n]));
    PIN(skip[n]);
  }
  cnt_wait(C_G, 4);
  STAMP(9);
#pragma unroll
  for (int n = 0; n < 5; n++) {
    const u32 gp = wm * 20480 + n * 1024 + lane * 16;
    gate[n] = (ld4(gp) + ld4(gp + 5120)) + (ld4(gp + 10240) + ld4(gp + 15360));
    PIN(gate[n]);
  }
  cnt_add(C_G2, lane);
#pragma unroll
  for (int n = 0; n < 5; n++) {
    const f32x4 gb = rbf4(gate[n] * st.y);
    f32x4 sg;
#pragma unroll
    for (int e2 = 0; e2 < 4; e2++) sg[e2] = gb[e2] * __builtin_amdgcn_rcpf(1.0f + __builtin_amdgcn_exp2f(gb[e2] * -1.4426950408889634f));
    gate[n] = rbf4(sg);
    PIN(gate[n]);
  }
  STAMP(11);
  cnt_wait(C_Y, 4);
  STAMP(12);
#pragma unroll
  for (int n = 0; n < 5; n++) {
    const u32 yp = (wm * 5 + n) * 1024 + lane * 16;
    const f32x4 y = (ld4(yp) + ld4(yp + 20480)) + (ld4(yp + 40960) + ld4(yp + 61440));
    __builtin_amdgcn_raw_buffer_store_b128(__builtin_bit_cast(u32x4, xs[n] + (2.0f * y + skip[n]) * gate[n]), mk_rsrc(a.out, 2048 * 640 * 4), (tok * 640 + dcol0 + 16 * n + 4 * q) * 4, 0, OAUX);
  }
  STAMP(13);
}

__device__ __forceinline__ void phase2_s(const Args& a, int c, int j, int wave, int lane, u32 ta, u32 tb, u32x4 (&cf)[5][5]) {
  const int wm = wave & 3;
  const rsrc_t wsr = mk_rsrc(a.ws, WS_END);
  const int rh = j & 1;
  STAMP(7);
  wait_vm<0>();
  if (cnt_add(C_F2, lane) == 3) put_flag(wsr, WS_F2 + (c * 16 + j) * 128, ta, tb, lane);
  STAMP(8);
  f32x4 y[4][5];
  {
    const u32 co = WS_CT + ((j >> 1) * 200 + wm * 50 + 25) * 1024 + lane * 16;
    polln(wsr, WS_F2 + (c * 16 + 4 * wm) * 128, 3, ta, tb, lane);
    STAMP(9);
    STAMPJ(1);
    constexpr int AW = 3;
    u32x4 hf[AW][4];
    const u32 hb = WS_HS + ((c * 8 + 4 * rh) * 40 + wm * 10) * 1024 + lane * 16;
#pragma unroll
    for (int k = 0; k < AW; k++)
#pragma unroll
      for (int m = 0; m < 4; m++) hf[k][m] = gld(wsr, hb, (m * 40 + k) * 1024);
    SCHED0();
#pragma unroll
    for (int m = 0; m < 4; m++)
#pragma unroll
      for (int n = 0; n < 5; n++) y[m][n] = f32x4{0.f, 0.f, 0.f, 0.f};
#pragma unroll
    for (int k = 0; k < 10; k++) {
#pragma unroll
      for (int n = 0; n < 5; n++)
#pragma unroll
        for (int m = 0; m < 4; m++) y[m][n] = mfma(asfrag(cf[k % 5][n]), asfrag(hf[k % AW][m]), y[m][n]);
      SCHED0();
      STAMPJ(2 + k);
      if (k + AW < 10) {
#pragma unroll
        for (int m = 0; m < 4; m++) hf[k % AW][m] = gld(wsr, hb, (m * 40 + k + AW) * 1024);
      }
      if (k < 5) {
#pragma unroll
        for (int n = 0; n < 5; n++) cf[k][n] = gld(wsr, co, (k * 5 + n) * 1024);
      }
      SCHED0();
    }
  }
  STAMP(10);
  cnt_wait(C_G2, 4);
#pragma unroll
  for (int m = 0; m < 4; m++)
#pragma unroll
    for (int n = 0; n < 5; n++) *(LAS f32x4*)L(wm * 20480 + (m * 5 + n) * 1024 + lane * 16) = y[m][n];
  cnt_add(C_Y, lane);
  STAMP(11);
}

__global__ __launch_bounds__(512, 2) void s5_fused(Args a) {
  extern __shared__ __attribute__((aligned(16))) char smem[];
  const int b = blockIdx.x, xcd = b & 7, r = b >> 3;
  const int c = xcd * 2 + (r >> 4), j = r & 15;
  const int wave = __builtin_amdgcn_readfirstlane(threadIdx.x >> 6), lane = threadIdx.x & 63;
  if ((u32)(size_t)(lptr)smem != 0) return;
  STAMP(0);
  u32 ta, tb;
  u32x4 cf[5][5];
  if (wave >= 4) {
    if (j & 1) phase1<true, 4>(a, c, j, wave, lane, ta, tb, cf);
    else phase1<true, 3>(a, c, j, wave, lane, ta, tb, cf);
    phase2_m(a, c, j, wave, lane, ta, tb);
  } else {
    if (j & 1) phase1<false, 2>(a, c, j, wave, lane, ta, tb, cf);
    else phase1<false, 3>(a, c, j, wave, lane, ta, tb, cf);
    phase2_s(a, c, j, wave, lane, ta, tb, cf);
  }
}
}

extern "C" void kernel_launch(void* const* d_in, const int* in_sizes, int n_in, void* d_out, int out_size, void* d_ws,
                              size_t ws_size, hipStream_t stream) {
  using namespace s5k;
  Args a;
  a.x = (const u16*)d_in[0];
  a.lre = (const u16*)d_in[1];
  a.lim = (const u16*)d_in[2];
  a.bre = (const u16*)d_in[3];
  a.bim = (const u16*)d_in[4];
  a.cre = (const u16*)d_in[5];
  a.cim = (const u16*)d_in[6];
  a.ldt = (const u16*)d_in[7];
  a.dsk = (const u16*)d_in[8];
  a.w1 = (const u16*)d_in[10];
  a.w2 = (const u16*)d_in[11];
  a.out = (float*)d_out;
  a.ws = (char*)d_ws;
  (void)hipFuncSetAttribute((const void*)s5_fused, hipFuncAttributeMaxDynamicSharedMemorySize, LDS_BYTES);
  s5_fused<<<256, 512, LDS_BYTES, stream>>>(a);
}
```

```cpp
#include <hip/hip_runtime.h>

#define STAMPJ(id)
namespace s5k {
typedef unsigned short u16;
typedef unsigned int u32;
typedef unsigned long long u64;
typedef short bf16x8 __attribute__((ext_vector_type(8)));
typedef short s16x2 __attribute__((ext_vector_type(2)));
typedef float f32x4 __attribute__((ext_vector_type(4)));
typedef float f32x2 __attribute__((ext_vector_type(2)));
typedef u32 u32x4 __attribute__((ext_vector_type(4)));
typedef u32 u32x2 __attribute__((ext_vector_type(2)));
typedef __bf16 bf16x2 __attribute__((ext_vector_type(2)));
#define LAS __attribute__((address_space(3)))
#define GAS __attribute__((address_space(1)))
typedef LAS char* lptr;
typedef __amdgpu_buffer_rsrc_t rsrc_t;

constexpr u32 WS_STATS = 4096;
constexpr u32 WS_AGG = 65536;
constexpr u32 WS_F1 = 262144, WS_F2 = WS_F1 + 32768, WS_F3 = WS_F2 + 32768;
constexpr u32 WS_PROF = 512 << 10;
constexpr u32 WS_H1 = 1u << 20;
constexpr u32 WS_HS = 9u << 20;
constexpr u32 WS_CT = 16u << 20;
constexpr u32 WS_WT = 18u << 20;
constexpr u32 WS_END = 21u << 20;
constexpr int CHUNK = 40960;
constexpr int LD_STG = 143360, LD_ST2 = LD_STG + 2048, LD_CS = LD_ST2 + 1024, LD_AGW = LD_CS + 1024, LD_CIN = LD_AGW + 1536,
              LD_CNT = LD_CIN + 1536, LD_TRB = LD_CNT + 256, LD_TAB = LD_TRB + 4096, LD_END = LD_TAB + 48 * 160;
constexpr int LDS_BYTES = 163840;
static_assert(LD_END <= LDS_BYTES, "lds");
enum { T_A = 0, T_C = 1, T_8 = 2, T_32 = 6, T_128 = 10, T_512 = 14 };
enum { C_F1 = 0, C_B2 = 1, C_S1 = 2, C_S2 = 3, C_F2 = 4, C_G = 5, C_G2 = 6, C_Y = 7 };
constexpr int SC1 = 16;
#define MPRIO 1
#define SPRIO 0
#define CPRE 2
#define MPRIO2 0
#define SPRIO2 0
#define CODE_BYTES 73728
#define CPF_PARTS 9
#define GMAP 2
constexpr u32 SPIN_CAP = 1u << 16;

struct Args {
  const u16 *x, *lre, *lim, *bre, *bim, *cre, *cim, *ldt, *dsk, *w1, *w2;
  float* out;
  char* ws;
};

__device__ __forceinline__ float u2f(u32 v) { return __builtin_bit_cast(float, v); }
__device__ __forceinline__ u32 f2u(float v) { return __builtin_bit_cast(u32, v); }
__device__ __forceinline__ float bf2f(u32 v) { return __builtin_bit_cast(float, v << 16); }
__device__ __forceinline__ float bfhi(u32 v) { return __builtin_bit_cast(float, v & 0xffff0000u); }
__device__ __forceinline__ u32 pk2(float a, float b) { return __builtin_bit_cast(u32, __builtin_convertvector(f32x2{a, b}, bf16x2)); }
__device__ __forceinline__ float rbf(float a) { return bf2f(pk2(a, a)); }
__device__ __forceinline__ f32x4 unpk4(u32x2 v) { return f32x4{bf2f(v.x), bfhi(v.x), bf2f(v.y), bfhi(v.y)}; }
__device__ __forceinline__ f32x4 rbf4(f32x4 v) { return unpk4(u32x2{pk2(v.x, v.y), pk2(v.z, v.w)}); }
__device__ __forceinline__ u32 relu2(u32 v) {
  return __builtin_bit_cast(u32, __builtin_elementwise_max(__builtin_bit_cast(s16x2, v), s16x2{0, 0}));
}
__device__ __forceinline__ f32x2 cmul(f32x2 a, f32x2 b) {
  float x, y;
  asm("v_mul_f32 %0, %2, %4\n\tv_mul_f32 %1, %2, %5\n\tv_fma_f32 %0, -%3, %5, %0\n\tv_fma_f32 %1, %3, %4, %1"
      : "=&v"(x), "=&v"(y) : "v"(a.x), "v"(a.y), "v"(b.x), "v"(b.y));
  return f32x2{x, y};
}
__device__ __forceinline__ f32x2 cfma(f32x2 a, f32x2 b, f32x2 c) {
  float x, y;
  asm("v_fma_f32 %0, %2, %4, %6\n\tv_fma_f32 %1, %2, %5, %7\n\tv_fma_f32 %0, -%3, %5, %0\n\tv_fma_f32 %1, %3, %4, %1"
      : "=&v"(x), "=&v"(y) : "v"(a.x), "v"(a.y), "v"(b.x), "v"(b.y), "v"(c.x), "v"(c.y));
  return f32x2{x, y};
}
__device__ __forceinline__ rsrc_t mk_rsrc(const void* p, u32 bytes) { return __builtin_amdgcn_make_buffer_rsrc((void*)p, 0, bytes, 0x00020000); }
__device__ __forceinline__ f32x4 mfma(bf16x8 a, bf16x8 b, f32x4 c) { return __builtin_amdgcn_mfma_f32_16x16x32_bf16(a, b, c, 0, 0, 0); }
__device__ __forceinline__ bf16x8 asfrag(u32x4 v) { return __builtin_bit_cast(bf16x8, v); }
__device__ __forceinline__ void dma16(rsrc_t rs, u32 dst, u32 voff, u32 soff) {
  asm volatile("s_mov_b32 m0, %0\n\ts_nop 0\n\tbuffer_load_dwordx4 %1, %2, %3 offen lds" ::"s"(dst), "v"(voff), "s"(rs), "s"(soff) : "memory");
}
__device__ __forceinline__ void dma16_sc1(rsrc_t rs, u32 dst, u32 voff, u32 soff) {
  asm volatile("s_mov_b32 m0, %0\n\ts_nop 0\n\tbuffer_load_dwordx4 %1, %2, %3 offen sc1 lds" ::"s"(dst), "v"(voff), "s"(rs), "s"(soff) : "memory");
}
__device__ __forceinline__ void dma4_sc1(rsrc_t rs, u32 dst, u32 voff, u32 soff) {
  asm volatile("s_mov_b32 m0, %0\n\ts_nop 0\n\tbuffer_load_dword %1, %2, %3 offen sc1 lds" ::"s"(dst), "v"(voff), "s"(rs), "s"(soff) : "memory");
}
__device__ __forceinline__ void dma4_plain(rsrc_t rs, u32 dst, u32 voff, u32 soff) {
  asm volatile("s_mov_b32 m0, %0\n\ts_nop 0\n\tbuffer_load_dword %1, %2, %3 offen lds" ::"s"(dst), "v"(voff), "s"(rs), "s"(soff) : "memory");
}
__device__ __forceinline__ lptr L(u32 addr) { return (lptr)(size_t)addr; }
__device__ __forceinline__ bf16x8 ld128(u32 p) { return *(LAS bf16x8*)L(p); }
__device__ __forceinline__ f32x2 ldc(u32 p) { return *(LAS f32x2*)L(p); }
__device__ __forceinline__ f32x4 ld4(u32 p) { return *(LAS f32x4*)L(p); }
__device__ __forceinline__ bf16x8 ldtr(u32 p0, u32 p1) {
  typedef short v4i16 __attribute__((ext_vector_type(4)));
  v4i16 a = __builtin_amdgcn_ds_read_tr16_b64_v4i16((LAS v4i16*)L(p0));
  v4i16 b = __builtin_amdgcn_ds_read_tr16_b64_v4i16((LAS v4i16*)L(p1));
  return __builtin_shufflevector(a, b, 0, 1, 2, 3, 4, 5, 6, 7);
}
__device__ __forceinline__ u32x4 gld(rsrc_t rs, u32 voff, u32 soff) { return __builtin_amdgcn_raw_buffer_load_b128(rs, voff, soff, SC1); }
__device__ __forceinline__ void gst(u32x4 v, rsrc_t rs, u32 voff) { __builtin_amdgcn_raw_buffer_store_b128(v, rs, voff, 0, SC1); }
template <int N> __device__ __forceinline__ void wait_vm() { asm volatile("s_waitcnt vmcnt(%0)" ::"i"(N) : "memory"); }
__device__ __forceinline__ void wait_lgkm0() { __builtin_amdgcn_s_waitcnt(0xc07f); asm volatile("" ::: "memory"); }
__device__ __forceinline__ void wg_barrier() {
  asm volatile("" ::: "memory");
  __builtin_amdgcn_s_barrier();
  asm volatile("" ::: "memory");
}
#define PIN(x) asm volatile("" : "+v"(x))
#define SCHED0() __builtin_amdgcn_sched_barrier(0)
__device__ __forceinline__ u32 cnt_add(int idx, int lane) {
  u32 old;
  const u32 addr = LD_CNT + idx * 4 + (lane == 0 ? 0 : 64), one = 1;
  asm volatile("s_waitcnt lgkmcnt(0)\n\ts_mov_b64 exec, 1\n\tds_add_rtn_u32 %0, %1, %2\n\ts_mov_b64 exec, -1\n\ts_waitcnt lgkmcnt(0)"
               : "=&v"(old) : "v"(addr), "v"(one) : "memory");
  return __builtin_amdgcn_readfirstlane(old);
}
__device__ __forceinline__ void cnt_wait(int idx, u32 want) {
  const u32 addr = LD_CNT + idx * 4;
#pragma unroll 1
  for (u32 s = 0; s < (SPIN_CAP << 4); s++) {
    u32 v;
    asm volatile("ds_read_b32 %0, %1\n\ts_waitcnt lgkmcnt(0)" : "=&v"(v) : "v"(addr) : "memory");
    if (__builtin_amdgcn_readfirstlane(v) >= want) break;
    __builtin_amdgcn_s_sleep(1);
  }
}
__device__ __forceinline__ void polln(rsrc_t wsr, u32 off, int mask, u32 ta, u32 tb, int lane) {
  const u32 vo = off + (lane & mask) * 128;
#pragma unroll 1
  for (u32 s = 0; s < SPIN_CAP; s++) {
    u32x2 v = __builtin_amdgcn_raw_buffer_load_b64(wsr, vo, 0, SC1);
    if (__all((v.x == ta) & (v.y == tb))) break;
    __builtin_amdgcn_s_sleep(2);
  }
  __builtin_amdgcn_fence(__ATOMIC_ACQUIRE, "wavefront");
  asm volatile("" ::: "memory");
}
__device__ __forceinline__ void put_flag(rsrc_t wsr, u32 off, u32 ta, u32 tb, int lane) {
  __builtin_amdgcn_raw_buffer_store_b64(u32x2{ta, tb}, wsr, lane == 0 ? off : WS_END, 0, SC1);
}
template <int CTRL> __device__ __forceinline__ float dpp(float x) {
  return __builtin_bit_cast(float, __builtin_amdgcn_mov_dpp(__builtin_bit_cast(int, x), CTRL, 0xf, 0xf, true));
}
__device__ __forceinline__ void rows4(float x, float (&r)[4]) {
  auto s = __builtin_amdgcn_permlane16_swap(f2u(x), f2u(x), false, false);
  const u32 e = s[0], o = s[1];
  auto te = __builtin_amdgcn_permlane32_swap(e, e, false, false);
  auto to = __builtin_amdgcn_permlane32_swap(o, o, false, false);
  const u32 r0 = te[0], r2 = te[1], r1 = to[0], r3 = to[1];
  r[0] = u2f(r0); r[1] = u2f(r1); r[2] = u2f(r2); r[3] = u2f(r3);
}
__device__ __forceinline__ float rows_sum(float x) {
  auto s = __builtin_amdgcn_permlane16_swap(f2u(x), f2u(x), false, false);
  const u32 a = s[0], b = s[1];
  x = u2f(a) + u2f(b);
  auto t = __builtin_amdgcn_permlane32_swap(f2u(x), f2u(x), false, false);
  const u32 c = t[0], d = t[1];
  return u2f(c) + u2f(d);
}
__device__ __forceinline__ float wave_sum(float v) {
  v += dpp<0xB1>(v); v += dpp<0x4E>(v); v += dpp<0x124>(v); v += dpp<0x128>(v);
  return rows_sum(v);
}
__device__ __forceinline__ float fexp(float x) {
  const float hi = x * 1.4426950408889634f;
  const float lo = __builtin_fmaf(x, 1.4426950408889634f, -hi) + x * 1.925963033500011e-8f;
  return __builtin_amdgcn_exp2f(hi) * (1.0f + lo * 0.6931471805599453f);
}
__device__ __forceinline__ void fsincos(float x, float& sn, float& cs) {
  if (__builtin_expect(__any(!(__builtin_fabsf(x) < 2000.f)), 0)) { sincosf(x, &sn, &cs); return; }
  const float n = __builtin_rintf(x * 0.6366197723675814f);
  float r = __builtin_fmaf(n, -1.5703125f, x);
  r = __builtin_fmaf(n, -4.837512969970703125e-4f, r);
  r = __builtin_fmaf(n, -7.54978995489188e-8f, r);
  const float r2 = r * r;
  const float s = __builtin_fmaf(r * r2, __builtin_fmaf(r2, __builtin_fmaf(r2, -1.9515295891e-4f, 8.3321608736e-3f), -1.6666654611e-1f), r);
  const float c = __builtin_fmaf(r2 * r2, __builtin_fmaf(r2, __builtin_fmaf(r2, 2.443315711809948e-5f, -1.388731625493765e-3f), 4.166664568298827e-2f),
                                 __builtin_fmaf(r2, -0.5f, 1.0f));
  const int k = (int)n;
  const float s1 = (k & 1) ? c : s, c1 = (k & 1) ? s : c;
  sn = (k & 2) ? -s1 : s1;
  cs = ((k + 1) & 2) ? -c1 : c1;
}
#define STAMP(id) asm volatile("; MARK " #id)
#define STAMPI(id)

template <bool IS_M, int NP>
__device__ __forceinline__ void phase1(const Args& a, int c, int j, int wave, int lane, u32& ta, u32& tb) {
  constexpr int NU = 2 * NP;
  constexpr bool TYPE_B = IS_M ? (NP == 4) : (NP == 2);
  constexpr int nS = TYPE_B ? 2 : 3, nM = 6 - nS, MP = nM * 64;
  const int wm = wave & 3;
  const int i2 = j >> 1;
  const int s0 = 5 * i2 + (TYPE_B ? 3 : 0);
  const int m0 = 7 * i2 + ((i2 & 1) ? (TYPE_B ? 3 : 0) : (TYPE_B ? 0 : 4));
  const int li = lane & 15, q = lane >> 4, r8 = lane >> 3, sl = lane & 7;
  const rsrc_t wsr = mk_rsrc(a.ws, WS_END);

  u32x2 ep;
  u32x4 xr0, xr1;
  u32 plr, pli, pdt;
  {
    const GAS char* xrow = (const GAS char*)a.x + (size_t)(c * 128 + j * 8 + wave) * 1280;
    asm volatile("global_load_dwordx2 %0, %1, %2 sc1" : "=v"(ep) : "v"(0), "s"(a.ws) : "memory");
    asm volatile("global_load_dwordx4 %0, %1, %2" : "=v"(xr0) : "v"(lane * 16), "s"(xrow) : "memory");
    asm volatile("global_load_dwordx4 %0, %1, %2 offset:1024" : "=v"(xr1) : "v"(li * 16), "s"(xrow) : "memory");
    if (!IS_M) {
      const int ch = (s0 * 16 + (q < NP ? lane : li)) * 2;
      asm volatile("global_load_ushort %0, %1, %2" : "=v"(plr) : "v"(ch), "s"(a.lre) : "memory");
      asm volatile("global_load_ushort %0, %1, %2" : "=v"(pli) : "v"(ch), "s"(a.lim) : "memory");
      asm volatile("global_load_ushort %0, %1, %2" : "=v"(pdt) : "v"(ch), "s"(a.ldt) : "memory");
    }
  }
  rsrc_t drs[5];
  u32 dvo[5], dso[5], dstep[5], ddst[5];
  {
    const rsrc_t brs = mk_rsrc(wm < 2 ? a.bre : a.bim, 640 * 1280);
    const u32 bvo = ((wm & 1) * 8 + r8) * 1280 + ((sl ^ ((wm & 1) * 4 + (r8 >> 1))) << 4);
#pragma unroll
    for (int b = 0; b < 5; b++) {
      if (!IS_M && b < 4) {
        drs[b] = mk_rsrc(a.x, 2048 * 1280);
        dvo[b] = (8 * (r8 >> 2) + (r8 & 3)) * 1280 + ((sl ^ ((b & 1) * 4 + (r8 >> 1))) << 4);
        dso[b] = (c * 128 + wm * 32 + (b & 1) * 16 + (b >> 1) * 4) * 1280;
        dstep[b] = 128;
        ddst[b] = (wm * 4 + b) * 1024;
      } else if (!IS_M || b < nS - 1) {
        const int p = IS_M ? b + 1 : 0;
        drs[b] = brs;
        dvo[b] = bvo;
        dso[b] = (s0 + p) * 16 * 1280;
        dstep[b] = 128;
        ddst[b] = 16384 + p * 4096 + wm * 1024;
      } else {
        const int t = b - (nS - 1);
        const int idx = t * 64 + lane;
        const int kr = idx / (4 * nM), cpos = idx - kr * (4 * nM);
        const int sw = TYPE_B ? ((kr & 3) | (((kr >> 3) & 1) << 2)) : ((kr >> 3) & 1);
        int col16 = m0 * 4 + (((cpos >> 1) ^ sw) << 1) + (cpos & 1);
        if (TYPE_B && col16 >= 216) col16 -= 8;
        drs[b] = mk_rsrc(a.w1, 640 * 3456);
        dvo[b] = kr * 3456 + col16 * 16;
        dso[b] = wm * 16 * 3456;
        dstep[b] = 64 * 3456;
        ddst[b] = 16384 + nS * 4096 + (wm * nM + t) * 1024;
      }
    }
  }
  int kc = c + j;
  kc -= kc >= 20 ? 20 : kc >= 10 ? 10 : 0;
  kc = kc == 10 ? 0 : kc;
  auto dma1 = [&](int bufoff, int b) { dma16_sc1(drs[b], bufoff + ddst[b], dvo[b], dso[b] + kc * dstep[b]); };
  auto issue = [&](int bufoff) {
#pragma unroll
    for (int b = 0; b < 5; b++) dma1(bufoff, b);
    kc = kc == 9 ? 0 : kc + 1;
  };
  issue(0);
  *(LAS u32*)L(LD_CNT + li * 4) = 0;
  const int rid = c * 2 + (j & 1);
  STAMP(1);

  int pr[NP];
#pragma unroll
  for (int p = 0; p < NP; p++) pr[p] = (p + wm) % NP;
  u32 xb[2][2], wb[2][NU];
  {
    const u32 xo = li * 128 + ((q ^ (li >> 1)) << 4);
    const int qq = li >> 2;
    const u32 rowb = 16384 + nS * 4096 + (8 * q + qq) * MP + (lane & 3) * 8;
    const int f = TYPE_B ? (qq | ((q & 1) << 2)) : (q & 1);
#pragma unroll
    for (int h = 0; h < 2; h++) {
#pragma unroll
      for (int kk = 0; kk < 2; kk++) {
        xb[h][kk] = h * 2 * CHUNK + wm * 4096 + (xo ^ (kk << 6));
        PIN(xb[h][kk]);
      }
#pragma unroll
      for (int u = 0; u < NU; u++) {
        wb[h][u] = h * 2 * CHUNK + (IS_M ? rowb + (((2 * pr[u >> 1] + (u & 1)) ^ f) << 5) : 16384 + pr[u >> 1] * 4096 + (xo ^ ((u & 1) << 6)));
        PIN(wb[h][u]);
      }
    }
  }
  bf16x8 xf[2][2], wf[2][NU];
  auto read1 = [&](int set, int bufoff, int kk, int f) {
    const int h = bufoff == 2 * CHUNK, bo = h ? 0 : bufoff;
    if (f < 2) {
      xf[set][f] = ld128(xb[h][kk] + (bo + f * 2048));
    } else if (f < NU + 2) {
      const int u = f - 2;
      if (!IS_M) wf[set][u] = ld128(wb[h][(u & ~1) + kk] + (bo + (u & 1) * 2048));
      else wf[set][u] = ldtr(wb[h][u] + (bo + kk * 32 * MP), wb[h][u] + (bo + kk * 32 * MP + 4 * MP));
    }
  };
  f32x4 acc[2][NU], csum[2];
#pragma unroll
  for (int m = 0; m < 2; m++) {
#pragma unroll
    for (int u = 0; u < NU; u++) { acc[m][u] = f32x4{0.f, 0.f, 0.f, 0.f}; PIN(acc[m][u]); }
    csum[m] = f32x4{0.f, 0.f, 0.f, 0.f};
    PIN(csum[m]);
  }
  u32x4 ones4 = {0x3f803f80u, 0x3f803f80u, 0x3f803f80u, 0x3f803f80u};
  PIN(ones4);
  constexpr int G = NU + 1;
  constexpr int NDB = IS_M ? 2 : 3;
  auto dpos = [](int d) { return (2 * G * d + (IS_M ? G : 0)) / 5; };
  static_assert((2 * G * (NDB - 1) + (IS_M ? G : 0)) / 5 < G && (2 * G * NDB + (IS_M ? G : 0)) / 5 >= G, "split");
  auto step = [&](int set, int rbuf, int rkk, int dbuf) {
#pragma unroll
    for (int g = 0; g < G; g++) {
      if (g > 0) {
        const int u = g - 1;
#pragma unroll
        for (int m = 0; m < 2; m++)
          acc[m][u] = IS_M ? mfma(wf[set][u], xf[set][m], acc[m][u]) : mfma(xf[set][m], wf[set][u], acc[m][u]);
      } else {
        csum[0] = mfma(asfrag(ones4), wf[set][0], csum[0]);
        csum[1] = mfma(asfrag(ones4), wf[set][1], csum[1]);
      }
      if (rbuf >= 0) {
        if (g == 0) { read1(set ^ 1, rbuf, rkk, 2); read1(set ^ 1, rbuf, rkk, 3); }
        else if (g == 1) { read1(set ^ 1, rbuf, rkk, 0); read1(set ^ 1, rbuf, rkk, 1); }
        else read1(set ^ 1, rbuf, rkk, g + 2);
      } else if (g == 3) {
        LAS float* csp = (LAS float*)L(LD_CS + (IS_M ? 512 : 0) + (wm < NP ? wm : 3) * 128 + (lane & 31) * 4);
        float e0 = csum[0].x, e1 = csum[1].x;
        PIN(e0); PIN(e1);
        if (wm < NP) *csp = (q & 1) ? e1 : e0;
      }
#pragma unroll
      for (int d = 0; d < 5; d++)
        if ((set == 1 ? g : g + G) == dpos(d)) dma1(dbuf, d);
      SCHED0();
    }
  };

  asm volatile("s_waitcnt vmcnt(5)" : "+v"(ep), "+v"(xr0), "+v"(xr1)::"memory");
  if (!IS_M) asm volatile("" : "+v"(plr), "+v"(pli), "+v"(pdt));
  {
    u64 z = ((u64)__builtin_amdgcn_readfirstlane(ep.y) << 32 | __builtin_amdgcn_readfirstlane(ep.x)) + 0x9E3779B97F4A7C15ull;
    z = (z ^ (z >> 30)) * 0xBF58476D1CE4E5B9ull;
    z = (z ^ (z >> 27)) * 0x94D049BB133111EBull;
    z ^= z >> 31;
    ta = (u32)z;
    tb = (u32)(z >> 32);
    float v[16];
#pragma unroll
    for (int e = 0; e < 4; e++) {
      v[2 * e] = bf2f(xr0[e]); v[2 * e + 1] = bfhi(xr0[e]);
      v[8 + 2 * e] = bf2f(xr1[e]); v[8 + 2 * e + 1] = bfhi(xr1[e]);
    }
    float s0_ = 0.f, s1_ = 0.f;
#pragma unroll
    for (int e = 0; e < 8; e++) { s0_ += v[e]; s1_ += v[8 + e]; }
    const float mean = wave_sum(lane < 16 ? s0_ + s1_ : s0_) * (1.0f / 640.0f);
    s0_ = 0.f; s1_ = 0.f;
#pragma unroll
    for (int e = 0; e < 8; e++) {
      const float d0 = v[e] - mean, d1 = v[8 + e] - mean;
      s0_ += d0 * d0; s1_ += d1 * d1;
    }
    const float var = wave_sum(lane < 16 ? s0_ + s1_ : s0_) * (1.0f / 640.0f);
    const float mb = rbf(mean), den = rbf(__builtin_amdgcn_sqrtf(rbf(rbf(var) + 1e-5f)));
    gst(u32x4{f2u(mb), ta, f2u(__builtin_amdgcn_rcpf(den)), tb}, wsr, lane == 0 ? WS_STATS + (c * 128 + j * 8 + wave) * 16 : WS_END);
  }
  if (!IS_M) {
    if (wm == 0) {
      const float lr = bf2f(plr), lim = bf2f(pli);
      const float dt = rbf(fexp(bf2f(pdt)));
      const float er = fexp(dt * lr);
      float sn, cn;
      fsincos(dt * lim, sn, cn);
      const f32x2 a1 = {er * cn, er * sn};
      const float nr = a1.x - 1.0f, dn = 1.0f / (lr * lr + lim * lim);
      const u32 tp = LD_TAB + (lane < 48 ? lane : 47) * 160;
      if (q < NP) {
        *(LAS f32x4*)L(tp) = f32x4{a1.x, a1.y, (nr * lr + a1.y * lim) * dn, (a1.y * lr - nr * lim) * dn};
        const f32x2 a2 = cmul(a1, a1), a4 = cmul(a2, a2);
        f32x2 b = cmul(a4, a4);
#pragma unroll
        for (int i = 0; i < 4; i++) {
          const f32x2 b2 = cmul(b, b), b3 = cmul(b2, b);
          *(LAS f32x4*)L(tp + 16 + i * 32) = f32x4{1.f, 0.f, b.x, b.y};
          *(LAS f32x4*)L(tp + 32 + i * 32) = f32x4{b2.x, b2.y, b3.x, b3.y};
          b = cmul(b2, b2);
        }
      }
    }
  }
  issue(CHUNK);
#pragma unroll
  for (int d = 0; d < NDB; d++) dma1(2 * CHUNK, d);
  u32x4 rt[2];
  const int rks = min(rid * 2 + (wm & 1), 53);
  if (!IS_M) {
#pragma unroll
    for (int i = 0; i < 2; i++) {
      const int f = min(rid * 8 + wm * 2 + i, 199);
      const int wk = f / 5, n = f - wk * 5, w = wk / 10, k = wk - w * 10;
      const GAS char* src = (const GAS char*)(k < 5 ? a.cre : a.cim) + (i2 * 80 + n * 16) * 1280 + (160 * w + 32 * (k < 5 ? k : k - 5)) * 2;
      asm volatile("global_load_dwordx4 %0, %1, %2" : "=v"(rt[i]) : "v"(li * 1280 + q * 16), "s"(src) : "memory");
    }
    wait_vm<6 + NDB + 2>();
  } else {
    wait_vm<6 + NDB>();
  }
  wait_lgkm0();
  wg_barrier();
  STAMP(2);
  __builtin_amdgcn_s_setprio(IS_M ? MPRIO : SPRIO);
#pragma unroll
  for (int f = 0; f < NU + 2; f++) read1(0, 0, 0, f);
  int it = 0;
  auto body = [&](int prv, int cur, int nxt) {
    step(0, cur, 1, prv);
    kc = kc == 9 ? 0 : kc + 1;
    if (nxt < 0) {
      const int r = wave * 16 + li;
      u32x4 g = *(LAS u32x4*)L(LD_STG + r * 16);
      if (!__all((g.y == ta) & (g.w == tb))) {
#pragma unroll 1
        for (u32 s = 0; s < SPIN_CAP; s++) {
          g = gld(wsr, WS_STATS + (c * 128 + r) * 16, 0);
          if (__all((g.y == ta) & (g.w == tb))) break;
        }
      }
      const int t5 = r & 31;
      *(LAS f32x2*)L(LD_ST2 + ((r & ~31) + (((t5 >> 2) & 1) << 4) + ((t5 >> 3) << 2) + (t5 & 3)) * 8) = f32x2{u2f(g.x), u2f(g.z)};
    } else if (!IS_M && it == 0) wait_vm<7>();
    else if (IS_M && it == 1 && wm < 2) wait_vm<10>();
    else wait_vm<5>();
    wait_lgkm0();
    wg_barrier();
    STAMPI(3 + it);
    if (IS_M && it == 0 && wm < 2) {
      const rsrc_t w2r = mk_rsrc(a.w2, 1728 * 1280);
#pragma unroll
      for (int b = 0; b < 5; b++) {
        const int idx = b * 64 + lane, kr = idx / 10, cpos = idx - kr * 10;
        dma16(w2r, 3 * CHUNK + wm * 5120 + b * 1024, kr * 1280 + i2 * 160 + cpos * 16, rks * (32 * 1280));
      }
    }
    if (it == 2) {
      if (!IS_M) {
        asm volatile("" : "+v"(rt[0]), "+v"(rt[1]));
#pragma unroll
        for (int i = 0; i < 2; i++) gst(rt[i], wsr, WS_CT + (i2 * 200 + min(rid * 8 + wm * 2 + i, 199)) * 1024 + lane * 16);
      } else if (wm < 2) {
        const u32 p = 3 * CHUNK + wm * 5120 + (4 * q + (li >> 2)) * 160 + (lane & 3) * 8;
#pragma unroll
        for (int n = 0; n < 5; n++)
          gst(__builtin_bit_cast(u32x4, ldtr(p + n * 32, p + n * 32 + 2560)), wsr, WS_WT + ((i2 * 54 + rks) * 5 + n) * 1024 + lane * 16);
      }
    }
    if (IS_M && it == 3 && wm == 3) put_flag(wsr, WS_F3 + (i2 * 32 + rid) * 128, ta, tb, lane);
    if (it >= 7) {
      if (it == 7) {
        const u32 f = *(LAS u32*)L(LD_TRB + wave * 256 + lane * 4);
        if (!__all(f == ((lane & 1) ? tb : ta))) polln(wsr, WS_F3 + i2 * 32 * 128, 31, ta, tb, lane);
#pragma unroll
        for (int b = 0; b < 5; b++) {
          drs[b] = wsr; dvo[b] = lane * 16; dso[b] = WS_WT + i2 * 54 * 5120 + b * 1024; dstep[b] = 5120; ddst[b] = wave * 5120 + b * 1024;
        }
      }
      kc = wm * 14 + (cur / CHUNK) * 2 + (IS_M ? 1 : 0);
    }
    SCHED0();
    step(1, nxt, 0, cur);
    if (it == 5) {
      dma4_sc1(wsr, LD_TRB + wave * 256, (lane >> 1) * 128 + (lane & 1) * 4, WS_F3 + i2 * 32 * 128);
      dma4_sc1(wsr, LD_STG + wave * 256, lane * 4, WS_STATS + c * 2048 + wave * 256);
    }
    it++;
  };
#pragma unroll 1
  for (int r = 0; r < 3; r++) {
    body(2 * CHUNK, 0, CHUNK);
    body(0, CHUNK, 2 * CHUNK);
    body(CHUNK, 2 * CHUNK, 0);
  }
  body(2 * CHUNK, 0, -1);
#pragma unroll
  for (int d = NDB; d < 5; d++) dma1(0, d);
  __builtin_amdgcn_s_setprio(IS_M ? MPRIO2 : SPRIO2);
  STAMP(3);
  float mb[2];
  f32x4 st[4];
  f32x2 ca[NP], cc[NP], a8r[NP];
  u32 tp[NP];
  if (IS_M) {
#pragma unroll
    for (int m = 0; m < 2; m++) mb[m] = *(LAS float*)L(LD_ST2 + (wm * 32 + m * 16 + li) * 8);
  } else {
#pragma unroll
    for (int i = 0; i < 4; i++) st[i] = ld4(LD_ST2 + (wm * 32 + (i >> 1) * 16 + 4 * q + 2 * (i & 1)) * 8);
#pragma unroll
    for (int p = 0; p < NP; p++) {
      tp[p] = LD_TAB + (pr[p] * 16 + li) * 160;
      const f32x4 t0 = ld4(tp[p]);
      ca[p] = f32x2{t0.x, t0.y};
      cc[p] = f32x2{t0.z, t0.w};
      a8r[p] = ldc(tp[p] + (T_8 + 3 - q) * 8);
    }
  }
  wait_lgkm0();
  wg_barrier();
  STAMP(4);

  if (IS_M) {
#pragma unroll
    for (int p = 0; p < NP; p++) {
      const f32x4 c0 = ld4(LD_CS + 512 + pr[p] * 128 + q * 16), c1 = ld4(LD_CS + 512 + pr[p] * 128 + 64 + q * 16);
      const u32 off = (m0 + pr[p] < 54) ? WS_H1 + ((c * 8 + 2 * wm) * 54 + m0 + pr[p]) * 1024 : WS_END;
#pragma unroll
      for (int m = 0; m < 2; m++) {
        const f32x4 v0 = acc[m][2 * p] - mb[m] * c0, v1 = acc[m][2 * p + 1] - mb[m] * c1;
        gst(u32x4{relu2(pk2(v0.x, v0.y)), relu2(pk2(v0.z, v0.w)), relu2(pk2(v1.x, v1.y)), relu2(pk2(v1.z, v1.w))}, wsr,
            off + m * 54 * 1024 + lane * 16);
      }
    }
  } else {
    f32x2 u[NP][8];
    f32x2 seg[NP];
    float Sr[NP], Si[NP];
#pragma unroll
    for (int p = 0; p < NP; p++) {
      Sr[p] = *(LAS float*)L(LD_CS + pr[p] * 128 + li * 4);
      Si[p] = *(LAS float*)L(LD_CS + pr[p] * 128 + 64 + li * 4);
    }
#pragma unroll
    for (int p = 0; p < NP; p++) {
      f32x2 h;
#pragma unroll
      for (int t = 0; t < 8; t++) {
        const float mu = st[t >> 1][(t & 1) * 2], rs = st[t >> 1][(t & 1) * 2 + 1];
        u[p][t] = f32x2{(acc[t >> 2][2 * p][t & 3] - mu * Sr[p]) * rs, (acc[t >> 2][2 * p + 1][t & 3] - mu * Si[p]) * rs};
        h = t ? cfma(ca[p], h, u[p][t]) : u[p][0];
      }
      seg[p] = h;
      h = cmul(a8r[p], h);
      *(LAS f32x2*)L(LD_AGW + ((wm * 3 + pr[p]) * 16 + li) * 8) = f32x2{rows_sum(h.x), rows_sum(h.y)};
    }
    if (cnt_add(C_S1, lane) == 3) {
      const int ch = lane < 48 ? lane : 47;
      const f32x2 a32 = ldc(LD_TAB + ch * 160 + (T_32 + 1) * 8);
      f32x2 h = ldc(LD_AGW + ch * 8);
#pragma unroll
      for (int w = 1; w < 4; w++) h = cfma(a32, h, ldc(LD_AGW + (w * 48 + ch) * 8));
      gst(u32x4{f2u(h.x), ta, f2u(h.y), tb}, wsr, q < NP ? WS_AGG + (c * 640 + s0 * 16 + lane) * 16 : WS_END);
    }
    STAMP(5);
    const int cp = 4 * wm + q, e = c - 1 - cp;
    const u32 go = WS_AGG + (cp * 640 + s0 * 16 + li) * 16;
    u32x4 lg[NP];
#pragma unroll
    for (int p = 0; p < NP; p++) lg[p] = gld(wsr, go, pr[p] * 256);
    SCHED0();
    cnt_wait(C_S1, 4);
    f32x2 aq[NP], pre[NP];
    u32 hso[NP];
#pragma unroll
    for (int p = 0; p < NP; p++) {
      const f32x2 a32 = ldc(tp[p] + (T_32 + 1) * 8), a8q = ldc(tp[p] + (T_8 + q) * 8), aw = ldc(tp[p] + (T_32 + wm) * 8);
      f32x2 h = {0.f, 0.f};
#pragma unroll
      for (int w = 0; w < 3; w++) {
        const f32x2 g = ldc(LD_AGW + ((w * 3 + pr[p]) * 16 + li) * 8);
        if (w < wm) h = cfma(a32, h, g);
      }
#pragma unroll
      for (int t = 0; t < 8; t++) u[p][t] = cmul(cc[p], u[p][t]);
      {
        const f32x2 a8 = ldc(tp[p] + (T_8 + 1) * 8);
        float gr[4], gi[4];
        rows4(seg[p].x, gr); rows4(seg[p].y, gi);
#pragma unroll
        for (int w = 0; w < 3; w++) {
          const f32x2 t = cfma(a8, h, f32x2{gr[w], gi[w]});
          h.x = w < q ? t.x : h.x;
          h.y = w < q ? t.y : h.y;
          PIN(h);
        }
      }
      pre[p] = cmul(cc[p], h);
      aq[p] = cmul(cc[p], cmul(a8q, aw));
      const int cu = s0 + pr[p];
      hso[p] = WS_HS + ((c * 8 + 2 * wm) * 40 + (cu / 10) * 10 + ((cu % 10) >> 1) + (lane >> 5) * 5) * 1024 + (cu & 1) * 512 + (lane & 31) * 16;
      asm volatile("" : "+v"(pre[p]), "+v"(aq[p]), "+v"(hso[p]), "+v"(u[p][0]), "+v"(u[p][1]), "+v"(u[p][2]), "+v"(u[p][3]), "+v"(u[p][4]),
                   "+v"(u[p][5]), "+v"(u[p][6]), "+v"(u[p][7]));
    }
    u32 tw = LD_TRB + wm * 1024 + (li >> 3) * 256 + q * 64 + (li & 7) * 2, tr = LD_TRB + wm * 1024 + lane * 16;
    PIN(tw); PIN(tr);
    {
      f32x2 pw[NP];
#pragma unroll
      for (int p = 0; p < NP; p++) {
        pw[p] = cmul(ldc(tp[p] + (T_128 + (e & 3)) * 8), ldc(tp[p] + (T_512 + ((e >> 2) & 3)) * 8));
        PIN(pw[p]);
      }
#pragma unroll 1
      for (u32 s = 0; s < SPIN_CAP; s++) {
        bool ok = true;
#pragma unroll
        for (int p = 0; p < NP; p++) ok = ok & (lg[p].y == ta) & (lg[p].w == tb);
        if (__all(ok | (e < 0))) break;
#pragma unroll
        for (int p = 0; p < NP; p++) lg[p] = gld(wsr, go, pr[p] * 256);
      }
#pragma unroll
      for (int p = 0; p < NP; p++) {
        f32x2 h = cmul(pw[p], f32x2{u2f(lg[p].x), u2f(lg[p].z)});
        h.x = e < 0 ? 0.f : h.x;
        h.y = e < 0 ? 0.f : h.y;
        *(LAS f32x2*)L(LD_CIN + ((wm * 3 + pr[p]) * 16 + li) * 8) = f32x2{rows_sum(h.x), rows_sum(h.y)};
      }
      cnt_add(C_S2, lane);
      cnt_add(C_B2, lane);
    }
    cnt_wait(C_S2, 4);
    STAMP(6);
#pragma unroll
    for (int p = 0; p < NP; p++) {
      const u32 ci = LD_CIN + (pr[p] * 16 + li) * 8;
      f32x2 h = (ldc(ci) + ldc(ci + 384)) + (ldc(ci + 768) + ldc(ci + 1152));
      h = cfma(aq[p], h, pre[p]);
#pragma unroll
      for (int m = 0; m < 2; m++) {
#pragma unroll
        for (int r = 0; r < 4; r++) {
          h = cfma(ca[p], h, u[p][4 * m + r]);
          const u32 pk = pk2(h.x, -h.y);
          *(LAS u16*)L(tw + r * 16) = (u16)pk;
          *(LAS u16*)L(tw + r * 16 + 512) = (u16)(pk >> 16);
        }
        asm volatile("" ::: "memory");
        gst(*(LAS u32x4*)L(tr), wsr, hso[p] + m * 40 * 1024);
        asm volatile("" ::: "memory");
      }
    }
    if (c == 15 && j == 0 && wm == 0) {
      const u64 e = ((u64)ep.y << 32 | ep.x) + 1;
      __builtin_amdgcn_raw_buffer_store_b64(u32x2{(u32)e, (u32)(e >> 32)}, wsr, lane == 0 ? 0 : WS_END, 0, SC1);
    }
  }
}

__device__ __forceinline__ void phase2_m(const Args& a, int c, int j, int wave, int lane, u32 ta, u32 tb) {
  const int wm = wave & 3, li = lane & 15, q = lane >> 4;
  const rsrc_t wsr = mk_rsrc(a.ws, WS_END);
  const int rh = j & 1, i2 = j >> 1, dcol0 = i2 * 80;
  const u32 wo = WS_WT + (i2 * 54 + wm * 14) * 5120;
  u32x4 w[3][5];
  auto ldw = [&](int r, int k) {
#pragma unroll
    for (int n = 0; n < 5; n++) w[r][n] = gld(wsr, lane * 16, wo + (((k < 12 || wm < 3) ? k : 0) * 5 + n) * 1024);
  };
#pragma unroll
  for (int b = 0; b < 5; b++) dma16_sc1(wsr, (24 + wm) * 5120 + b * 1024, lane * 16, wo + (30 + b) * 1024);
  wait_vm<5>();
  if (cnt_add(C_F1, lane) == 3) put_flag(wsr, WS_F1 + (c * 16 + j) * 128, ta, tb, lane);
  STAMP(5);
#pragma unroll
  for (int k = 0; k < 3; k++) ldw(k, 7 + k);
  SCHED0();
  polln(wsr, WS_F1 + (c * 16 + 4 * wm) * 128, 3, ta, tb, lane);
  cnt_add(C_B2, lane);
  STAMP(7);
  f32x4 g[4][5];
  {
    u32 wbase[3];
#pragma unroll
    for (int h = 0; h < 3; h++) {
      wbase[h] = wm * 5120 + h * 61440 + lane * 16;
      PIN(wbase[h]);
    }
    auto rd = [&](int r, int slot) {
#pragma unroll
      for (int n = 0; n < 5; n++) w[r][n] = __builtin_bit_cast(u32x4, ld128(wbase[slot / 3] + ((slot % 3) * 20480 + n * 1024)));
    };
    constexpr int KO[14] = {7, 8, 9, 3, 4, 10, 5, 6, 11, 0, 1, 2, 12, 13};
    constexpr int RS[14] = {0, 1, 2, 1, 2, 0, 1, 2, 0, 1, 2, 1, 0, 2};
    u32x4 hf[4][4];
    const u32 hb = WS_H1 + ((c * 8 + 4 * rh) * 54 + wm * 14) * 1024 + lane * 16;
    auto lda = [&](int s) {
#pragma unroll
      for (int m = 0; m < 4; m++) hf[s & 3][m] = gld(wsr, hb, (((m + wm) & 3) * 54 + ((s < 12 || wm < 3) ? KO[s] : 0)) * 1024);
    };
#pragma unroll
    for (int s = 0; s < 4; s++) lda(s);
    SCHED0();
#pragma unroll
    for (int m = 0; m < 4; m++)
#pragma unroll
      for (int n = 0; n < 5; n++) g[m][n] = f32x4{0.f, 0.f, 0.f, 0.f};
    STAMPJ(1);
#pragma unroll
    for (int s = 0; s < 14; s++) {
      if (s < 12 || wm < 3) {
        if (s == 2) cnt_wait(C_B2, 8);
        if (s + 1 < 14 && KO[s + 1] < 7) rd(RS[s + 1], KO[s + 1]);
#pragma unroll
        for (int n = 0; n < 5; n++)
#pragma unroll
          for (int m = 0; m < 4; m++) g[m][n] = mfma(asfrag(w[RS[s]][n]), asfrag(hf[s & 3][m]), g[m][n]);
        SCHED0();
        if (s < 11) STAMPJ(2 + s);
        if (s + 4 < 14) lda(s + 4);
        if (s == 0) ldw(0, 10);
        if (s == 5) ldw(0, 11);
        if (s == 8) ldw(0, 12);
        if (s == 10) ldw(2, 13);
        if (s == 7) cnt_add(C_G2, lane);
        SCHED0();
      }
    }
  }
  STAMP(8);
  const int tt = 4 * rh + wm;
  const int tok = c * 128 + (tt >> 1) * 32 + 8 * (li >> 2) + 4 * (tt & 1) + (li & 3);
  u32x2 xv[5], dv[5];
#pragma unroll
  for (int n = 0; n < 5; n++) {
    xv[n] = *(const GAS u32x2*)((const GAS u16*)a.x + tok * 640 + dcol0 + 16 * n + 4 * q);
    dv[n] = *(const GAS u32x2*)((const GAS u16*)a.dsk + dcol0 + 16 * n + 4 * q);
  }
#pragma unroll
  for (int m = 1; m < 4; m++)
#pragma unroll
    for (int n = 0; n < 5; n++) *(LAS f32x4*)L(((m - 1) * 4 + wm) * 5120 + n * 1024 + lane * 16) = g[m][n];
  cnt_add(C_G, lane);
  f32x4 skip[5], xs[5];
  const f32x2 st = ldc(LD_ST2 + (tt * 16 + li) * 8);
#pragma unroll
  for (int n = 0; n < 5; n++) {
    xs[n] = unpk4(xv[n]);
    skip[n] = rbf4(rbf4(rbf4(xs[n] - st.x) * st.y) * unpk4(dv[n]));
    PIN(skip[n]);
  }
  cnt_wait(C_G, 4);
  STAMP(9);
#pragma unroll
  for (int n = 0; n < 5; n++) {
    const u32 gp = n * 1024 + lane * 16;
    const f32x4 sum = (g[0][n] + ld4(gp + ((wm + 3) & 3) * 5120)) + (ld4(gp + (4 + ((wm + 2) & 3)) * 5120) + ld4(gp + (8 + ((wm + 1) & 3)) * 5120));
    const f32x4 gb = rbf4(sum * st.y);
    f32x4 sg;
#pragma unroll
    for (int e2 = 0; e2 < 4; e2++) sg[e2] = gb[e2] * __builtin_amdgcn_rcpf(1.0f + __builtin_amdgcn_exp2f(gb[e2] * -1.4426950408889634f));
    g[0][n] = rbf4(sg);
    PIN(g[0][n]);
  }
  STAMP(11);
  cnt_wait(C_Y, 4);
  STAMP(12);
  const rsrc_t outr = mk_rsrc(a.out, 2048 * 640 * 4);
#pragma unroll
  for (int n = 0; n < 5; n++) {
    const u32 yp = (3 + wm) * 20480 + n * 1024 + lane * 16;
    const f32x4 y = (ld4(yp) + ld4(yp + 5120)) + (ld4(yp + 10240) + ld4(yp + 15360));
    gst(__builtin_bit_cast(u32x4, xs[n] + (2.0f * y + skip[n]) * g[0][n]), outr, (tok * 640 + dcol0 + 16 * n + 4 * q) * 4);
  }
  STAMP(13);
}

__device__ __forceinline__ void phase2_s(const Args& a, int c, int j, int wave, int lane, u32 ta, u32 tb) {
  const int wm = wave & 3;
  const rsrc_t wsr = mk_rsrc(a.ws, WS_END);
  const int rh = j & 1;
  STAMP(7);
  u32x4 cf[5][5];
  {
    const u32 co = WS_CT + ((j >> 1) * 200 + wm * 50) * 1024 + lane * 16;
#pragma unroll
    for (int k = 0; k < CPRE; k++)
#pragma unroll
      for (int n = 0; n < 5; n++) cf[k][n] = gld(wsr, co, (k * 5 + n) * 1024);
    SCHED0();
    wait_vm<5 * CPRE>();
    if (cnt_add(C_F2, lane) == 3) put_flag(wsr, WS_F2 + (c * 16 + j) * 128, ta, tb, lane);
#pragma unroll
    for (int k = CPRE; k < 5; k++)
#pragma unroll
      for (int n = 0; n < 5; n++) cf[k][n] = gld(wsr, co, (k * 5 + n) * 1024);
    SCHED0();
  }
  STAMP(8);
  f32x4 y[4][5];
  {
    const u32 co = WS_CT + ((j >> 1) * 200 + wm * 50 + 25) * 1024 + lane * 16;
    polln(wsr, WS_F2 + (c * 16 + 4 * wm) * 128, 3, ta, tb, lane);
    STAMP(9);
    STAMPJ(1);
    constexpr int AW = 3;
    u32x4 hf[AW][4];
    const u32 hb = WS_HS + ((c * 8 + 4 * rh) * 40 + wm * 10) * 1024 + lane * 16;
#pragma unroll
    for (int k = 0; k < AW; k++)
#pragma unroll
      for (int m = 0; m < 4; m++) hf[k][m] = gld(wsr, hb, (m * 40 + k) * 1024);
    SCHED0();
#pragma unroll
    for (int m = 0; m < 4; m++)
#pragma unroll
      for (int n = 0; n < 5; n++) y[m][n] = f32x4{0.f, 0.f, 0.f, 0.f};
#pragma unroll
    for (int k = 0; k < 10; k++) {
#pragma unroll
      for (int n = 0; n < 5; n++)
#pragma unroll
        for (int m = 0; m < 4; m++) y[m][n] = mfma(asfrag(cf[k % 5][n]), asfrag(hf[k % AW][m]), y[m][n]);
      SCHED0();
      STAMPJ(2 + k);
      if (k + AW < 10) {
#pragma unroll
        for (int m = 0; m < 4; m++) hf[k % AW][m] = gld(wsr, hb, (m * 40 + k + AW) * 1024);
      }
      if (k < 5) {
#pragma unroll
        for (int n = 0; n < 5; n++) cf[k][n] = gld(wsr, co, (k * 5 + n) * 1024);
      }
      SCHED0();
    }
  }
  STAMP(10);
  cnt_wait(C_G2, 4);
#pragma unroll
  for (int m = 0; m < 4; m++)
#pragma unroll
    for (int n = 0; n < 5; n++) *(LAS f32x4*)L(((3 + m) * 4 + wm) * 5120 + n * 1024 + lane * 16) = y[m][n];
  cnt_add(C_Y, lane);
  STAMP(11);
}

__global__ __launch_bounds__(512, 2) void s5_fused(Args a) {
  extern __shared__ __attribute__((aligned(16))) char smem[];
  const int b = blockIdx.x, xcd = b & 7, r = b >> 3;
  const int c = xcd * 2 + (r >> 4), j = r & 15;
  const int wave = __builtin_amdgcn_readfirstlane(threadIdx.x >> 6), lane = threadIdx.x & 63;
  if ((u32)(size_t)(lptr)smem != 0) return;
  STAMP(0);
  {
    const u64 pc = __builtin_amdgcn_s_getpc() & ~(u64)127;
    const int part = (r * 8 + wave) % CPF_PARTS;
    dma4_plain(mk_rsrc((const void*)pc, CODE_BYTES), 163328 + 0, (part * 64 + lane) * 128, 0);
  }
  u32 ta, tb;
  if (wave >= 4) {
    if (j & 1) phase1<true, 4>(a, c, j, wave, lane, ta, tb);
    else phase1<true, 3>(a, c, j, wave, lane, ta, tb);
    phase2_m(a, c, j, wave, lane, ta, tb);
  } else {
    if (j & 1) phase1<false, 2>(a, c, j, wave, lane, ta, tb);
    else phase1<false, 3>(a, c, j, wave, lane, ta, tb);
    phase2_s(a, c, j, wave, lane, ta, tb);
  }
}
}

extern "C" void kernel_launch(void* const* d_in, const int* in_sizes, int n_in, void* d_out, int out_size, void* d_ws,
                              size_t ws_size, hipStream_t stream) {
  using namespace s5k;
  Args a;
  a.x = (const u16*)d_in[0];
  a.lre = (const u16*)d_in[1];
  a.lim = (const u16*)d_in[2];
  a.bre = (const u16*)d_in[3];
  a.bim = (const u16*)d_in[4];
  a.cre = (const u16*)d_in[5];
  a.cim = (const u16*)d_in[6];
  a.ldt = (const u16*)d_in[7];
  a.dsk = (const u16*)d_in[8];
  a.w1 = (const u16*)d_in[10];
  a.w2 = (const u16*)d_in[11];
  a.out = (float*)d_out;
  a.ws = (char*)d_ws + ((size_t)32 << 20);
  (void)hipFuncSetAttribute((const void*)s5_fused, hipFuncAttributeMaxDynamicSharedMemorySize, LDS_BYTES);
  s5_fused<<<256, 512, LDS_BYTES, stream>>>(a);
}
```

```cpp
#include <hip/hip_runtime.h>

#define STAMPJ(id)
namespace s5k {
typedef unsigned short u16;
typedef unsigned int u32;
typedef unsigned long long u64;
typedef short bf16x8 __attribute__((ext_vector_type(8)));
typedef short s16x2 __attribute__((ext_vector_type(2)));
typedef float f32x4 __attribute__((ext_vector_type(4)));
typedef float f32x2 __attribute__((ext_vector_type(2)));
typedef u32 u32x4 __attribute__((ext_vector_type(4)));
typedef u32 u32x2 __attribute__((ext_vector_type(2)));
typedef __bf16 bf16x2 __attribute__((ext_vector_type(2)));
#define LAS __attribute__((address_space(3)))
#define GAS __attribute__((address_space(1)))
typedef LAS char* lptr;
typedef __amdgpu_buffer_rsrc_t rsrc_t;

constexpr u32 WS_STATS = 4096;
constexpr u32 WS_AGG = 65536;
constexpr u32 WS_F1 = 262144, WS_F2 = WS_F1 + 32768, WS_F3 = WS_F2 + 32768;
constexpr u32 WS_PROF = 512 << 10;
constexpr u32 WS_H1 = 1u << 20;
constexpr u32 WS_HS = 9u << 20;
constexpr u32 WS_CT = 16u << 20;
constexpr u32 WS_WT = 18u << 20;
constexpr u32 WS_END = 21u << 20;
constexpr int CHUNK = 40960;
constexpr int LD_TRB = 143360, LD_STG = LD_TRB + 4096, LD_ST2 = LD_TRB + 8192, LD_CS = LD_ST2 + 1024, LD_AGW = LD_CS + 1024,
              LD_CIN = LD_AGW + 1536, LD_CNT = LD_CIN + 1536, LD_TAB = LD_CNT + 256, TABP = 144, LD_END = LD_TAB + 48 * TABP;
constexpr int LDS_BYTES = 163840;
static_assert(LD_END <= LDS_BYTES, "lds");
enum { T_A = 0, T_C = 1, T_8 = 2, T_32 = 6, T_128 = 10, T_512 = 14 };
enum { C_F1 = 0, C_B2 = 1, C_S1 = 2, C_S2 = 3, C_F2 = 4, C_G = 5, C_G2 = 6, C_Y = 7 };
constexpr int SC1 = 16;
#define MPRIO 1
#define SPRIO 0
#define CPRE 5
#define MPRIO2 2
#define SPRIO2 1
#define CODE_BYTES 74880
#define CPF_PARTS 9
#define YPRIO 0
#define GAW 3
#define GBD 0
#define WPRE 3
#define GPRIO 1
#define C1EARLY 0
#define CTAIL 2
#define WEARLY 0
#define SPRIO3 2
#define PSLEEP 2
#define OAUX 2
#define GBW 3
#define YAW 3
#define GMAP 4
constexpr u32 SPIN_CAP = 1u << 16;

struct Args {
  const u16 *x, *lre, *lim, *bre, *bim, *cre, *cim, *ldt, *dsk, *w1, *w2;
  float* out;
  char* ws;
};

__device__ __forceinline__ float u2f(u32 v) { return __builtin_bit_cast(float, v); }
__device__ __forceinline__ u32 f2u(float v) { return __builtin_bit_cast(u32, v); }
__device__ __forceinline__ float bf2f(u32 v) { return __builtin_bit_cast(float, v << 16); }
__device__ __forceinline__ float bfhi(u32 v) { return __builtin_bit_cast(float, v & 0xffff0000u); }
__device__ __forceinline__ u32 pk2(float a, float b) { return __builtin_bit_cast(u32, __builtin_convertvector(f32x2{a, b}, bf16x2)); }
__device__ __forceinline__ float rbf(float a) { return bf2f(pk2(a, a)); }
__device__ __forceinline__ f32x4 unpk4(u32x2 v) { return f32x4{bf2f(v.x), bfhi(v.x), bf2f(v.y), bfhi(v.y)}; }
__device__ __forceinline__ f32x4 rbf4(f32x4 v) { return unpk4(u32x2{pk2(v.x, v.y), pk2(v.z, v.w)}); }
__device__ __forceinline__ u32 relu2(u32 v) {
  return __builtin_bit_cast(u32, __builtin_elementwise_max(__builtin_bit_cast(s16x2, v), s16x2{0, 0}));
}
__device__ __forceinline__ f32x2 cmul(f32x2 a, f32x2 b) {
  float x, y;
  asm("v_mul_f32 %0, %2, %4\n\tv_mul_f32 %1, %2, %5\n\tv_fma_f32 %0, -%3, %5, %0\n\tv_fma_f32 %1, %3, %4, %1"
      : "=&v"(x), "=&v"(y) : "v"(a.x), "v"(a.y), "v"(b.x), "v"(b.y));
  return f32x2{x, y};
}
__device__ __forceinline__ f32x2 cfma(f32x2 a, f32x2 b, f32x2 c) {
  float x, y;
  asm("v_fma_f32 %0, %2, %4, %6\n\tv_fma_f32 %1, %2, %5, %7\n\tv_fma_f32 %0, -%3, %5, %0\n\tv_fma_f32 %1, %3, %4, %1"
      : "=&v"(x), "=&v"(y) : "v"(a.x), "v"(a.y), "v"(b.x), "v"(b.y), "v"(c.x), "v"(c.y));
  return f32x2{x, y};
}
__device__ __forceinline__ rsrc_t mk_rsrc(const void* p, u32 bytes) { return __builtin_amdgcn_make_buffer_rsrc((void*)p, 0, bytes, 0x00020000); }
__device__ __forceinline__ f32x4 mfma(bf16x8 a, bf16x8 b, f32x4 c) { return __builtin_amdgcn_mfma_f32_16x16x32_bf16(a, b, c, 0, 0, 0); }
__device__ __forceinline__ void mfma4(f32x4& c0, f32x4& c1, f32x4& c2, f32x4& c3, u32x4 a, const u32x4 (&b)[4]) {
  asm volatile("s_nop 1\n\tv_mfma_f32_16x16x32_bf16 %0, %4, %5, %0\n\tv_mfma_f32_16x16x32_bf16 %1, %4, %6, %1\n\t"
               "v_mfma_f32_16x16x32_bf16 %2, %4, %7, %2\n\tv_mfma_f32_16x16x32_bf16 %3, %4, %8, %3"
               : "+v"(c0), "+v"(c1), "+v"(c2), "+v"(c3) : "v"(a), "v"(b[0]), "v"(b[1]), "v"(b[2]), "v"(b[3]));
}
__device__ __forceinline__ void mfma_done() {
  __builtin_amdgcn_sched_barrier(0);
  asm volatile("s_nop 7\n\ts_nop 3" ::: "memory");
  __builtin_amdgcn_sched_barrier(0);
}
__device__ __forceinline__ bf16x8 asfrag(u32x4 v) { return __builtin_bit_cast(bf16x8, v); }
__device__ __forceinline__ void dma16(rsrc_t rs, u32 dst, u32 voff, u32 soff) {
  asm volatile("s_mov_b32 m0, %0\n\ts_nop 0\n\tbuffer_load_dwordx4 %1, %2, %3 offen lds" ::"s"(dst), "v"(voff), "s"(rs), "s"(soff) : "memory");
}
__device__ __forceinline__ void dma16_sc1(rsrc_t rs, u32 dst, u32 voff, u32 soff) {
  asm volatile("s_mov_b32 m0, %0\n\ts_nop 0\n\tbuffer_load_dwordx4 %1, %2, %3 offen sc1 lds" ::"s"(dst), "v"(voff), "s"(rs), "s"(soff) : "memory");
}
template <int DOFF> __device__ __forceinline__ void gdma(const char* base, u32 dst, u32 voff) {
  asm volatile("s_add_i32 m0, %0, %1\n\ts_nop 0\n\tglobal_load_lds_dwordx4 %2, %3 sc1" ::"s"(dst), "i"(DOFF), "v"(voff), "s"(base) : "memory", "scc");
}
__device__ __forceinline__ void dma4_sc1(rsrc_t rs, u32 dst, u32 voff, u32 soff) {
  asm volatile("s_mov_b32 m0, %0\n\ts_nop 0\n\tbuffer_load_dword %1, %2, %3 offen sc1 lds" ::"s"(dst), "v"(voff), "s"(rs), "s"(soff) : "memory");
}
__device__ __forceinline__ void dma4_plain(rsrc_t rs, u32 dst, u32 voff, u32 soff) {
  asm volatile("s_mov_b32 m0, %0\n\ts_nop 0\n\tbuffer_load_dword %1, %2, %3 offen lds" ::"s"(dst), "v"(voff), "s"(rs), "s"(soff) : "memory");
}
__device__ __forceinline__ lptr L(u32 addr) { return (lptr)(size_t)addr; }
__device__ __forceinline__ bf16x8 ld128(u32 p) { return *(LAS bf16x8*)L(p); }
__device__ __forceinline__ f32x2 ldc(u32 p) { return *(LAS f32x2*)L(p); }
__device__ __forceinline__ f32x4 ld4(u32 p) { return *(LAS f32x4*)L(p); }
__device__ __forceinline__ bf16x8 ldtr(u32 p0, u32 p1) {
  typedef short v4i16 __attribute__((ext_vector_type(4)));
  v4i16 a = __builtin_amdgcn_ds_read_tr16_b64_v4i16((LAS v4i16*)L(p0));
  v4i16 b = __builtin_amdgcn_ds_read_tr16_b64_v4i16((LAS v4i16*)L(p1));
  return __builtin_shufflevector(a, b, 0, 1, 2, 3, 4, 5, 6, 7);
}
__device__ __forceinline__ u32x4 gld(rsrc_t rs, u32 voff, u32 soff) { return __builtin_amdgcn_raw_buffer_load_b128(rs, voff, soff, SC1); }
__device__ __forceinline__ void gst(u32x4 v, rsrc_t rs, u32 voff) { __builtin_amdgcn_raw_buffer_store_b128(v, rs, voff, 0, SC1); }
template <int N> __device__ __forceinline__ void wait_vm() { asm volatile("s_waitcnt vmcnt(%0)" ::"i"(N) : "memory"); }
__device__ __forceinline__ void wait_lgkm0() { __builtin_amdgcn_s_waitcnt(0xc07f); asm volatile("" ::: "memory"); }
__device__ __forceinline__ void wg_barrier() {
  asm volatile("" ::: "memory");
  __builtin_amdgcn_s_barrier();
  asm volatile("" ::: "memory");
}
#define PIN(x) asm volatile("" : "+v"(x))
#define SCHED0() __builtin_amdgcn_sched_barrier(0)
__device__ __forceinline__ u32 cnt_add(int idx, int lane) {
  u32 old;
  const u32 addr = LD_CNT + idx * 4 + (lane == 0 ? 0 : 64), one = 1;
  asm volatile("s_waitcnt lgkmcnt(0)\n\ts_mov_b64 exec, 1\n\tds_add_rtn_u32 %0, %1, %2\n\ts_mov_b64 exec, -1\n\ts_waitcnt lgkmcnt(0)"
               : "=&v"(old) : "v"(addr), "v"(one) : "memory");
  return __builtin_amdgcn_readfirstlane(old);
}
__device__ __forceinline__ void cnt_wait(int idx, u32 want) {
  const u32 addr = LD_CNT + idx * 4;
#pragma unroll 1
  for (u32 s = 0; s < (SPIN_CAP << 4); s++) {
    u32 v;
    asm volatile("ds_read_b32 %0, %1\n\ts_waitcnt lgkmcnt(0)" : "=&v"(v) : "v"(addr) : "memory");
    if (__builtin_amdgcn_readfirstlane(v) >= want) break;
    __builtin_amdgcn_s_sleep(1);
  }
}
__device__ __forceinline__ void polln(rsrc_t wsr, u32 off, int mask, u32 ta, u32 tb, int lane) {
  const u32 vo = off + lane * 128;
#pragma unroll 1
  for (u32 s = 0; s < SPIN_CAP; s++) {
    asm volatile("" ::: "memory");
    u32x2 v = {ta, tb};
    if (lane <= mask) v = __builtin_amdgcn_raw_buffer_load_b64(wsr, vo, 0, SC1);
    if (__all((v.x == ta) & (v.y == tb))) break;
    __builtin_amdgcn_s_sleep(PSLEEP);
  }
  __builtin_amdgcn_fence(__ATOMIC_ACQUIRE, "wavefront");
  asm volatile("" ::: "memory");
}
__device__ __forceinline__ void put_flag(rsrc_t wsr, u32 off, u32 ta, u32 tb, int lane) {
  __builtin_amdgcn_raw_buffer_store_b64(u32x2{ta, tb}, wsr, lane == 0 ? off : WS_END, 0, SC1);
}
template <int CTRL> __device__ __forceinline__ float dpp(float x) {
  return __builtin_bit_cast(float, __builtin_amdgcn_mov_dpp(__builtin_bit_cast(int, x), CTRL, 0xf, 0xf, true));
}
__device__ __forceinline__ void rows4(float x, float (&r)[4]) {
  auto s = __builtin_amdgcn_permlane16_swap(f2u(x), f2u(x), false, false);
  const u32 e = s[0], o = s[1];
  auto te = __builtin_amdgcn_permlane32_swap(e, e, false, false);
  auto to = __builtin_amdgcn_permlane32_swap(o, o, false, false);
  const u32 r0 = te[0], r2 = te[1], r1 = to[0], r3 = to[1];
  r[0] = u2f(r0); r[1] = u2f(r1); r[2] = u2f(r2); r[3] = u2f(r3);
}
__device__ __forceinline__ float rows_sum(float x) {
  auto s = __builtin_amdgcn_permlane16_swap(f2u(x), f2u(x), false, false);
  const u32 a = s[0], b = s[1];
  x = u2f(a) + u2f(b);
  auto t = __builtin_amdgcn_permlane32_swap(f2u(x), f2u(x), false, false);
  const u32 c = t[0], d = t[1];
  return u2f(c) + u2f(d);
}
__device__ __forceinline__ float wave_sum(float v) {
  v += dpp<0xB1>(v); v += dpp<0x4E>(v); v += dpp<0x124>(v); v += dpp<0x128>(v);
  return rows_sum(v);
}
__device__ __forceinline__ float fexp(float x) {
  const float hi = x * 1.4426950408889634f;
  const float lo = __builtin_fmaf(x, 1.4426950408889634f, -hi) + x * 1.925963033500011e-8f;
  return __builtin_amdgcn_exp2f(hi) * (1.0f + lo * 0.6931471805599453f);
}
__device__ __forceinline__ void fsincos(float x, float& sn, float& cs) {
  if (__builtin_expect(__any(!(__builtin_fabsf(x) < 2000.f)), 0)) { sincosf(x, &sn, &cs); return; }
  const float n = __builtin_rintf(x * 0.6366197723675814f);
  float r = __builtin_fmaf(n, -1.5703125f, x);
  r = __builtin_fmaf(n, -4.837512969970703125e-4f, r);
  r = __builtin_fmaf(n, -7.54978995489188e-8f, r);
  const float r2 = r * r;
  const float s = __builtin_fmaf(r * r2, __builtin_fmaf(r2, __builtin_fmaf(r2, -1.9515295891e-4f, 8.3321608736e-3f), -1.6666654611e-1f), r);
  const float c = __builtin_fmaf(r2 * r2, __builtin_fmaf(r2, __builtin_fmaf(r2, 2.443315711809948e-5f, -1.388731625493765e-3f), 4.166664568298827e-2f),
                                 __builtin_fmaf(r2, -0.5f, 1.0f));
  const int k = (int)n;
  const float s1 = (k & 1) ? c : s, c1 = (k & 1) ? s : c;
  sn = (k & 2) ? -s1 : s1;
  cs = ((k + 1) & 2) ? -c1 : c1;
}
#define STAMP(id) asm volatile("; MARK " #id)
#define STAMPI(id)

struct Pre {
  u64 epoch;
  const char* gb[5];
  u32 dvo[5], dstep[4];
  u32 dd0, dd1;
  int kc;
};
__device__ __forceinline__ void head(const Args& a, Pre& P, u64 pc, int c, int j, int wave, int lane, u32& ta, u32& tb) {
  const int li = lane & 15, r8 = lane >> 3, sl = lane & 7, i2 = j >> 1, tB = j & 1;
  const int nS = 3 - tB, nM = 3 + tB;
  const int s0 = 5 * i2 + 3 * tB;
  const int m0 = 7 * i2 + ((i2 & 1) ? 3 * tB : 4 - 4 * tB);
  int kc = c + j;
  kc -= kc >= 20 ? 20 : kc >= 10 ? 10 : 0;
  kc = kc == 10 ? 0 : kc;
  u64 pbre = (u64)a.bre, pbim = (u64)a.bim, pw1 = (u64)a.w1;
  asm volatile("" : "+s"(pbre), "+s"(pbim), "+s"(pw1) : "s"(a.x), "s"(a.ws), "s"(a.lre), "s"(a.lim), "s"(a.ldt));
  u32x2 ep;
  u32x4 xr0, xr1;
  u32 plr, pli, pdt;
  const GAS char* xrow = (const GAS char*)a.x + (size_t)(c * 128 + j * 8 + wave) * 1280;
  asm volatile("global_load_dwordx2 %0, %1, %2 sc1" : "=v"(ep) : "v"(0), "s"(a.ws) : "memory");
  asm volatile("global_load_dwordx4 %0, %1, %2" : "=v"(xr0) : "v"(lane * 16), "s"(xrow) : "memory");
  asm volatile("global_load_dwordx4 %0, %1, %2 offset:1024" : "=v"(xr1) : "v"(li * 16), "s"(xrow) : "memory");
  const int ch = (s0 * 16 + (lane < 16 * nS ? lane : li)) * 2;
  asm volatile("global_load_ushort %0, %1, %2" : "=v"(plr) : "v"(ch), "s"(a.lre) : "memory");
  asm volatile("global_load_ushort %0, %1, %2" : "=v"(pli) : "v"(ch), "s"(a.lim) : "memory");
  asm volatile("global_load_ushort %0, %1, %2" : "=v"(pdt) : "v"(ch), "s"(a.ldt) : "memory");
  P.dd0 = wave * 2048;
  P.dd1 = 16384 + wave * 3072;
  P.dstep[0] = 128;
#pragma unroll
  for (int b = 0; b < 2; b++) {
    P.gb[b] = (const char*)a.x + (c * 128 + (wave >> 1) * 32 + b * 16 + (wave & 1) * 4) * 1280;
    P.dvo[b] = (8 * (r8 >> 2) + (r8 & 3)) * 1280 + ((sl ^ (b * 4 + (r8 >> 1))) << 4);
  }
  gdma<0>(P.gb[0] + kc * 128, P.dd0, P.dvo[0]);
  gdma<1024>(P.gb[1] + kc * 128, P.dd0, P.dvo[1]);
#pragma unroll
  for (int t = 0; t < 3; t++) {
    const int k = wave * 3 + t, b = 2 + t;
    const bool isw = k >= 4 * nS;
    const int sub = k & 3;
    const u32 bvo = r8 * 1280 + ((sl ^ ((sub & 1) * 4 + (r8 >> 1))) << 4);
    const u32 idx = (k - 4 * nS) * 64 + lane;
    const u32 kr = (idx * (tB ? 32768u : 43691u)) >> 19, cpos = idx - kr * (4 * nM);
    const u32 sw = (kr & (3 * tB)) | (((kr >> 3) & 1) << (2 * tB));
    u32 col16 = m0 * 4 + (((cpos >> 1) ^ sw) << 1) + (cpos & 1);
    col16 -= col16 >= 216 ? 8 : 0;
    P.gb[b] = (const char*)(isw ? pw1 : sub < 2 ? pbre : pbim) + (isw ? 0 : ((s0 + (k >> 2)) * 16 + (sub & 1) * 8) * 1280);
    P.dvo[b] = isw ? kr * 3456 + col16 * 16 : bvo;
    P.dstep[b - 1] = isw ? 64 * 3456 : 128;
    if (t == 0) gdma<0>(P.gb[b] + kc * P.dstep[b - 1], P.dd1, P.dvo[b]);
    if (t == 1) gdma<1024>(P.gb[b] + kc * P.dstep[b - 1], P.dd1, P.dvo[b]);
    if (t == 2) gdma<2048>(P.gb[b] + kc * P.dstep[b - 1], P.dd1, P.dvo[b]);
  }
  kc = kc == 9 ? 0 : kc + 1;
  {
    const int part = (blockIdx.x >> 3) & 15;
    dma4_plain(mk_rsrc((const void*)pc, CODE_BYTES), LD_TRB + 6144, wave == 7 ? (part * 64 + lane) * 128 : CODE_BYTES, 0);
  }
  *(LAS u32*)L(LD_CNT + li * 4) = 0;
  const rsrc_t wsr = mk_rsrc(a.ws, WS_END);
  STAMP(1);
  auto issue1 = [&]() {
    gdma<CHUNK>(P.gb[0] + kc * 128, P.dd0, P.dvo[0]);
    gdma<CHUNK + 1024>(P.gb[1] + kc * 128, P.dd0, P.dvo[1]);
    gdma<CHUNK>(P.gb[2] + kc * P.dstep[1], P.dd1, P.dvo[2]);
    gdma<CHUNK + 1024>(P.gb[3] + kc * P.dstep[2], P.dd1, P.dvo[3]);
    gdma<CHUNK + 2048>(P.gb[4] + kc * P.dstep[3], P.dd1, P.dvo[4]);
  };
  asm volatile("s_waitcnt vmcnt(6)" : "+v"(ep), "+v"(xr0), "+v"(xr1), "+v"(plr), "+v"(pli), "+v"(pdt)::"memory");
  P.kc = kc == 9 ? 0 : kc + 1;
  {
    P.epoch = (u64)__builtin_amdgcn_readfirstlane(ep.y) << 32 | __builtin_amdgcn_readfirstlane(ep.x);
    u64 z = P.epoch + 0x9E3779B97F4A7C15ull;
    z = (z ^ (z >> 30)) * 0xBF58476D1CE4E5B9ull;
    z = (z ^ (z >> 27)) * 0x94D049BB133111EBull;
    z ^= z >> 31;
    ta = (u32)z;
    tb = (u32)(z >> 32);
    float v[16];
#pragma unroll
    for (int e = 0; e < 4; e++) {
      v[2 * e] = bf2f(xr0[e]); v[2 * e + 1] = bfhi(xr0[e]);
      v[8 + 2 * e] = bf2f(xr1[e]); v[8 + 2 * e + 1] = bfhi(xr1[e]);
    }
    float s0_ = 0.f, s1_ = 0.f;
#pragma unroll
    for (int e = 0; e < 8; e++) { s0_ += v[e]; s1_ += v[8 + e]; }
    const float mean = wave_sum(lane < 16 ? s0_ + s1_ : s0_) * (1.0f / 640.0f);
    s0_ = 0.f; s1_ = 0.f;
#pragma unroll
    for (int e = 0; e < 8; e++) {
      const float d0 = v[e] - mean, d1 = v[8 + e] - mean;
      s0_ += d0 * d0; s1_ += d1 * d1;
    }
    const float var = wave_sum(lane < 16 ? s0_ + s1_ : s0_) * (1.0f / 640.0f);
    const float mb = rbf(mean), den = rbf(__builtin_amdgcn_sqrtf(rbf(rbf(var) + 1e-5f)));
    gst(u32x4{f2u(mb), ta, f2u(__builtin_amdgcn_rcpf(den)), tb}, wsr, lane == 0 ? WS_STATS + (c * 128 + j * 8 + wave) * 16 : WS_END);
  }
  issue1();
  {
    if (wave == 0) {
      const float lr = bf2f(plr), lim = bf2f(pli);
      const float dt = rbf(fexp(bf2f(pdt)));
      const float er = fexp(dt * lr);
      float sn, cn;
      fsincos(dt * lim, sn, cn);
      const f32x2 a1 = {er * cn, er * sn};
      const float nr = a1.x - 1.0f, dn = 1.0f / (lr * lr + lim * lim);
      const u32 tp = LD_TAB + (lane < 48 ? lane : 47) * TABP;
      if (lane < 16 * nS) {
        *(LAS f32x4*)L(tp) = f32x4{a1.x, a1.y, (nr * lr + a1.y * lim) * dn, (a1.y * lr - nr * lim) * dn};
        const f32x2 a2 = cmul(a1, a1), a4 = cmul(a2, a2);
        f32x2 b = cmul(a4, a4);
#pragma unroll
        for (int i = 0; i < 4; i++) {
          const f32x2 b2 = cmul(b, b), b3 = cmul(b2, b);
          *(LAS f32x4*)L(tp + 16 + i * 32) = f32x4{1.f, 0.f, b.x, b.y};
          *(LAS f32x4*)L(tp + 32 + i * 32) = f32x4{b2.x, b2.y, b3.x, b3.y};
          b = cmul(b2, b2);
        }
      }
    }
  }
}

template <bool IS_M, int NP>
__device__ __forceinline__ void phase1(const Args& a, Pre& P, int c, int j, int wave, int lane, u32 ta, u32 tb, u32x4 (&cf)[5][5]) {
  constexpr int NU = 2 * NP;
  constexpr bool TYPE_B = IS_M ? (NP == 4) : (NP == 2);
  constexpr int nS = TYPE_B ? 2 : 3, nM = 6 - nS, MP = nM * 64;
  const int wm = wave & 3;
  const int i2 = j >> 1;
  const int s0 = 5 * i2 + (TYPE_B ? 3 : 0);
  const int m0 = 7 * i2 + ((i2 & 1) ? (TYPE_B ? 3 : 0) : (TYPE_B ? 0 : 4));
  const int li = lane & 15, q = lane >> 4;
  const rsrc_t wsr = mk_rsrc(a.ws, WS_END);
  u32 kc = P.kc;
  auto dma1 = [&](int bufoff, int b) {
    const char* src = P.gb[b] + kc * P.dstep[b < 2 ? 0 : b - 1];
    const u32 d = b < 2 ? P.dd0 : P.dd1;
#define GD(B, O) if (bufoff == B && (b < 2 ? b : b - 2) == O) gdma<B + O * 1024>(src, d, P.dvo[b]);
    GD(0, 0) GD(0, 1) GD(0, 2) GD(CHUNK, 0) GD(CHUNK, 1) GD(CHUNK, 2) GD(2 * CHUNK, 0) GD(2 * CHUNK, 1) GD(2 * CHUNK, 2)
#undef GD
  };
  const int rid = c * 2 + (j & 1);

  int pr[NP];
#pragma unroll
  for (int p = 0; p < NP; p++) pr[p] = (p + wm) % NP;
  u32 xb[2][2], wb[2][NU];
  {
    const u32 xo = li * 128 + ((q ^ (li >> 1)) << 4);
    const int qq = li >> 2;
    const u32 rowb = 16384 + nS * 4096 + (8 * q + qq) * MP + (lane & 3) * 8;
    const int f = TYPE_B ? (qq | ((q & 1) << 2)) : (q & 1);
#pragma unroll
    for (int h = 0; h < 2; h++) {
#pragma unroll
      for (int kk = 0; kk < 2; kk++) {
        xb[h][kk] = h * 2 * CHUNK + wm * 4096 + (xo ^ (kk << 6));
        PIN(xb[h][kk]);
      }
#pragma unroll
      for (int u = 0; u < NU; u++) {
        wb[h][u] = h * 2 * CHUNK + (IS_M ? rowb + (((2 * pr[u >> 1] + (u & 1)) ^ f) << 5) : 16384 + pr[u >> 1] * 4096 + (xo ^ ((u & 1) << 6)));
        PIN(wb[h][u]);
      }
    }
  }
  bf16x8 xf[2][2], wf[2][NU];
  auto read1 = [&](int set, int bufoff, int kk, int f) {
    const int h = bufoff == 2 * CHUNK, bo = h ? 0 : bufoff;
    if (f < 2) {
      xf[set][f] = ld128(xb[h][kk] + (bo + f * 2048));
    } else if (f < NU + 2) {
      const int u = f - 2;
      if (!IS_M) wf[set][u] = ld128(wb[h][(u & ~1) + kk] + (bo + (u & 1) * 2048));
      else wf[set][u] = ldtr(wb[h][u] + (bo + kk * 32 * MP), wb[h][u] + (bo + kk * 32 * MP + 4 * MP));
    }
  };
  f32x4 acc[2][NU], csum[2];
#pragma unroll
  for (int m = 0; m < 2; m++) {
#pragma unroll
    for (int u = 0; u < NU; u++) { acc[m][u] = f32x4{0.f, 0.f, 0.f, 0.f}; PIN(acc[m][u]); }
    csum[m] = f32x4{0.f, 0.f, 0.f, 0.f};
    PIN(csum[m]);
  }
  u32x4 ones4 = {0x3f803f80u, 0x3f803f80u, 0x3f803f80u, 0x3f803f80u};
  PIN(ones4);
  constexpr int G = NU + 1;
  constexpr int NDB = IS_M ? 2 : 3;
  auto dpos = [](int d) { return (2 * G * d + (IS_M ? G : 0)) / 5; };
  static_assert((2 * G * (NDB - 1) + (IS_M ? G : 0)) / 5 < G && (2 * G * NDB + (IS_M ? G : 0)) / 5 >= G, "split");
  auto step = [&](int set, int rbuf, int rkk, int dbuf, bool tail) {
#pragma unroll
    for (int g = 0; g < G; g++) {
      if (g > 0) {
        const int u = g - 1;
#pragma unroll
        for (int m = 0; m < 2; m++)
          acc[m][u] = IS_M ? mfma(wf[set][u], xf[set][m], acc[m][u]) : mfma(xf[set][m], wf[set][u], acc[m][u]);
      } else {
        csum[0] = mfma(asfrag(ones4), wf[set][0], csum[0]);
        csum[1] = mfma(asfrag(ones4), wf[set][1], csum[1]);
      }
      if (rbuf >= 0) {
        if (g == 0) { read1(set ^ 1, rbuf, rkk, 2); read1(set ^ 1, rbuf, rkk, 3); }
        else if (g == 1) { read1(set ^ 1, rbuf, rkk, 0); read1(set ^ 1, rbuf, rkk, 1); }
        else read1(set ^ 1, rbuf, rkk, g + 2);
      } else if (g == 3) {
        LAS float* csp = (LAS float*)L(LD_CS + (IS_M ? 512 : 0) + (wm < NP ? wm : 3) * 128 + (lane & 31) * 4);
        float e0 = csum[0].x, e1 = csum[1].x;
        PIN(e0); PIN(e1);
        if (wm < NP) *csp = (q & 1) ? e1 : e0;
      }
      if (!IS_M && tail) {
#pragma unroll
        for (int f = 0; f < 5 * CTAIL; f++)
          if (f * 2 * G / (5 * CTAIL) == (set == 0 ? g : g + G))
            cf[f / 5][f % 5] = gld(wsr, lane * 16, WS_CT + (i2 * 200 + wm * 50 + f) * 1024);
      }
#pragma unroll
      for (int d = 0; d < 5; d++)
        if ((set == 1 ? g : g + G) == dpos(d)) dma1(dbuf, d);
      SCHED0();
    }
  };

#pragma unroll
  for (int d = 0; d < NDB; d++) dma1(2 * CHUNK, d);
  u32x4 rt[2];
  const int rks = min(rid * 2 + (wm & 1), 53);
  if (!IS_M) {
#pragma unroll
    for (int i = 0; i < 2; i++) {
      const int f = min(rid * 8 + wm * 2 + i, 199);
      const int wk = f / 5, n = f - wk * 5, w = wk / 10, k = wk - w * 10;
      const GAS char* src = (const GAS char*)(k < 5 ? a.cre : a.cim) + (i2 * 80 + n * 16) * 1280 + (160 * w + 32 * (k < 5 ? k : k - 5)) * 2;
      asm volatile("global_load_dwordx4 %0, %1, %2" : "=v"(rt[i]) : "v"(li * 1280 + q * 16), "s"(src) : "memory");
    }
    wait_vm<7 + NDB + 2>();
  } else {
    wait_vm<7 + NDB>();
  }
  wait_lgkm0();
  wg_barrier();
  STAMP(2);
  __builtin_amdgcn_s_setprio(IS_M ? MPRIO : SPRIO);
#pragma unroll
  for (int f = 0; f < NU + 2; f++) read1(0, 0, 0, f);
  int it = 0;
  auto body = [&](int prv, int cur, int nxt) {
    step(0, cur, 1, prv, nxt < 0);
    kc = kc == 9 ? 0 : kc + 1;
    if (nxt < 0) {
      const int r = wave * 16 + li;
      u32x4 g = *(LAS u32x4*)L(LD_STG + r * 16);
      if (!__all((g.y == ta) & (g.w == tb))) {
#pragma unroll 1
        for (u32 s = 0; s < SPIN_CAP; s++) {
          asm volatile("" ::: "memory");
          g = gld(wsr, WS_STATS + (c * 128 + r) * 16, 0);
          if (__all((g.y == ta) & (g.w == tb))) break;
        }
      }
      const int t5 = r & 31;
      *(LAS f32x2*)L(LD_ST2 + ((r & ~31) + (((t5 >> 2) & 1) << 4) + ((t5 >> 3) << 2) + (t5 & 3)) * 8) = f32x2{u2f(g.x), u2f(g.z)};
    } else if (!IS_M && it == 0) wait_vm<7>();
    else if (IS_M && it == 1 && wm < 2) wait_vm<10>();
    else wait_vm<5>();
    wait_lgkm0();
    wg_barrier();
    STAMPI(3 + it);
    if (IS_M && it == 0 && wm < 2) {
      const rsrc_t w2r = mk_rsrc(a.w2, 1728 * 1280);
#pragma unroll
      for (int b = 0; b < 5; b++) {
        const int idx = b * 64 + lane, kr = idx / 10, cpos = idx - kr * 10;
        dma16(w2r, 3 * CHUNK + wm * 5120 + b * 1024, kr * 1280 + i2 * 160 + cpos * 16, rks * (32 * 1280));
      }
    }
    if (it == 2) {
      if (!IS_M) {
        asm volatile("" : "+v"(rt[0]), "+v"(rt[1]));
#pragma unroll
        for (int i = 0; i < 2; i++) gst(rt[i], wsr, WS_CT + (i2 * 200 + min(rid * 8 + wm * 2 + i, 199)) * 1024 + lane * 16);
      } else if (wm < 2) {
        const u32 p = 3 * CHUNK + wm * 5120 + (4 * q + (li >> 2)) * 160 + (lane & 3) * 8;
#pragma unroll
        for (int n = 0; n < 5; n++)
          gst(__builtin_bit_cast(u32x4, ldtr(p + n * 32, p + n * 32 + 2560)), wsr, WS_WT + ((i2 * 54 + rks) * 5 + n) * 1024 + lane * 16);
      }
    }
    if (IS_M && it == 3 && wm == 3) put_flag(wsr, WS_F3 + (i2 * 32 + rid) * 128, ta, tb, lane);
    if (it >= 7) {
      if (it == 7) {
        const u32 f = *(LAS u32*)L(LD_TRB + wave * 256 + lane * 4);
        if (!__all(f == ((lane & 1) ? tb : ta))) polln(wsr, WS_F3 + i2 * 32 * 128, 31, ta, tb, lane);
#pragma unroll
        for (int b = 0; b < 5; b++) {
          P.gb[b] = a.ws + b * 1024;
          P.dvo[b] = lane * 16;
          if (b < 4) P.dstep[b] = 1024;
        }
        P.dd0 = wave * 5120;
        P.dd1 = wave * 5120 + 2048;
      }
      const int lk = (cur / CHUNK) * 2 + (IS_M ? 1 : 0);
      kc = lk < 3 ? (WS_WT >> 10) + (i2 * 54 + wm * 14 + lk) * 5 : (WS_CT >> 10) + i2 * 200 + wm * 50 + 10 + lk * 5;
    }
    SCHED0();
    step(1, nxt, 0, cur, nxt < 0);
    if (IS_M && it == 8) {
#pragma unroll
      for (int b = 0; b < 5; b++) dma16_sc1(wsr, (24 + wm) * 5120 + b * 1024, lane * 16, WS_CT + (i2 * 200 + wm * 50 + 40 + b) * 1024);
    }
    if (it == 5) {
      dma4_sc1(wsr, LD_TRB + wave * 256, (lane >> 1) * 128 + (lane & 1) * 4, WS_F3 + i2 * 32 * 128);
      dma4_sc1(wsr, LD_STG + wave * 256, lane * 4, WS_STATS + c * 2048 + wave * 256);
    }
    it++;
  };
#pragma unroll 1
  for (int r = 0; r < 3; r++) {
    body(2 * CHUNK, 0, CHUNK);
    body(0, CHUNK, 2 * CHUNK);
    body(CHUNK, 2 * CHUNK, 0);
  }
  body(2 * CHUNK, 0, -1);
#pragma unroll
  for (int d = NDB; d < 5; d++) dma1(0, d);
  __builtin_amdgcn_s_setprio(IS_M ? MPRIO2 : SPRIO2);
  STAMP(3);
  float mb[2];
  f32x4 st[4];
  f32x2 ca[NP], cc[NP], a8r[NP];
  u32 tp[NP];
  if (IS_M) {
#pragma unroll
    for (int m = 0; m < 2; m++) mb[m] = *(LAS float*)L(LD_ST2 + (wm * 32 + m * 16 + li) * 8);
  } else {
#pragma unroll
    for (int i = 0; i < 4; i++) st[i] = ld4(LD_ST2 + (wm * 32 + (i >> 1) * 16 + 4 * q + 2 * (i & 1)) * 8);
#pragma unroll
    for (int p = 0; p < NP; p++) {
      tp[p] = LD_TAB + (pr[p] * 16 + li) * TABP;
      const f32x4 t0 = ld4(tp[p]);
      ca[p] = f32x2{t0.x, t0.y};
      cc[p] = f32x2{t0.z, t0.w};
      a8r[p] = ldc(tp[p] + (T_8 + 3 - q) * 8);
    }
  }
  wait_lgkm0();
  wg_barrier();
  STAMP(4);

  if (IS_M) {
#pragma unroll
    for (int p = 0; p < NP; p++) {
      const f32x4 c0 = ld4(LD_CS + 512 + pr[p] * 128 + q * 16), c1 = ld4(LD_CS + 512 + pr[p] * 128 + 64 + q * 16);
      const u32 off = (m0 + pr[p] < 54) ? WS_H1 + ((c * 8 + 2 * wm) * 54 + m0 + pr[p]) * 1024 : WS_END;
#pragma unroll
      for (int m = 0; m < 2; m++) {
        const f32x4 v0 = acc[m][2 * p] - mb[m] * c0, v1 = acc[m][2 * p + 1] - mb[m] * c1;
        gst(u32x4{relu2(pk2(v0.x, v0.y)), relu2(pk2(v0.z, v0.w)), relu2(pk2(v1.x, v1.y)), relu2(pk2(v1.z, v1.w))}, wsr,
            off + m * 54 * 1024 + lane * 16);
      }
    }
  } else {
    f32x2 u[NP][8];
    f32x2 seg[NP];
    float Sr[NP], Si[NP];
#pragma unroll
    for (int p = 0; p < NP; p++) {
      Sr[p] = *(LAS float*)L(LD_CS + pr[p] * 128 + li * 4);
      Si[p] = *(LAS float*)L(LD_CS + pr[p] * 128 + 64 + li * 4);
    }
#pragma unroll
    for (int p = 0; p < NP; p++) {
      f32x2 h;
#pragma unroll
      for (int t = 0; t < 8; t++) {
        const float mu = st[t >> 1][(t & 1) * 2], rs = st[t >> 1][(t & 1) * 2 + 1];
        u[p][t] = f32x2{(acc[t >> 2][2 * p][t & 3] - mu * Sr[p]) * rs, (acc[t >> 2][2 * p + 1][t & 3] - mu * Si[p]) * rs};
        h = t ? cfma(ca[p], h, u[p][t]) : u[p][0];
      }
      seg[p] = h;
      h = cmul(a8r[p], h);
      *(LAS f32x2*)L(LD_AGW + ((wm * 3 + pr[p]) * 16 + li) * 8) = f32x2{rows_sum(h.x), rows_sum(h.y)};
    }
    if (cnt_add(C_S1, lane) == 3) {
      const int ch = lane < 48 ? lane : 47;
      const f32x2 a32 = ldc(LD_TAB + ch * TABP + (T_32 + 1) * 8);
      f32x2 h = ldc(LD_AGW + ch * 8);
#pragma unroll
      for (int w = 1; w < 4; w++) h = cfma(a32, h, ldc(LD_AGW + (w * 48 + ch) * 8));
      gst(u32x4{f2u(h.x), ta, f2u(h.y), tb}, wsr, q < NP ? WS_AGG + (c * 640 + s0 * 16 + lane) * 16 : WS_END);
    }
    STAMP(5);
    __builtin_amdgcn_s_setprio(SPRIO3);
    const int cp = 4 * wm + q, e = c - 1 - cp;
    const u32 go = WS_AGG + (cp * 640 + s0 * 16 + li) * 16;
    u32x4 lg[NP];
    {
      const u32 co = WS_CT + (i2 * 200 + wm * 50) * 1024 + lane * 16;
#pragma unroll
      for (int k = CTAIL; k < CPRE; k++)
#pragma unroll
        for (int n = 0; n < 5; n++) cf[k][n] = gld(wsr, co, (k * 5 + n) * 1024);
      SCHED0();
    }
    cnt_wait(C_S1, 4);
    f32x2 aq[NP], pre[NP];
    u32 hso[NP];
    bool done = false;
    auto lb_issue = [&]() {
      asm volatile("" ::: "memory");
#pragma unroll
      for (int p = 0; p < NP; p++) lg[p] = gld(wsr, go, pr[p] * 256);
      SCHED0();
    };
    auto lb_check = [&]() {
      bool ok = true;
#pragma unroll
      for (int p = 0; p < NP; p++) ok = ok & (lg[p].y == ta) & (lg[p].w == tb);
      done = __all(ok | (e < 0));
    };
#pragma unroll
    for (int p = 0; p < NP; p++) {
      if (p == NP - 1) lb_issue();
      const f32x2 a32 = ldc(tp[p] + (T_32 + 1) * 8), a8q = ldc(tp[p] + (T_8 + q) * 8), aw = ldc(tp[p] + (T_32 + wm) * 8);
      f32x2 h = {0.f, 0.f};
#pragma unroll
      for (int w = 0; w < 3; w++) {
        const f32x2 g = ldc(LD_AGW + ((w * 3 + pr[p]) * 16 + li) * 8);
        if (w < wm) h = cfma(a32, h, g);
      }
#pragma unroll
      for (int t = 0; t < 8; t++) u[p][t] = cmul(cc[p], u[p][t]);
      {
        const f32x2 a8 = ldc(tp[p] + (T_8 + 1) * 8);
        float gr[4], gi[4];
        rows4(seg[p].x, gr); rows4(seg[p].y, gi);
#pragma unroll
        for (int w = 0; w < 3; w++) {
          const f32x2 t = cfma(a8, h, f32x2{gr[w], gi[w]});
          h.x = w < q ? t.x : h.x;
          h.y = w < q ? t.y : h.y;
          PIN(h);
        }
      }
      pre[p] = cmul(cc[p], h);
      aq[p] = cmul(cc[p], cmul(a8q, aw));
      const int cu = s0 + pr[p];
      hso[p] = WS_HS + ((c * 8 + 2 * wm) * 40 + (cu / 10) * 10 + ((cu % 10) >> 1) + (lane >> 5) * 5) * 1024 + (cu & 1) * 512 + (lane & 31) * 16;
      asm volatile("" : "+v"(pre[p]), "+v"(aq[p]), "+v"(hso[p]), "+v"(u[p][0]), "+v"(u[p][1]), "+v"(u[p][2]), "+v"(u[p][3]), "+v"(u[p][4]),
                   "+v"(u[p][5]), "+v"(u[p][6]), "+v"(u[p][7]));
    }
    u32 tw = LD_TRB + wm * 2048 + (li >> 3) * 256 + q * 64 + (li & 7) * 2, tr = LD_TRB + wm * 2048 + lane * 16;
    PIN(tw); PIN(tr);
    {
      f32x2 pw[NP];
#pragma unroll
      for (int p = 0; p < NP; p++) {
        pw[p] = cmul(ldc(tp[p] + (T_128 + (e & 3)) * 8), ldc(tp[p] + (T_512 + ((e >> 2) & 3)) * 8));
        PIN(pw[p]);
      }
      lb_check();
#pragma unroll 1
      for (u32 s = 0; s < SPIN_CAP && !done; s++) {
        lb_issue();
        lb_check();
      }
#pragma unroll
      for (int p = 0; p < NP; p++) {
        f32x2 h = cmul(pw[p], f32x2{u2f(lg[p].x), u2f(lg[p].z)});
        h.x = e < 0 ? 0.f : h.x;
        h.y = e < 0 ? 0.f : h.y;
        *(LAS f32x2*)L(LD_CIN + ((wm * 3 + pr[p]) * 16 + li) * 8) = f32x2{rows_sum(h.x), rows_sum(h.y)};
      }
      STAMP(12);
      cnt_add(C_S2, lane);
      cnt_add(C_B2, lane);
    }
    cnt_wait(C_S2, 4);
    STAMP(6);
    f32x2 hin[NP];
#pragma unroll
    for (int p = 0; p < NP; p++) {
      const u32 ci = LD_CIN + (pr[p] * 16 + li) * 8;
      hin[p] = (ldc(ci) + ldc(ci + 384)) + (ldc(ci + 768) + ldc(ci + 1152));
    }
    u32x4 o0, o1;
#pragma unroll
    for (int p = 0; p <= NP; p++) {
      u32 pk[8];
      if (p < NP) {
        f32x2 h = cfma(aq[p], hin[p], pre[p]);
#pragma unroll
        for (int t = 0; t < 8; t++) {
          h = cfma(ca[p], h, u[p][t]);
          pk[t] = pk2(h.x, -h.y);
        }
      }
      SCHED0();
      if (p > 0) {
        gst(o0, wsr, hso[p - 1]);
        gst(o1, wsr, hso[p - 1] + 40 * 1024);
      }
      if (p < NP) {
        asm volatile("" ::: "memory");
#pragma unroll
        for (int t = 0; t < 8; t++) {
          *(LAS u16*)L(tw + (t >> 2) * 1024 + (t & 3) * 16) = (u16)pk[t];
          *(LAS u16*)L(tw + (t >> 2) * 1024 + (t & 3) * 16 + 512) = (u16)(pk[t] >> 16);
        }
        asm volatile("" ::: "memory");
        o0 = *(LAS u32x4*)L(tr);
        o1 = *(LAS u32x4*)L(tr + 1024);
      }
      SCHED0();
    }
    if (c == 15 && j == 0 && wm == 0) {
      const u64 e = P.epoch + 1;
      __builtin_amdgcn_raw_buffer_store_b64(u32x2{(u32)e, (u32)(e >> 32)}, wsr, lane == 0 ? 0 : WS_END, 0, SC1);
    }
  }
}

__device__ __forceinline__ void phase2_m(const Args& a, int c, int j, int wave, int lane, u32 ta, u32 tb, u32x4 (&w)[5][5]) {
  const int wm = wave & 3, li = lane & 15, q = lane >> 4;
  const rsrc_t wsr = mk_rsrc(a.ws, WS_END);
  const int rh = j & 1, i2 = j >> 1, dcol0 = i2 * 80;
  STAMP(6);
  const u32 wo = WS_WT + (i2 * 54 + wm * 14) * 5120;
  constexpr int BW = GBW;
  auto ldw = [&](int s) {
#pragma unroll
    for (int n = 0; n < 5; n++) w[s % BW][n] = gld(wsr, lane * 16, wo + (((s < 9 || wm < 3) ? s + 3 : 0) * 5 + n) * 1024);
  };
  asm volatile("" ::: "memory");
#pragma unroll
  for (int s = 0; s < WPRE; s++) ldw(s);
  SCHED0();
  wait_vm<5 * WPRE>();
  if (cnt_add(C_F1, lane) == 3) put_flag(wsr, WS_F1 + (c * 16 + j) * 128, ta, tb, lane);
  cnt_add(C_B2, lane);
#pragma unroll
  for (int s = WPRE; s < BW; s++) ldw(s);
  SCHED0();
  __builtin_amdgcn_s_setprio(GPRIO);
  STAMP(5);
  const int tt = 4 * rh + wm, r = lane >> 2;
  u32 oo = (c * 128 + (tt >> 1) * 32 + 8 * (r >> 2) + 4 * (tt & 1) + (r & 3)) * 640 + dcol0 + (lane & 3) * 4;
  u32x2 xv[5], sk[5];
  {
#pragma unroll
    for (int n = 0; n < 5; n++) {
      xv[n] = *(const GAS u32x2*)((const GAS u16*)a.x + oo + 16 * n);
      sk[n] = *(const GAS u32x2*)((const GAS u16*)a.dsk + dcol0 + (lane & 3) * 4 + 16 * n);
    }
    const f32x2 sr = ldc(LD_ST2 + (tt * 16 + r) * 8);
#pragma unroll
    for (int n = 0; n < 5; n++) {
      const f32x4 t = rbf4(rbf4(unpk4(xv[n]) - sr.x) * sr.y) * unpk4(sk[n]);
      sk[n] = u32x2{pk2(t.x, t.y), pk2(t.z, t.w)};
      PIN(sk[n]); PIN(xv[n]);
    }
    PIN(oo);
  }
  polln(wsr, WS_F1 + (c * 16 + 4 * wm) * 128, 3, ta, tb, lane);
  STAMP(7);
  f32x4 g[4][5];
  {
    u32 wbase = wm * 5120 + lane * 16;
    PIN(wbase);
    constexpr int AW = GAW;
    u32x4 hf[AW][4];
    const u32 hb = WS_H1 + ((c * 8 + 4 * rh) * 54 + wm * 14) * 1024 + lane * 16;
    auto lda = [&](int s) {
#pragma unroll
      for (int m = 0; m < 4; m++)
        hf[s % AW][m] = gld(wsr, hb, (((m + wm) & 3) * 54 + (s >= 11 ? s - 11 : (s < 9 || wm < 3) ? s + 3 : 0)) * 1024);
    };
#pragma unroll
    for (int s = 0; s < AW; s++) lda(s);
    SCHED0();
#pragma unroll
    for (int m = 0; m < 4; m++)
#pragma unroll
      for (int n = 0; n < 5; n++) g[m][n] = f32x4{0.f, 0.f, 0.f, 0.f};
    STAMPJ(1);
#pragma unroll
    for (int s = 0; s < 14; s++) {
      if (s < 9 || s > 10 || wm < 3) {
#pragma unroll
        for (int n = 0; n < 5; n++) mfma4(g[0][n], g[1][n], g[2][n], g[3][n], w[s % BW][n], hf[s % AW]);
      }
      SCHED0();
      if (s >= 3) STAMPJ(s - 1);
      if (s + AW < 14) lda(s + AW);
      if (s + BW < 11) ldw(s + BW);
      else if (s + BW < 14) {
        if (s + BW == 11) cnt_wait(C_B2, 8);
#pragma unroll
        for (int n = 0; n < 5; n++) w[s % BW][n] = __builtin_bit_cast(u32x4, ld128(wbase + ((s + BW - 11) * 20480 + n * 1024)));
      }
      SCHED0();
    }
  }
  mfma_done();
  STAMP(8);
#pragma unroll
  for (int m = 1; m < 4; m++)
#pragma unroll
    for (int n = 0; n < 5; n++) *(LAS f32x4*)L(((m - 1) * 4 + wm) * 5120 + n * 1024 + lane * 16) = g[m][n];
  cnt_add(C_G, lane);
  const float rstd = *(LAS float*)L(LD_ST2 + (tt * 16 + li) * 8 + 4);
  cnt_wait(C_G, 4);
  STAMP(9);
  STAMP(10);
  const u32 gq = ((wm + 3) & 3) * 5120;
#pragma unroll
  for (int n = 0; n < 5; n++) {
    const u32 gp = n * 1024 + lane * 16;
    g[0][n] = (g[0][n] + ld4(gp + gq)) + (ld4(gp + (4 + ((wm + 2) & 3)) * 5120) + ld4(gp + (8 + ((wm + 1) & 3)) * 5120));
  }
  asm volatile("" ::: "memory");
#pragma unroll
  for (int n = 0; n < 5; n++) {
    const f32x4 gb = rbf4(g[0][n] * rstd);
    f32x4 sg;
#pragma unroll
    for (int e2 = 0; e2 < 4; e2++) sg[e2] = gb[e2] * __builtin_amdgcn_rcpf(1.0f + __builtin_amdgcn_exp2f(gb[e2] * -1.4426950408889634f));
    *(LAS u32x2*)L(gq + n * 512 + li * 32 + q * 8) = u32x2{pk2(sg.x, sg.y), pk2(sg.z, sg.w)};
  }
  asm volatile("" ::: "memory");
  f32x4 fa[5], fb[5];
#pragma unroll
  for (int n = 0; n < 5; n++) {
    const f32x4 gt = unpk4(*(LAS u32x2*)L(gq + n * 512 + lane * 8));
    fa[n] = unpk4(xv[n]) + unpk4(sk[n]) * gt;
    fb[n] = gt + gt;
    PIN(fa[n]); PIN(fb[n]);
  }
  STAMP(11);
  cnt_wait(C_Y, 4);
  STAMP(12);
  const rsrc_t outr = mk_rsrc(a.out, 2048 * 640 * 4);
#pragma unroll
  for (int n = 0; n < 5; n++) {
    const u32 yp = (3 + wm) * 20480 + n * 1024 + (lane >> 2) * 64 + ((lane ^ (lane >> 3)) & 3) * 16;
    const f32x4 y = (ld4(yp) + ld4(yp + 5120)) + (ld4(yp + 10240) + ld4(yp + 15360));
    __builtin_amdgcn_raw_buffer_store_b128(__builtin_bit_cast(u32x4, fa[n] + fb[n] * y), outr, oo * 4 + n * 64, 0, OAUX);
  }
  STAMP(13);
}

__device__ __forceinline__ void phase2_s(const Args& a, int c, int j, int wave, int lane, u32 ta, u32 tb, u32x4 (&cf)[5][5]) {
  const int wm = wave & 3;
  const rsrc_t wsr = mk_rsrc(a.ws, WS_END);
  const int rh = j & 1;
  STAMP(7);
  asm volatile("" ::: "memory");
#pragma unroll
  for (int k = CPRE; k < 5; k++)
#pragma unroll
    for (int n = 0; n < 5; n++) cf[k][n] = gld(wsr, lane * 16, WS_CT + ((j >> 1) * 200 + wm * 50 + k * 5 + n) * 1024);
  SCHED0();
  wait_vm<25 - 5 * CPRE>();
  if (cnt_add(C_F2, lane) == 3) put_flag(wsr, WS_F2 + (c * 16 + j) * 128, ta, tb, lane);
  STAMP(8);
  f32x4 y[4][5];
  {
    u32 cb0 = (12 + wm) * 5120 + lane * 16, cb1 = cb0 + 40960;
    PIN(cb0); PIN(cb1);
    cnt_wait(C_B2, 8);
    polln(wsr, WS_F2 + (c * 16 + 4 * wm) * 128, 3, ta, tb, lane);
    __builtin_amdgcn_s_setprio(YPRIO);
    STAMP(9);
    STAMPJ(1);
    constexpr int AW = YAW;
    u32x4 hf[AW][4];
    const u32 hb = WS_HS + ((c * 8 + 4 * rh) * 40 + wm * 10) * 1024 + lane * 16;
#pragma unroll
    for (int k = 0; k < AW; k++)
#pragma unroll
      for (int m = 0; m < 4; m++) hf[k][m] = gld(wsr, hb, (m * 40 + k) * 1024);
    SCHED0();
#pragma unroll
    for (int m = 0; m < 4; m++)
#pragma unroll
      for (int n = 0; n < 5; n++) y[m][n] = f32x4{0.f, 0.f, 0.f, 0.f};
#pragma unroll
    for (int k = 0; k < 10; k++) {
      const int r = k < 5 ? k : k < 9 ? k - 4 : 0;
#pragma unroll
      for (int n = 0; n < 5; n++) mfma4(y[0][n], y[1][n], y[2][n], y[3][n], cf[r][n], hf[k % AW]);
      SCHED0();
      STAMPJ(2 + k);
      if (k + AW < 10) {
#pragma unroll
        for (int m = 0; m < 4; m++) hf[k % AW][m] = gld(wsr, hb, (m * 40 + k + AW) * 1024);
      }
#pragma unroll
      for (int n = 0; n < 5; n++) {
        if (k == 0) cf[0][n] = gld(wsr, lane * 16, WS_CT + ((j >> 1) * 200 + wm * 50 + 45 + n) * 1024);
        else if (k < 5) cf[k][n] = __builtin_bit_cast(u32x4, ld128((k < 3 ? cb0 : cb1) + (((k - 1) & 1) * 20480 + n * 1024)));
      }
      SCHED0();
    }
  }
  mfma_done();
  STAMP(10);
#pragma unroll
  for (int m = 0; m < 4; m++)
#pragma unroll
    for (int n = 0; n < 5; n++) *(LAS f32x4*)L(((3 + m) * 4 + wm) * 5120 + n * 1024 + (lane & 15) * 64 + (((lane >> 4) ^ (lane >> 1)) & 3) * 16) = y[m][n];
  cnt_add(C_Y, lane);
  STAMP(11);
}

__global__ __launch_bounds__(512, 2) void s5_fused(Args a) {
  extern __shared__ __attribute__((aligned(16))) char smem[];
  const int b = blockIdx.x, xcd = b & 7, r = b >> 3;
  const int c = (xcd & 3) * 4 + (r >> 3), j = (xcd >> 2) * 8 + (r & 7);
  const int wave = __builtin_amdgcn_readfirstlane(threadIdx.x >> 6), lane = threadIdx.x & 63;
  if ((u32)(size_t)(lptr)smem != 0) return;
  const u64 pc = __builtin_amdgcn_s_getpc() & ~(u64)127;
  STAMP(0);
  Pre P;
  u32 ta, tb;
  head(a, P, pc, c, j, wave, lane, ta, tb);
  u32x4 cf[5][5];
  if (wave >= 4) {
    if (j & 1) phase1<true, 4>(a, P, c, j, wave, lane, ta, tb, cf);
    else phase1<true, 3>(a, P, c, j, wave, lane, ta, tb, cf);
    phase2_m(a, c, j, wave, lane, ta, tb, cf);
  } else {
    if (j & 1) phase1<false, 2>(a, P, c, j, wave, lane, ta, tb, cf);
    else phase1<false, 3>(a, P, c, j, wave, lane, ta, tb, cf);
    phase2_s(a, c, j, wave, lane, ta, tb, cf);
  }
}
}

extern "C" void kernel_launch(void* const* d_in, const int* in_sizes, int n_in, void* d_out, int out_size, void* d_ws,
                              size_t ws_size, hipStream_t stream) {
  using namespace s5k;
  Args a;
  a.x = (const u16*)d_in[0];
  a.lre = (const u16*)d_in[1];
  a.lim = (const u16*)d_in[2];
  a.bre = (const u16*)d_in[3];
  a.bim = (const u16*)d_in[4];
  a.cre = (const u16*)d_in[5];
  a.cim = (const u16*)d_in[6];
  a.ldt = (const u16*)d_in[7];
  a.dsk = (const u16*)d_in[8];
  a.w1 = (const u16*)d_in[10];
  a.w2 = (const u16*)d_in[11];
  a.out = (float*)d_out;
  a.ws = (char*)d_ws;
  (void)hipFuncSetAttribute((const void*)s5_fused, hipFuncAttributeMaxDynamicSharedMemorySize, LDS_BYTES);
  s5_fused<<<256, 512, LDS_BYTES, stream>>>(a);
}
```

```cpp
#define TLBW 344064
#include <hip/hip_runtime.h>

#define STAMPJ(id)
namespace s5k {
typedef unsigned short u16;
typedef unsigned int u32;
typedef unsigned long long u64;
typedef short bf16x8 __attribute__((ext_vector_type(8)));
typedef short s16x2 __attribute__((ext_vector_type(2)));
typedef float f32x4 __attribute__((ext_vector_type(4)));
typedef float f32x2 __attribute__((ext_vector_type(2)));
typedef u32 u32x4 __attribute__((ext_vector_type(4)));
typedef u32 u32x2 __attribute__((ext_vector_type(2)));
typedef __bf16 bf16x2 __attribute__((ext_vector_type(2)));
#define LAS __attribute__((address_space(3)))
#define GAS __attribute__((address_space(1)))
typedef LAS char* lptr;
typedef __amdgpu_buffer_rsrc_t rsrc_t;

constexpr u32 WS_STATS = 4096;
constexpr u32 WS_AGG = 65536;
constexpr u32 WS_F1 = 262144, WS_F2 = WS_F1 + 32768, WS_F3 = WS_F2 + 32768;
constexpr u32 WS_PROF = 512 << 10;
constexpr u32 WS_H1 = 1u << 20;
constexpr u32 WS_HS = 9u << 20;
constexpr u32 WS_CT = 16u << 20;
constexpr u32 WS_WT = 18u << 20;
constexpr u32 WS_END = 21u << 20;
constexpr int CHUNK = 40960;
constexpr int LD_TRB = 143360, LD_STG = LD_TRB + 4096, LD_ST2 = LD_TRB + 8192, LD_CS = LD_ST2 + 1024, LD_AGW = LD_CS + 1024,
              LD_CIN = LD_AGW + 1536, LD_CNT = LD_CIN + 1536, LD_TAB = LD_CNT + 256, TABP = 144, LD_END = LD_TAB + 48 * TABP;
constexpr int LDS_BYTES = 163840;
static_assert(LD_END <= LDS_BYTES, "lds");
enum { T_A = 0, T_C = 1, T_8 = 2, T_32 = 6, T_128 = 10, T_512 = 14 };
enum { C_F1 = 0, C_B2 = 1, C_S1 = 2, C_S2 = 3, C_F2 = 4, C_G = 5, C_G2 = 6, C_Y = 7 };
constexpr int SC1 = 16;
#define MPRIO 1
#define SPRIO 0
#define CPRE 5
#define MPRIO2 2
#define SPRIO2 1
#define CODE_BYTES 74880
#define CPF_PARTS 9
#define YPRIO 0
#define GAW 3
#define GBD 0
#define WPRE 3
#define GPRIO 1
#define C1EARLY 0
#define CTAIL 2
#define WEARLY 0
#define SPRIO3 2
#define PSLEEP 2
#define OAUX 2
#define GBW 3
#define YAW 3
#define TPRIO GPRIO
#define GMAP 4
constexpr u32 SPIN_CAP = 1u << 16;

struct Args {
  const u16 *x, *lre, *lim, *bre, *bim, *cre, *cim, *ldt, *dsk, *w1, *w2;
  float* out;
  char* ws;
};

__device__ __forceinline__ float u2f(u32 v) { return __builtin_bit_cast(float, v); }
__device__ __forceinline__ u32 f2u(float v) { return __builtin_bit_cast(u32, v); }
__device__ __forceinline__ float bf2f(u32 v) { return __builtin_bit_cast(float, v << 16); }
__device__ __forceinline__ float bfhi(u32 v) { return __builtin_bit_cast(float, v & 0xffff0000u); }
__device__ __forceinline__ u32 pk2(float a, float b) { return __builtin_bit_cast(u32, __builtin_convertvector(f32x2{a, b}, bf16x2)); }
__device__ __forceinline__ float rbf(float a) { return bf2f(pk2(a, a)); }
__device__ __forceinline__ f32x4 unpk4(u32x2 v) { return f32x4{bf2f(v.x), bfhi(v.x), bf2f(v.y), bfhi(v.y)}; }
__device__ __forceinline__ f32x4 rbf4(f32x4 v) { return unpk4(u32x2{pk2(v.x, v.y), pk2(v.z, v.w)}); }
__device__ __forceinline__ u32 relu2(u32 v) {
  return __builtin_bit_cast(u32, __builtin_elementwise_max(__builtin_bit_cast(s16x2, v), s16x2{0, 0}));
}
__device__ __forceinline__ f32x2 cmul(f32x2 a, f32x2 b) {
  float x, y;
  asm("v_mul_f32 %0, %2, %4\n\tv_mul_f32 %1, %2, %5\n\tv_fma_f32 %0, -%3, %5, %0\n\tv_fma_f32 %1, %3, %4, %1"
      : "=&v"(x), "=&v"(y) : "v"(a.x), "v"(a.y), "v"(b.x), "v"(b.y));
  return f32x2{x, y};
}
__device__ __forceinline__ f32x2 cfma(f32x2 a, f32x2 b, f32x2 c) {
  float x, y;
  asm("v_fma_f32 %0, %2, %4, %6\n\tv_fma_f32 %1, %2, %5, %7\n\tv_fma_f32 %0, -%3, %5, %0\n\tv_fma_f32 %1, %3, %4, %1"
      : "=&v"(x), "=&v"(y) : "v"(a.x), "v"(a.y), "v"(b.x), "v"(b.y), "v"(c.x), "v"(c.y));
  return f32x2{x, y};
}
__device__ __forceinline__ rsrc_t mk_rsrc(const void* p, u32 bytes) { return __builtin_amdgcn_make_buffer_rsrc((void*)p, 0, bytes, 0x00020000); }
__device__ __forceinline__ f32x4 mfma(bf16x8 a, bf16x8 b, f32x4 c) { return __builtin_amdgcn_mfma_f32_16x16x32_bf16(a, b, c, 0, 0, 0); }
__device__ __forceinline__ void mfma4(f32x4& c0, f32x4& c1, f32x4& c2, f32x4& c3, u32x4 a, const u32x4 (&b)[4]) {
  asm volatile("s_nop 1\n\tv_mfma_f32_16x16x32_bf16 %0, %4, %5, %0\n\tv_mfma_f32_16x16x32_bf16 %1, %4, %6, %1\n\t"
               "v_mfma_f32_16x16x32_bf16 %2, %4, %7, %2\n\tv_mfma_f32_16x16x32_bf16 %3, %4, %8, %3"
               : "+v"(c0), "+v"(c1), "+v"(c2), "+v"(c3) : "v"(a), "v"(b[0]), "v"(b[1]), "v"(b[2]), "v"(b[3]));
}
__device__ __forceinline__ void mfma_done() {
  __builtin_amdgcn_sched_barrier(0);
  asm volatile("s_nop 7\n\ts_nop 3" ::: "memory");
  __builtin_amdgcn_sched_barrier(0);
}
__device__ __forceinline__ bf16x8 asfrag(u32x4 v) { return __builtin_bit_cast(bf16x8, v); }
__device__ __forceinline__ void dma16(rsrc_t rs, u32 dst, u32 voff, u32 soff) {
  asm volatile("s_mov_b32 m0, %0\n\ts_nop 0\n\tbuffer_load_dwordx4 %1, %2, %3 offen lds" ::"s"(dst), "v"(voff), "s"(rs), "s"(soff) : "memory");
}
__device__ __forceinline__ void dma16_sc1(rsrc_t rs, u32 dst, u32 voff, u32 soff) {
  asm volatile("s_mov_b32 m0, %0\n\ts_nop 0\n\tbuffer_load_dwordx4 %1, %2, %3 offen sc1 lds" ::"s"(dst), "v"(voff), "s"(rs), "s"(soff) : "memory");
}
template <int DOFF> __device__ __forceinline__ void gdma(const char* base, u32 dst, u32 voff) {
  asm volatile("s_add_i32 m0, %0, %1\n\ts_nop 0\n\tglobal_load_lds_dwordx4 %2, %3 sc1" ::"s"(dst), "i"(DOFF), "v"(voff), "s"(base) : "memory", "scc");
}
__device__ __forceinline__ void dma4_sc1(rsrc_t rs, u32 dst, u32 voff, u32 soff) {
  asm volatile("s_mov_b32 m0, %0\n\ts_nop 0\n\tbuffer_load_dword %1, %2, %3 offen sc1 lds" ::"s"(dst), "v"(voff), "s"(rs), "s"(soff) : "memory");
}
__device__ __forceinline__ void dma4_plain(rsrc_t rs, u32 dst, u32 voff, u32 soff) {
  asm volatile("s_mov_b32 m0, %0\n\ts_nop 0\n\tbuffer_load_dword %1, %2, %3 offen lds" ::"s"(dst), "v"(voff), "s"(rs), "s"(soff) : "memory");
}
__device__ __forceinline__ lptr L(u32 addr) { return (lptr)(size_t)addr; }
__device__ __forceinline__ bf16x8 ld128(u32 p) { return *(LAS bf16x8*)L(p); }
__device__ __forceinline__ f32x2 ldc(u32 p) { return *(LAS f32x2*)L(p); }
__device__ __forceinline__ f32x4 ld4(u32 p) { return *(LAS f32x4*)L(p); }
__device__ __forceinline__ bf16x8 ldtr(u32 p0, u32 p1) {
  typedef short v4i16 __attribute__((ext_vector_type(4)));
  v4i16 a = __builtin_amdgcn_ds_read_tr16_b64_v4i16((LAS v4i16*)L(p0));
  v4i16 b = __builtin_amdgcn_ds_read_tr16_b64_v4i16((LAS v4i16*)L(p1));
  return __builtin_shufflevector(a, b, 0, 1, 2, 3, 4, 5, 6, 7);
}
__device__ __forceinline__ u32x4 gld(rsrc_t rs, u32 voff, u32 soff) { return __builtin_amdgcn_raw_buffer_load_b128(rs, voff, soff, SC1); }
__device__ __forceinline__ void gst(u32x4 v, rsrc_t rs, u32 voff) { __builtin_amdgcn_raw_buffer_store_b128(v, rs, voff, 0, SC1); }
template <int N> __device__ __forceinline__ void wait_vm() { asm volatile("s_waitcnt vmcnt(%0)" ::"i"(N) : "memory"); }
__device__ __forceinline__ void wait_lgkm0() { __builtin_amdgcn_s_waitcnt(0xc07f); asm volatile("" ::: "memory"); }
__device__ __forceinline__ void wg_barrier() {
  asm volatile("" ::: "memory");
  __builtin_amdgcn_s_barrier();
  asm volatile("" ::: "memory");
}
#define PIN(x) asm volatile("" : "+v"(x))
#define SCHED0() __builtin_amdgcn_sched_barrier(0)
__device__ __forceinline__ u32 cnt_add(int idx, int lane) {
  u32 old;
  const u32 addr = LD_CNT + idx * 4 + (lane == 0 ? 0 : 64), one = 1;
  asm volatile("s_waitcnt lgkmcnt(0)\n\ts_mov_b64 exec, 1\n\tds_add_rtn_u32 %0, %1, %2\n\ts_mov_b64 exec, -1\n\ts_waitcnt lgkmcnt(0)"
               : "=&v"(old) : "v"(addr), "v"(one) : "memory");
  return __builtin_amdgcn_readfirstlane(old);
}
__device__ __forceinline__ void cnt_wait(int idx, u32 want) {
  const u32 addr = LD_CNT + idx * 4;
#pragma unroll 1
  for (u32 s = 0; s < (SPIN_CAP << 4); s++) {
    u32 v;
    asm volatile("ds_read_b32 %0, %1\n\ts_waitcnt lgkmcnt(0)" : "=&v"(v) : "v"(addr) : "memory");
    if (__builtin_amdgcn_readfirstlane(v) >= want) break;
    __builtin_amdgcn_s_sleep(1);
  }
}
__device__ __forceinline__ void polln(rsrc_t wsr, u32 off, int mask, u32 ta, u32 tb, int lane) {
  const u32 vo = off + lane * 128;
#pragma unroll 1
  for (u32 s = 0; s < SPIN_CAP; s++) {
    asm volatile("" ::: "memory");
    u32x2 v = {ta, tb};
    if (lane <= mask) v = __builtin_amdgcn_raw_buffer_load_b64(wsr, vo, 0, SC1);
    if (__all((v.x == ta) & (v.y == tb))) break;
    __builtin_amdgcn_s_sleep(PSLEEP);
  }
  __builtin_amdgcn_fence(__ATOMIC_ACQUIRE, "wavefront");
  asm volatile("" ::: "memory");
}
__device__ __forceinline__ void put_flag(rsrc_t wsr, u32 off, u32 ta, u32 tb, int lane) {
  __builtin_amdgcn_raw_buffer_store_b64(u32x2{ta, tb}, wsr, lane == 0 ? off : WS_END, 0, SC1);
}
template <int CTRL> __device__ __forceinline__ float dpp(float x) {
  return __builtin_bit_cast(float, __builtin_amdgcn_mov_dpp(__builtin_bit_cast(int, x), CTRL, 0xf, 0xf, true));
}
__device__ __forceinline__ void rows4(float x, float (&r)[4]) {
  auto s = __builtin_amdgcn_permlane16_swap(f2u(x), f2u(x), false, false);
  const u32 e = s[0], o = s[1];
  auto te = __builtin_amdgcn_permlane32_swap(e, e, false, false);
  auto to = __builtin_amdgcn_permlane32_swap(o, o, false, false);
  const u32 r0 = te[0], r2 = te[1], r1 = to[0], r3 = to[1];
  r[0] = u2f(r0); r[1] = u2f(r1); r[2] = u2f(r2); r[3] = u2f(r3);
}
__device__ __forceinline__ float rows_sum(float x) {
  auto s = __builtin_amdgcn_permlane16_swap(f2u(x), f2u(x), false, false);
  const u32 a = s[0], b = s[1];
  x = u2f(a) + u2f(b);
  auto t = __builtin_amdgcn_permlane32_swap(f2u(x), f2u(x), false, false);
  const u32 c = t[0], d = t[1];
  return u2f(c) + u2f(d);
}
__device__ __forceinline__ float wave_sum(float v) {
  v += dpp<0xB1>(v); v += dpp<0x4E>(v); v += dpp<0x124>(v); v += dpp<0x128>(v);
  return rows_sum(v);
}
__device__ __forceinline__ float fexp(float x) {
  const float hi = x * 1.4426950408889634f;
  const float lo = __builtin_fmaf(x, 1.4426950408889634f, -hi) + x * 1.925963033500011e-8f;
  return __builtin_amdgcn_exp2f(hi) * (1.0f + lo * 0.6931471805599453f);
}
__device__ __forceinline__ void fsincos(float x, float& sn, float& cs) {
  if (__builtin_expect(__any(!(__builtin_fabsf(x) < 2000.f)), 0)) { sincosf(x, &sn, &cs); return; }
  const float n = __builtin_rintf(x * 0.6366197723675814f);
  float r = __builtin_fmaf(n, -1.5703125f, x);
  r = __builtin_fmaf(n, -4.837512969970703125e-4f, r);
  r = __builtin_fmaf(n, -7.54978995489188e-8f, r);
  const float r2 = r * r;
  const float s = __builtin_fmaf(r * r2, __builtin_fmaf(r2, __builtin_fmaf(r2, -1.9515295891e-4f, 8.3321608736e-3f), -1.6666654611e-1f), r);
  const float c = __builtin_fmaf(r2 * r2, __builtin_fmaf(r2, __builtin_fmaf(r2, 2.443315711809948e-5f, -1.388731625493765e-3f), 4.166664568298827e-2f),
                                 __builtin_fmaf(r2, -0.5f, 1.0f));
  const int k = (int)n;
  const float s1 = (k & 1) ? c : s, c1 = (k & 1) ? s : c;
  sn = (k & 2) ? -s1 : s1;
  cs = ((k + 1) & 2) ? -c1 : c1;
}
#define STAMP(id) asm volatile("; MARK " #id)
#define STAMPI(id)

struct Pre {
  u64 epoch;
  const char* gb[5];
  u32 dvo[5], dstep[4];
  u32 dd0, dd1;
  int kc;
};
__device__ __forceinline__ void head(const Args& a, Pre& P, u64 pc, int c, int j, int wave, int lane, u32& ta, u32& tb) {
  const int li = lane & 15, r8 = lane >> 3, sl = lane & 7, i2 = j >> 1, tB = j & 1;
  const int nS = 3 - tB, nM = 3 + tB;
  const int s0 = 5 * i2 + 3 * tB;
  const int m0 = 7 * i2 + ((i2 & 1) ? 3 * tB : 4 - 4 * tB);
  int kc = c + j;
  kc -= kc >= 20 ? 20 : kc >= 10 ? 10 : 0;
  kc = kc == 10 ? 0 : kc;
  u64 pbre = (u64)a.bre, pbim = (u64)a.bim, pw1 = (u64)a.w1;
  asm volatile("" : "+s"(pbre), "+s"(pbim), "+s"(pw1) : "s"(a.x), "s"(a.ws), "s"(a.lre), "s"(a.lim), "s"(a.ldt));
  u32x2 ep;
  u32x4 xr0, xr1;
  u32 plr, pli, pdt;
  const GAS char* xrow = (const GAS char*)a.x + (size_t)(c * 128 + j * 8 + wave) * 1280;
  asm volatile("global_load_dwordx2 %0, %1, %2 sc1" : "=v"(ep) : "v"(0), "s"(a.ws) : "memory");
  asm volatile("global_load_dwordx4 %0, %1, %2" : "=v"(xr0) : "v"(lane * 16), "s"(xrow) : "memory");
  asm volatile("global_load_dwordx4 %0, %1, %2 offset:1024" : "=v"(xr1) : "v"(li * 16), "s"(xrow) : "memory");
  const int ch = (s0 * 16 + (lane < 16 * nS ? lane : li)) * 2;
  asm volatile("global_load_ushort %0, %1, %2" : "=v"(plr) : "v"(ch), "s"(a.lre) : "memory");
  asm volatile("global_load_ushort %0, %1, %2" : "=v"(pli) : "v"(ch), "s"(a.lim) : "memory");
  asm volatile("global_load_ushort %0, %1, %2" : "=v"(pdt) : "v"(ch), "s"(a.ldt) : "memory");
  P.dd0 = wave * 2048;
  P.dd1 = 16384 + wave * 3072;
  P.dstep[0] = 128;
#pragma unroll
  for (int b = 0; b < 2; b++) {
    P.gb[b] = (const char*)a.x + (c * 128 + (wave >> 1) * 32 + b * 16 + (wave & 1) * 4) * 1280;
    P.dvo[b] = (8 * (r8 >> 2) + (r8 & 3)) * 1280 + ((sl ^ (b * 4 + (r8 >> 1))) << 4);
  }
  gdma<0>(P.gb[0] + kc * 128, P.dd0, P.dvo[0]);
  gdma<1024>(P.gb[1] + kc * 128, P.dd0, P.dvo[1]);
#pragma unroll
  for (int t = 0; t < 3; t++) {
    const int k = wave * 3 + t, b = 2 + t;
    const bool isw = k >= 4 * nS;
    const int sub = k & 3;
    const u32 bvo = r8 * 1280 + ((sl ^ ((sub & 1) * 4 + (r8 >> 1))) << 4);
    const u32 idx = (k - 4 * nS) * 64 + lane;
    const u32 kr = (idx * (tB ? 32768u : 43691u)) >> 19, cpos = idx - kr * (4 * nM);
    const u32 sw = (kr & (3 * tB)) | (((kr >> 3) & 1) << (2 * tB));
    u32 col16 = m0 * 4 + (((cpos >> 1) ^ sw) << 1) + (cpos & 1);
    col16 -= col16 >= 216 ? 8 : 0;
    P.gb[b] = (const char*)(isw ? pw1 : sub < 2 ? pbre : pbim) + (isw ? 0 : ((s0 + (k >> 2)) * 16 + (sub & 1) * 8) * 1280);
    P.dvo[b] = isw ? kr * 3456 + col16 * 16 : bvo;
    P.dstep[b - 1] = isw ? 64 * 3456 : 128;
    if (t == 0) gdma<0>(P.gb[b] + kc * P.dstep[b - 1], P.dd1, P.dvo[b]);
    if (t == 1) gdma<1024>(P.gb[b] + kc * P.dstep[b - 1], P.dd1, P.dvo[b]);
    if (t == 2) gdma<2048>(P.gb[b] + kc * P.dstep[b - 1], P.dd1, P.dvo[b]);
  }
  kc = kc == 9 ? 0 : kc + 1;
  {
    const int part = (blockIdx.x >> 3) & 15;
    dma4_plain(mk_rsrc((const void*)pc, CODE_BYTES), LD_TRB + 6144, wave == 7 ? (part * 64 + lane) * 128 : CODE_BYTES, 0);
  }
  *(LAS u32*)L(LD_CNT + li * 4) = 0;
  const rsrc_t wsr = mk_rsrc(a.ws, WS_END);
  STAMP(1);
  auto issue1 = [&]() {
    gdma<CHUNK>(P.gb[0] + kc * 128, P.dd0, P.dvo[0]);
    gdma<CHUNK + 1024>(P.gb[1] + kc * 128, P.dd0, P.dvo[1]);
    gdma<CHUNK>(P.gb[2] + kc * P.dstep[1], P.dd1, P.dvo[2]);
    gdma<CHUNK + 1024>(P.gb[3] + kc * P.dstep[2], P.dd1, P.dvo[3]);
    gdma<CHUNK + 2048>(P.gb[4] + kc * P.dstep[3], P.dd1, P.dvo[4]);
  };
  asm volatile("s_waitcnt vmcnt(6)" : "+v"(ep), "+v"(xr0), "+v"(xr1), "+v"(plr), "+v"(pli), "+v"(pdt)::"memory");
  P.kc = kc == 9 ? 0 : kc + 1;
  {
    P.epoch = (u64)__builtin_amdgcn_readfirstlane(ep.y) << 32 | __builtin_amdgcn_readfirstlane(ep.x);
    u64 z = P.epoch + 0x9E3779B97F4A7C15ull;
    z = (z ^ (z >> 30)) * 0xBF58476D1CE4E5B9ull;
    z = (z ^ (z >> 27)) * 0x94D049BB133111EBull;
    z ^= z >> 31;
    ta = (u32)z;
    tb = (u32)(z >> 32);
    float v[16];
#pragma unroll
    for (int e = 0; e < 4; e++) {
      v[2 * e] = bf2f(xr0[e]); v[2 * e + 1] = bfhi(xr0[e]);
      v[8 + 2 * e] = bf2f(xr1[e]); v[8 + 2 * e + 1] = bfhi(xr1[e]);
    }
    float s0_ = 0.f, s1_ = 0.f;
#pragma unroll
    for (int e = 0; e < 8; e++) { s0_ += v[e]; s1_ += v[8 + e]; }
    const float mean = wave_sum(lane < 16 ? s0_ + s1_ : s0_) * (1.0f / 640.0f);
    s0_ = 0.f; s1_ = 0.f;
#pragma unroll
    for (int e = 0; e < 8; e++) {
      const float d0 = v[e] - mean, d1 = v[8 + e] - mean;
      s0_ += d0 * d0; s1_ += d1 * d1;
    }
    const float var = wave_sum(lane < 16 ? s0_ + s1_ : s0_) * (1.0f / 640.0f);
    const float mb = rbf(mean), den = rbf(__builtin_amdgcn_sqrtf(rbf(rbf(var) + 1e-5f)));
    gst(u32x4{f2u(mb), ta, f2u(__builtin_amdgcn_rcpf(den)), tb}, wsr, lane == 0 ? WS_STATS + (c * 128 + j * 8 + wave) * 16 : WS_END);
  }
  issue1();
  if (wave == 6) {
    dma4_plain(wsr, LD_TRB + 6400, lane * TLBW, 0);
    dma4_plain(mk_rsrc(a.out, 2048 * 640 * 4), LD_TRB + 6400, c * 128 * 2560 + lane * 8192 < (c + 1) * 128 * 2560 ? c * 128 * 2560 + lane * 8192 : 0x7fffffff, 0);
  }
  {
    if (wave == 0) {
      const float lr = bf2f(plr), lim = bf2f(pli);
      const float dt = rbf(fexp(bf2f(pdt)));
      const float er = fexp(dt * lr);
      float sn, cn;
      fsincos(dt * lim, sn, cn);
      const f32x2 a1 = cmul(f32x2{er, 0.f}, f32x2{cn, sn});
      const float nr = a1.x - 1.0f, dn = 1.0f / (lr * lr + lim * lim);
      const u32 tp = LD_TAB + (lane < 48 ? lane : 47) * TABP;
      if (lane < 16 * nS) {
        const f32x2 cf_ = cmul(f32x2{nr, a1.y}, f32x2{lr * dn, -lim * dn});
        *(LAS f32x4*)L(tp) = f32x4{a1.x, a1.y, cf_.x, cf_.y};
        const f32x2 a2 = cmul(a1, a1), a4 = cmul(a2, a2);
        f32x2 b = cmul(a4, a4);
#pragma unroll
        for (int i = 0; i < 4; i++) {
          const f32x2 b2 = cmul(b, b), b3 = cmul(b2, b);
          *(LAS f32x4*)L(tp + 16 + i * 32) = f32x4{1.f, 0.f, b.x, b.y};
          *(LAS f32x4*)L(tp + 32 + i * 32) = f32x4{b2.x, b2.y, b3.x, b3.y};
          b = cmul(b2, b2);
        }
      }
    }
  }
}

template <bool IS_M, int NP>
__device__ __forceinline__ void phase1(const Args& a, Pre& P, int c, int j, int wave, int lane, u32 ta, u32 tb, u32x4 (&cf)[5][5]) {
  constexpr int NU = 2 * NP;
  constexpr bool TYPE_B = IS_M ? (NP == 4) : (NP == 2);
  constexpr int nS = TYPE_B ? 2 : 3, nM = 6 - nS, MP = nM * 64;
  const int wm = wave & 3;
  const int i2 = j >> 1;
  const int s0 = 5 * i2 + (TYPE_B ? 3 : 0);
  const int m0 = 7 * i2 + ((i2 & 1) ? (TYPE_B ? 3 : 0) : (TYPE_B ? 0 : 4));
  const int li = lane & 15, q = lane >> 4;
  const rsrc_t wsr = mk_rsrc(a.ws, WS_END);
  u32 kc = P.kc;
  auto dma1 = [&](int bufoff, int b) {
    const char* src = P.gb[b] + kc * P.dstep[b < 2 ? 0 : b - 1];
    const u32 d = b < 2 ? P.dd0 : P.dd1;
#define GD(B, O) if (bufoff == B && (b < 2 ? b : b - 2) == O) gdma<B + O * 1024>(src, d, P.dvo[b]);
    GD(0, 0) GD(0, 1) GD(0, 2) GD(CHUNK, 0) GD(CHUNK, 1) GD(CHUNK, 2) GD(2 * CHUNK, 0) GD(2 * CHUNK, 1) GD(2 * CHUNK, 2)
#undef GD
  };
  const int rid = c * 2 + (j & 1);

  int pr[NP];
#pragma unroll
  for (int p = 0; p < NP; p++) pr[p] = (p + wm) % NP;
  u32 xb[2][2], wb[2][NU];
  {
    const u32 xo = li * 128 + ((q ^ (li >> 1)) << 4);
    const int qq = li >> 2;
    const u32 rowb = 16384 + nS * 4096 + (8 * q + qq) * MP + (lane & 3) * 8;
    const int f = TYPE_B ? (qq | ((q & 1) << 2)) : (q & 1);
#pragma unroll
    for (int h = 0; h < 2; h++) {
#pragma unroll
      for (int kk = 0; kk < 2; kk++) {
        xb[h][kk] = h * 2 * CHUNK + wm * 4096 + (xo ^ (kk << 6));
        PIN(xb[h][kk]);
      }
#pragma unroll
      for (int u = 0; u < NU; u++) {
        wb[h][u] = h * 2 * CHUNK + (IS_M ? rowb + (((2 * pr[u >> 1] + (u & 1)) ^ f) << 5) : 16384 + pr[u >> 1] * 4096 + (xo ^ ((u & 1) << 6)));
        PIN(wb[h][u]);
      }
    }
  }
  bf16x8 xf[2][2], wf[2][NU];
  auto read1 = [&](int set, int bufoff, int kk, int f) {
    const int h = bufoff == 2 * CHUNK, bo = h ? 0 : bufoff;
    if (f < 2) {
      xf[set][f] = ld128(xb[h][kk] + (bo + f * 2048));
    } else if (f < NU + 2) {
      const int u = f - 2;
      if (!IS_M) wf[set][u] = ld128(wb[h][(u & ~1) + kk] + (bo + (u & 1) * 2048));
      else wf[set][u] = ldtr(wb[h][u] + (bo + kk * 32 * MP), wb[h][u] + (bo + kk * 32 * MP + 4 * MP));
    }
  };
  f32x4 acc[2][NU], csum[2];
#pragma unroll
  for (int m = 0; m < 2; m++) {
#pragma unroll
    for (int u = 0; u < NU; u++) { acc[m][u] = f32x4{0.f, 0.f, 0.f, 0.f}; PIN(acc[m][u]); }
    csum[m] = f32x4{0.f, 0.f, 0.f, 0.f};
    PIN(csum[m]);
  }
  u32x4 ones4 = {0x3f803f80u, 0x3f803f80u, 0x3f803f80u, 0x3f803f80u};
  PIN(ones4);
  constexpr int G = NU + 1;
  constexpr int NDB = IS_M ? 2 : 3;
  auto dpos = [](int d) { return (2 * G * d + (IS_M ? G : 0)) / 5; };
  static_assert((2 * G * (NDB - 1) + (IS_M ? G : 0)) / 5 < G && (2 * G * NDB + (IS_M ? G : 0)) / 5 >= G, "split");
  auto step = [&](int set, int rbuf, int rkk, int dbuf, bool tail) {
#pragma unroll
    for (int g = 0; g < G; g++) {
      if (g > 0) {
        const int u = g - 1;
#pragma unroll
        for (int m = 0; m < 2; m++)
          acc[m][u] = IS_M ? mfma(wf[set][u], xf[set][m], acc[m][u]) : mfma(xf[set][m], wf[set][u], acc[m][u]);
      } else {
        csum[0] = mfma(asfrag(ones4), wf[set][0], csum[0]);
        csum[1] = mfma(asfrag(ones4), wf[set][1], csum[1]);
      }
      if (rbuf >= 0) {
        if (g == 0) { read1(set ^ 1, rbuf, rkk, 2); read1(set ^ 1, rbuf, rkk, 3); }
        else if (g == 1) { read1(set ^ 1, rbuf, rkk, 0); read1(set ^ 1, rbuf, rkk, 1); }
        else read1(set ^ 1, rbuf, rkk, g + 2);
      } else if (g == 3) {
        LAS float* csp = (LAS float*)L(LD_CS + (IS_M ? 512 : 0) + (wm < NP ? wm : 3) * 128 + (lane & 31) * 4);
        float e0 = csum[0].x, e1 = csum[1].x;
        PIN(e0); PIN(e1);
        if (wm < NP) *csp = (q & 1) ? e1 : e0;
      }
      if (!IS_M && tail) {
#pragma unroll
        for (int f = 0; f < 5 * CTAIL; f++)
          if (f * 2 * G / (5 * CTAIL) == (set == 0 ? g : g + G))
            cf[f / 5][f % 5] = gld(wsr, lane * 16, WS_CT + (i2 * 200 + wm * 50 + f) * 1024);
      }
#pragma unroll
      for (int d = 0; d < 5; d++)
        if ((set == 1 ? g : g + G) == dpos(d)) dma1(dbuf, d);
      SCHED0();
    }
  };

#pragma unroll
  for (int d = 0; d < NDB; d++) dma1(2 * CHUNK, d);
  u32x4 rt[2];
  const int rks = min(rid * 2 + (wm & 1), 53);
  if (!IS_M) {
#pragma unroll
    for (int i = 0; i < 2; i++) {
      const int f = min(rid * 8 + wm * 2 + i, 199);
      const int wk = f / 5, n = f - wk * 5, w = wk / 10, k = wk - w * 10;
      const GAS char* src = (const GAS char*)(k < 5 ? a.cre : a.cim) + (i2 * 80 + n * 16) * 1280 + (160 * w + 32 * (k < 5 ? k : k - 5)) * 2;
      asm volatile("global_load_dwordx4 %0, %1, %2" : "=v"(rt[i]) : "v"(li * 1280 + q * 16), "s"(src) : "memory");
    }
    wait_vm<7 + NDB + 2>();
  } else {
    wait_vm<7 + NDB>();
  }
  wait_lgkm0();
  wg_barrier();
  STAMP(2);
  __builtin_amdgcn_s_setprio(IS_M ? MPRIO : SPRIO);
#pragma unroll
  for (int f = 0; f < NU + 2; f++) read1(0, 0, 0, f);
  int it = 0;
  auto body = [&](int prv, int cur, int nxt) {
    step(0, cur, 1, prv, nxt < 0);
    kc = kc == 9 ? 0 : kc + 1;
    if (nxt < 0) {
      const int r = wave * 16 + li;
      u32x4 g = *(LAS u32x4*)L(LD_STG + r * 16);
      if (!__all((g.y == ta) & (g.w == tb))) {
#pragma unroll 1
        for (u32 s = 0; s < SPIN_CAP; s++) {
          asm volatile("" ::: "memory");
          g = gld(wsr, WS_STATS + (c * 128 + r) * 16, 0);
          if (__all((g.y == ta) & (g.w == tb))) break;
        }
      }
      const int t5 = r & 31;
      *(LAS f32x2*)L(LD_ST2 + ((r & ~31) + (((t5 >> 2) & 1) << 4) + ((t5 >> 3) << 2) + (t5 & 3)) * 8) = f32x2{u2f(g.x), u2f(g.z)};
    } else if (!IS_M && it == 0) wait_vm<7>();
    else if (IS_M && it == 1 && wm < 2) wait_vm<10>();
    else wait_vm<5>();
    wait_lgkm0();
    wg_barrier();
    STAMPI(3 + it);
    if (IS_M && it == 0 && wm < 2) {
      const rsrc_t w2r = mk_rsrc(a.w2, 1728 * 1280);
#pragma unroll
      for (int b = 0; b < 5; b++) {
        const int idx = b * 64 + lane, kr = idx / 10, cpos = idx - kr * 10;
        dma16(w2r, 3 * CHUNK + wm * 5120 + b * 1024, kr * 1280 + i2 * 160 + cpos * 16, rks * (32 * 1280));
      }
    }
    if (it == 2) {
      if (!IS_M) {
        asm volatile("" : "+v"(rt[0]), "+v"(rt[1]));
#pragma unroll
        for (int i = 0; i < 2; i++) gst(rt[i], wsr, WS_CT + (i2 * 200 + min(rid * 8 + wm * 2 + i, 199)) * 1024 + lane * 16);
      } else if (wm < 2) {
        const u32 p = 3 * CHUNK + wm * 5120 + (4 * q + (li >> 2)) * 160 + (lane & 3) * 8;
#pragma unroll
        for (int n = 0; n < 5; n++)
          gst(__builtin_bit_cast(u32x4, ldtr(p + n * 32, p + n * 32 + 2560)), wsr, WS_WT + ((i2 * 54 + rks) * 5 + n) * 1024 + lane * 16);
      }
    }
    if (IS_M && it == 3 && wm == 3) put_flag(wsr, WS_F3 + (i2 * 32 + rid) * 128, ta, tb, lane);
    if (it >= 7) {
      if (it == 7) {
        const u32 f = *(LAS u32*)L(LD_TRB + wave * 256 + lane * 4);
        if (!__all(f == ((lane & 1) ? tb : ta))) polln(wsr, WS_F3 + i2 * 32 * 128, 31, ta, tb, lane);
#pragma unroll
        for (int b = 0; b < 5; b++) {
          P.gb[b] = a.ws + b * 1024;
          P.dvo[b] = lane * 16;
          if (b < 4) P.dstep[b] = 1024;
        }
        P.dd0 = wave * 5120;
        P.dd1 = wave * 5120 + 2048;
      }
      const int lk = (cur / CHUNK) * 2 + (IS_M ? 1 : 0);
      kc = lk < 3 ? (WS_WT >> 10) + (i2 * 54 + wm * 14 + lk) * 5 : (WS_CT >> 10) + i2 * 200 + wm * 50 + 10 + lk * 5;
    }
    SCHED0();
    step(1, nxt, 0, cur, nxt < 0);
    if (IS_M && it == 8) {
#pragma unroll
      for (int b = 0; b < 5; b++) dma16_sc1(wsr, (24 + wm) * 5120 + b * 1024, lane * 16, WS_CT + (i2 * 200 + wm * 50 + 40 + b) * 1024);
    }
    if (it == 5) {
      dma4_sc1(wsr, LD_TRB + wave * 256, (lane >> 1) * 128 + (lane & 1) * 4, WS_F3 + i2 * 32 * 128);
      dma4_sc1(wsr, LD_STG + wave * 256, lane * 4, WS_STATS + c * 2048 + wave * 256);
    }
    it++;
  };
#pragma unroll 1
  for (int r = 0; r < 3; r++) {
    body(2 * CHUNK, 0, CHUNK);
    body(0, CHUNK, 2 * CHUNK);
    body(CHUNK, 2 * CHUNK, 0);
  }
  body(2 * CHUNK, 0, -1);
#pragma unroll
  for (int d = NDB; d < 5; d++) dma1(0, d);
  __builtin_amdgcn_s_setprio(IS_M ? MPRIO2 : SPRIO2);
  STAMP(3);
  float mb[2];
  f32x4 st[4];
  f32x2 ca[NP], cc[NP], a8r[NP];
  u32 tp[NP];
  if (IS_M) {
#pragma unroll
    for (int m = 0; m < 2; m++) mb[m] = *(LAS float*)L(LD_ST2 + (wm * 32 + m * 16 + li) * 8);
  } else {
#pragma unroll
    for (int i = 0; i < 4; i++) st[i] = ld4(LD_ST2 + (wm * 32 + (i >> 1) * 16 + 4 * q + 2 * (i & 1)) * 8);
#pragma unroll
    for (int p = 0; p < NP; p++) {
      tp[p] = LD_TAB + (pr[p] * 16 + li) * TABP;
      const f32x4 t0 = ld4(tp[p]);
      ca[p] = f32x2{t0.x, t0.y};
      cc[p] = f32x2{t0.z, t0.w};
      a8r[p] = ldc(tp[p] + (T_8 + 3 - q) * 8);
    }
  }
  wait_lgkm0();
  wg_barrier();
  STAMP(4);

  if (IS_M) {
#pragma unroll
    for (int p = 0; p < NP; p++) {
      const f32x4 c0 = ld4(LD_CS + 512 + pr[p] * 128 + q * 16), c1 = ld4(LD_CS + 512 + pr[p] * 128 + 64 + q * 16);
      const u32 off = (m0 + pr[p] < 54) ? WS_H1 + ((c * 8 + 2 * wm) * 54 + m0 + pr[p]) * 1024 : WS_END;
#pragma unroll
      for (int m = 0; m < 2; m++) {
        const f32x4 v0 = acc[m][2 * p] - mb[m] * c0, v1 = acc[m][2 * p + 1] - mb[m] * c1;
        gst(u32x4{relu2(pk2(v0.x, v0.y)), relu2(pk2(v0.z, v0.w)), relu2(pk2(v1.x, v1.y)), relu2(pk2(v1.z, v1.w))}, wsr,
            off + m * 54 * 1024 + lane * 16);
      }
    }
  } else {
    f32x2 u[NP][8];
    f32x2 seg[NP];
    float Sr[NP], Si[NP];
#pragma unroll
    for (int p = 0; p < NP; p++) {
      Sr[p] = *(LAS float*)L(LD_CS + pr[p] * 128 + li * 4);
      Si[p] = *(LAS float*)L(LD_CS + pr[p] * 128 + 64 + li * 4);
    }
#pragma unroll
    for (int p = 0; p < NP; p++) {
      f32x2 h;
#pragma unroll
      for (int t = 0; t < 8; t++) {
        const float mu = st[t >> 1][(t & 1) * 2], rs = st[t >> 1][(t & 1) * 2 + 1];
        u[p][t] = f32x2{(acc[t >> 2][2 * p][t & 3] - mu * Sr[p]) * rs, (acc[t >> 2][2 * p + 1][t & 3] - mu * Si[p]) * rs};
        h = t ? cfma(ca[p], h, u[p][t]) : u[p][0];
      }
      seg[p] = h;
      h = cmul(a8r[p], h);
      *(LAS f32x2*)L(LD_AGW + ((wm * 3 + pr[p]) * 16 + li) * 8) = f32x2{rows_sum(h.x), rows_sum(h.y)};
    }
    if (cnt_add(C_S1, lane) == 3) {
      const int ch = lane < 48 ? lane : 47;
      const f32x2 a32 = ldc(LD_TAB + ch * TABP + (T_32 + 1) * 8);
      f32x2 h = ldc(LD_AGW + ch * 8);
#pragma unroll
      for (int w = 1; w < 4; w++) h = cfma(a32, h, ldc(LD_AGW + (w * 48 + ch) * 8));
      gst(u32x4{f2u(h.x), ta, f2u(h.y), tb}, wsr, q < NP ? WS_AGG + (c * 640 + s0 * 16 + lane) * 16 : WS_END);
    }
    STAMP(5);
    __builtin_amdgcn_s_setprio(SPRIO3);
    const int cp = 4 * wm + q, e = c - 1 - cp;
    const u32 go = WS_AGG + (cp * 640 + s0 * 16 + li) * 16;
    u32x4 lg[NP];
    {
      const u32 co = WS_CT + (i2 * 200 + wm * 50) * 1024 + lane * 16;
#pragma unroll
      for (int k = CTAIL; k < CPRE; k++)
#pragma unroll
        for (int n = 0; n < 5; n++) cf[k][n] = gld(wsr, co, (k * 5 + n) * 1024);
      SCHED0();
    }
    cnt_wait(C_S1, 4);
    f32x2 aq[NP], pre[NP];
    u32 hso[NP];
    bool done = false;
    auto lb_issue = [&]() {
      asm volatile("" ::: "memory");
#pragma unroll
      for (int p = 0; p < NP; p++) lg[p] = gld(wsr, go, pr[p] * 256);
      SCHED0();
    };
    auto lb_check = [&]() {
      bool ok = true;
#pragma unroll
      for (int p = 0; p < NP; p++) ok = ok & (lg[p].y == ta) & (lg[p].w == tb);
      done = __all(ok | (e < 0));
    };
#pragma unroll
    for (int p = 0; p < NP; p++) {
      if (p == NP - 1) lb_issue();
      const f32x2 a32 = ldc(tp[p] + (T_32 + 1) * 8), a8q = ldc(tp[p] + (T_8 + q) * 8), aw = ldc(tp[p] + (T_32 + wm) * 8);
      f32x2 h = {0.f, 0.f};
#pragma unroll
      for (int w = 0; w < 3; w++) {
        const f32x2 g = ldc(LD_AGW + ((w * 3 + pr[p]) * 16 + li) * 8);
        if (w < wm) h = cfma(a32, h, g);
      }
#pragma unroll
      for (int t = 0; t < 8; t++) u[p][t] = cmul(cc[p], u[p][t]);
      {
        const f32x2 a8 = ldc(tp[p] + (T_8 + 1) * 8);
        float gr[4], gi[4];
        rows4(seg[p].x, gr); rows4(seg[p].y, gi);
#pragma unroll
        for (int w = 0; w < 3; w++) {
          const f32x2 t = cfma(a8, h, f32x2{gr[w], gi[w]});
          h.x = w < q ? t.x : h.x;
          h.y = w < q ? t.y : h.y;
          PIN(h);
        }
      }
      pre[p] = cmul(cc[p], h);
      aq[p] = cmul(cc[p], cmul(a8q, aw));
      const int cu = s0 + pr[p];
      hso[p] = WS_HS + ((c * 8 + 2 * wm) * 40 + (cu / 10) * 10 + ((cu % 10) >> 1) + (lane >> 5) * 5) * 1024 + (cu & 1) * 512 + (lane & 31) * 16;
      asm volatile("" : "+v"(pre[p]), "+v"(aq[p]), "+v"(hso[p]), "+v"(u[p][0]), "+v"(u[p][1]), "+v"(u[p][2]), "+v"(u[p][3]), "+v"(u[p][4]),
                   "+v"(u[p][5]), "+v"(u[p][6]), "+v"(u[p][7]));
    }
    u32 tw = LD_TRB + wm * 2048 + (li >> 3) * 256 + q * 64 + (li & 7) * 2, tr = LD_TRB + wm * 2048 + lane * 16;
    PIN(tw); PIN(tr);
    {
      f32x2 pw[NP];
#pragma unroll
      for (int p = 0; p < NP; p++) {
        pw[p] = cmul(ldc(tp[p] + (T_128 + (e & 3)) * 8), ldc(tp[p] + (T_512 + ((e >> 2) & 3)) * 8));
        PIN(pw[p]);
      }
      lb_check();
#pragma unroll 1
      for (u32 s = 0; s < SPIN_CAP && !done; s++) {
        lb_issue();
        lb_check();
      }
#pragma unroll
      for (int p = 0; p < NP; p++) {
        f32x2 h = cmul(pw[p], f32x2{u2f(lg[p].x), u2f(lg[p].z)});
        h.x = e < 0 ? 0.f : h.x;
        h.y = e < 0 ? 0.f : h.y;
        *(LAS f32x2*)L(LD_CIN + ((wm * 3 + pr[p]) * 16 + li) * 8) = f32x2{rows_sum(h.x), rows_sum(h.y)};
      }
      STAMP(12);
      cnt_add(C_S2, lane);
      cnt_add(C_B2, lane);
    }
    cnt_wait(C_S2, 4);
    STAMP(6);
    f32x2 hin[NP];
#pragma unroll
    for (int p = 0; p < NP; p++) {
      const u32 ci = LD_CIN + (pr[p] * 16 + li) * 8;
      hin[p] = (ldc(ci) + ldc(ci + 384)) + (ldc(ci + 768) + ldc(ci + 1152));
    }
    u32x4 o0, o1;
#pragma unroll
    for (int p = 0; p <= NP; p++) {
      u32 pk[8];
      if (p < NP) {
        f32x2 h = cfma(aq[p], hin[p], pre[p]);
#pragma unroll
        for (int t = 0; t < 8; t++) {
          h = cfma(ca[p], h, u[p][t]);
          pk[t] = pk2(h.x, -h.y);
        }
      }
      SCHED0();
      if (p > 0) {
        gst(o0, wsr, hso[p - 1]);
        gst(o1, wsr, hso[p - 1] + 40 * 1024);
      }
      if (p < NP) {
        asm volatile("" ::: "memory");
#pragma unroll
        for (int t = 0; t < 8; t++) {
          *(LAS u16*)L(tw + (t >> 2) * 1024 + (t & 3) * 16) = (u16)pk[t];
          *(LAS u16*)L(tw + (t >> 2) * 1024 + (t & 3) * 16 + 512) = (u16)(pk[t] >> 16);
        }
        asm volatile("" ::: "memory");
        o0 = *(LAS u32x4*)L(tr);
        o1 = *(LAS u32x4*)L(tr + 1024);
      }
      SCHED0();
    }
    if (c == 15 && j == 0 && wm == 0) {
      const u64 e = P.epoch + 1;
      __builtin_amdgcn_raw_buffer_store_b64(u32x2{(u32)e, (u32)(e >> 32)}, wsr, lane == 0 ? 0 : WS_END, 0, SC1);
    }
  }
}

__device__ __forceinline__ void phase2_m(const Args& a, int c, int j, int wave, int lane, u32 ta, u32 tb, u32x4 (&w)[5][5]) {
  const int wm = wave & 3, li = lane & 15, q = lane >> 4;
  const rsrc_t wsr = mk_rsrc(a.ws, WS_END);
  const int rh = j & 1, i2 = j >> 1, dcol0 = i2 * 80;
  STAMP(6);
  const u32 wo = WS_WT + (i2 * 54 + wm * 14) * 5120;
  constexpr int BW = GBW;
  auto ldw = [&](int s) {
#pragma unroll
    for (int n = 0; n < 5; n++) w[s % BW][n] = gld(wsr, lane * 16, wo + (((s < 9 || wm < 3) ? s + 3 : 0) * 5 + n) * 1024);
  };
  asm volatile("" ::: "memory");
#pragma unroll
  for (int s = 0; s < WPRE; s++) ldw(s);
  SCHED0();
  wait_vm<5 * WPRE>();
  if (cnt_add(C_F1, lane) == 3) put_flag(wsr, WS_F1 + (c * 16 + j) * 128, ta, tb, lane);
  cnt_add(C_B2, lane);
#pragma unroll
  for (int s = WPRE; s < BW; s++) ldw(s);
  SCHED0();
  __builtin_amdgcn_s_setprio(GPRIO);
  STAMP(5);
  const int tt = 4 * rh + wm, r = lane >> 2;
  u32 oo = (c * 128 + (tt >> 1) * 32 + 8 * (r >> 2) + 4 * (tt & 1) + (r & 3)) * 640 + dcol0 + (lane & 3) * 4;
  u32x2 xv[5], sk[5];
  {
#pragma unroll
    for (int n = 0; n < 5; n++) {
      xv[n] = *(const GAS u32x2*)((const GAS u16*)a.x + oo + 16 * n);
      sk[n] = *(const GAS u32x2*)((const GAS u16*)a.dsk + dcol0 + (lane & 3) * 4 + 16 * n);
    }
    const f32x2 sr = ldc(LD_ST2 + (tt * 16 + r) * 8);
#pragma unroll
    for (int n = 0; n < 5; n++) {
      const f32x4 t = rbf4(rbf4(unpk4(xv[n]) - sr.x) * sr.y) * unpk4(sk[n]);
      sk[n] = u32x2{pk2(t.x, t.y), pk2(t.z, t.w)};
      PIN(sk[n]); PIN(xv[n]);
    }
    PIN(oo);
    asm volatile("" ::"v"(sr));
  }
  polln(wsr, WS_F1 + (c * 16 + 4 * wm) * 128, 3, ta, tb, lane);
  STAMP(7);
  f32x4 g[4][5];
  {
    u32 wbase = wm * 5120 + lane * 16;
    PIN(wbase);
    constexpr int AW = GAW;
    u32x4 hf[AW][4];
    const u32 hb = WS_H1 + ((c * 8 + 4 * rh) * 54 + wm * 14) * 1024 + lane * 16;
    auto lda = [&](int s) {
#pragma unroll
      for (int m = 0; m < 4; m++)
        hf[s % AW][m] = gld(wsr, hb, (((m + wm) & 3) * 54 + (s >= 11 ? s - 11 : (s < 9 || wm < 3) ? s + 3 : 0)) * 1024);
    };
#pragma unroll
    for (int s = 0; s < AW; s++) lda(s);
    SCHED0();
#pragma unroll
    for (int m = 0; m < 4; m++)
#pragma unroll
      for (int n = 0; n < 5; n++) g[m][n] = f32x4{0.f, 0.f, 0.f, 0.f};
    STAMPJ(1);
#pragma unroll
    for (int s = 0; s < 14; s++) {
      if (s < 9 || s > 10 || wm < 3) {
#pragma unroll
        for (int n = 0; n < 5; n++) mfma4(g[0][n], g[1][n], g[2][n], g[3][n], w[s % BW][n], hf[s % AW]);
      }
      SCHED0();
      if (s >= 3) STAMPJ(s - 1);
      if (s + AW < 14) lda(s + AW);
      if (s + BW < 11) ldw(s + BW);
      else if (s + BW < 14) {
        if (s + BW == 11) cnt_wait(C_B2, 8);
#pragma unroll
        for (int n = 0; n < 5; n++) w[s % BW][n] = __builtin_bit_cast(u32x4, ld128(wbase + ((s + BW - 11) * 20480 + n * 1024)));
      }
      SCHED0();
    }
  }
  mfma_done();
  __builtin_amdgcn_s_setprio(TPRIO);
  STAMP(8);
#pragma unroll
  for (int m = 1; m < 4; m++)
#pragma unroll
    for (int n = 0; n < 5; n++) *(LAS f32x4*)L(((m - 1) * 4 + wm) * 5120 + n * 1024 + lane * 16) = g[m][n];
  cnt_add(C_G, lane);
  const float rstd = *(LAS float*)L(LD_ST2 + (tt * 16 + li) * 8 + 4);
  cnt_wait(C_G, 4);
  STAMP(9);
  STAMP(10);
  const u32 gq = ((wm + 3) & 3) * 5120;
#pragma unroll
  for (int n = 0; n < 5; n++) {
    const u32 gp = n * 1024 + lane * 16;
    g[0][n] = (g[0][n] + ld4(gp + gq)) + (ld4(gp + (4 + ((wm + 2) & 3)) * 5120) + ld4(gp + (8 + ((wm + 1) & 3)) * 5120));
  }
  asm volatile("" ::: "memory");
#pragma unroll
  for (int n = 0; n < 5; n++) {
    const f32x4 gb = rbf4(g[0][n] * rstd);
    f32x4 sg;
#pragma unroll
    for (int e2 = 0; e2 < 4; e2++) sg[e2] = gb[e2] * __builtin_amdgcn_rcpf(1.0f + __builtin_amdgcn_exp2f(gb[e2] * -1.4426950408889634f));
    *(LAS u32x2*)L(gq + n * 512 + li * 32 + q * 8) = u32x2{pk2(sg.x, sg.y), pk2(sg.z, sg.w)};
  }
  asm volatile("" ::"v"(rstd) : "memory");
  f32x4 fa[5], fb[5];
#pragma unroll
  for (int n = 0; n < 5; n++) {
    const f32x4 gt = unpk4(*(LAS u32x2*)L(gq + n * 512 + lane * 8));
    fa[n] = unpk4(xv[n]) + unpk4(sk[n]) * gt;
    fb[n] = gt + gt;
    PIN(fa[n]); PIN(fb[n]);
  }
  STAMP(11);
  cnt_wait(C_Y, 4);
  STAMP(12);
  const rsrc_t outr = mk_rsrc(a.out, 2048 * 640 * 4);
#pragma unroll
  for (int n = 0; n < 5; n++) {
    const u32 yp = (3 + wm) * 20480 + n * 1024 + (lane >> 2) * 64 + ((lane ^ (lane >> 3)) & 3) * 16;
    const f32x4 y = (ld4(yp) + ld4(yp + 5120)) + (ld4(yp + 10240) + ld4(yp + 15360));
    __builtin_amdgcn_raw_buffer_store_b128(__builtin_bit_cast(u32x4, fa[n] + fb[n] * y), outr, oo * 4 + n * 64, 0, OAUX);
  }
  STAMP(13);
}

__device__ __forceinline__ void phase2_s(const Args& a, int c, int j, int wave, int lane, u32 ta, u32 tb, u32x4 (&cf)[5][5]) {
  const int wm = wave & 3;
  const rsrc_t wsr = mk_rsrc(a.ws, WS_END);
  const int rh = j & 1;
  STAMP(7);
  asm volatile("" ::: "memory");
#pragma unroll
  for (int k = CPRE; k < 5; k++)
#pragma unroll
    for (int n = 0; n < 5; n++) cf[k][n] = gld(wsr, lane * 16, WS_CT + ((j >> 1) * 200 + wm * 50 + k * 5 + n) * 1024);
  SCHED0();
  wait_vm<25 - 5 * CPRE>();
  if (cnt_add(C_F2, lane) == 3) put_flag(wsr, WS_F2 + (c * 16 + j) * 128, ta, tb, lane);
  STAMP(8);
  f32x4 y[4][5];
  {
    u32 cb0 = (12 + wm) * 5120 + lane * 16, cb1 = cb0 + 40960;
    PIN(cb0); PIN(cb1);
    cnt_wait(C_B2, 8);
    polln(wsr, WS_F2 + (c * 16 + 4 * wm) * 128, 3, ta, tb, lane);
    __builtin_amdgcn_s_setprio(YPRIO);
    STAMP(9);
    STAMPJ(1);
    constexpr int AW = YAW;
    u32x4 hf[AW][4];
    const u32 hb = WS_HS + ((c * 8 + 4 * rh) * 40 + wm * 10) * 1024 + lane * 16;
#pragma unroll
    for (int k = 0; k < AW; k++)
#pragma unroll
      for (int m = 0; m < 4; m++) hf[k][m] = gld(wsr, hb, (m * 40 + k) * 1024);
    SCHED0();
#pragma unroll
    for (int m = 0; m < 4; m++)
#pragma unroll
      for (int n = 0; n < 5; n++) y[m][n] = f32x4{0.f, 0.f, 0.f, 0.f};
#pragma unroll
    for (int k = 0; k < 10; k++) {
      const int r = k < 5 ? k : k < 9 ? k - 4 : 0;
#pragma unroll
      for (int n = 0; n < 5; n++) mfma4(y[0][n], y[1][n], y[2][n], y[3][n], cf[r][n], hf[k % AW]);
      SCHED0();
      STAMPJ(2 + k);
      if (k + AW < 10) {
#pragma unroll
        for (int m = 0; m < 4; m++) hf[k % AW][m] = gld(wsr, hb, (m * 40 + k + AW) * 1024);
      }
#pragma unroll
      for (int n = 0; n < 5; n++) {
        if (k == 0) cf[0][n] = gld(wsr, lane * 16, WS_CT + ((j >> 1) * 200 + wm * 50 + 45 + n) * 1024);
        else if (k < 5) cf[k][n] = __builtin_bit_cast(u32x4, ld128((k < 3 ? cb0 : cb1) + (((k - 1) & 1) * 20480 + n * 1024)));
      }
      SCHED0();
    }
  }
  mfma_done();
  STAMP(10);
#pragma unroll
  for (int m = 0; m < 4; m++)
#pragma unroll
    for (int n = 0; n < 5; n++) *(LAS f32x4*)L(((3 + m) * 4 + wm) * 5120 + n * 1024 + (lane & 15) * 64 + (((lane >> 4) ^ (lane >> 1)) & 3) * 16) = y[m][n];
  cnt_add(C_Y, lane);
  STAMP(11);
}

__global__ __launch_bounds__(512, 2) void s5_fused(Args a) {
  extern __shared__ __attribute__((aligned(16))) char smem[];
  const int b = blockIdx.x, xcd = b & 7, r = b >> 3;
  const int c = (xcd & 3) * 4 + (r >> 3), j = (xcd >> 2) * 8 + (r & 7);
  const int wave = __builtin_amdgcn_readfirstlane(threadIdx.x >> 6), lane = threadIdx.x & 63;
  if ((u32)(size_t)(lptr)smem != 0) return;
  const u64 pc = __builtin_amdgcn_s_getpc() & ~(u64)127;
  STAMP(0);
  Pre P;
  u32 ta, tb;
  head(a, P, pc, c, j, wave, lane, ta, tb);
  u32x4 cf[5][5];
  if (wave >= 4) {
    if (j & 1) phase1<true, 4>(a, P, c, j, wave, lane, ta, tb, cf);
    else phase1<true, 3>(a, P, c, j, wave, lane, ta, tb, cf);
    phase2_m(a, c, j, wave, lane, ta, tb, cf);
  } else {
    if (j & 1) phase1<false, 2>(a, P, c, j, wave, lane, ta, tb, cf);
    else phase1<false, 3>(a, P, c, j, wave, lane, ta, tb, cf);
    phase2_s(a, c, j, wave, lane, ta, tb, cf);
  }
}
}

extern "C" void kernel_launch(void* const* d_in, const int* in_sizes, int n_in, void* d_out, int out_size, void* d_ws,
                              size_t ws_size, hipStream_t stream) {
  using namespace s5k;
  Args a;
  a.x = (const u16*)d_in[0];
  a.lre = (const u16*)d_in[1];
  a.lim = (const u16*)d_in[2];
  a.bre = (const u16*)d_in[3];
  a.bim = (const u16*)d_in[4];
  a.cre = (const u16*)d_in[5];
  a.cim = (const u16*)d_in[6];
  a.ldt = (const u16*)d_in[7];
  a.dsk = (const u16*)d_in[8];
  a.w1 = (const u16*)d_in[10];
  a.w2 = (const u16*)d_in[11];
  a.out = (float*)d_out;
  a.ws = (char*)d_ws;
  (void)hipFuncSetAttribute((const void*)s5_fused, hipFuncAttributeMaxDynamicSharedMemorySize, LDS_BYTES);
  s5_fused<<<256, 512, LDS_BYTES, stream>>>(a);
}
```

```cpp
#include <hip/hip_runtime.h>

#define STAMPJ(id)
namespace s5k {
typedef unsigned short u16;
typedef unsigned int u32;
typedef unsigned long long u64;
typedef short bf16x8 __attribute__((ext_vector_type(8)));
typedef short s16x2 __attribute__((ext_vector_type(2)));
typedef float f32x4 __attribute__((ext_vector_type(4)));
typedef float f32x2 __attribute__((ext_vector_type(2)));
typedef u32 u32x4 __attribute__((ext_vector_type(4)));
typedef u32 u32x2 __attribute__((ext_vector_type(2)));
typedef __bf16 bf16x2 __attribute__((ext_vector_type(2)));
#define LAS __attribute__((address_space(3)))
#define GAS __attribute__((address_space(1)))
typedef LAS char* lptr;
typedef __amdgpu_buffer_rsrc_t rsrc_t;

constexpr u32 WS_STATS = 4096;
constexpr u32 WS_AGG = 65536;
constexpr u32 WS_F1 = 262144, WS_F2 = WS_F1 + 32768, WS_F3 = WS_F2 + 32768;
constexpr u32 WS_PROF = 512 << 10;
constexpr u32 WS_H1 = 1u << 20;
constexpr u32 WS_HS = 9u << 20;
constexpr u32 WS_CT = 16u << 20;
constexpr u32 WS_WT = 18u << 20;
constexpr u32 WS_END = 21u << 20;
constexpr int CHUNK = 40960;
constexpr int LD_TRB = 143360, LD_STG = LD_TRB + 4096, LD_ST2 = LD_TRB + 8192, LD_CS = LD_ST2 + 1024, LD_AGW = LD_CS + 1024,
              LD_CIN = LD_AGW + 1536, LD_CNT = LD_CIN + 1536, LD_TAB = LD_CNT + 256, TABP = 144, LD_END = LD_TAB + 48 * TABP;
constexpr int LDS_BYTES = 163840;
static_assert(LD_END <= LDS_BYTES, "lds");
enum { T_A = 0, T_C = 1, T_8 = 2, T_32 = 6, T_128 = 10, T_512 = 14 };
enum { C_F1 = 0, C_B2 = 1, C_S1 = 2, C_S2 = 3, C_F2 = 4, C_G = 5, C_PRE = 6, C_Y = 7, C_G2 = 8 };
constexpr int SC1 = 16;
#define MPRIO 1
#define SPRIO 0
#define CPRE 5
#define MPRIO2 2
#define SPRIO2 1
#define CODE_BYTES 72704
#define CPF_PARTS 9
#define YPRIO 0
#define GAW 3
#define GBD 0
#define WPRE 3
#define GPRIO 1
#define C1EARLY 0
#define CTAIL 2
#define WEARLY 0
#define SPRIO3 2
#define PSLEEP 2
#define OAUX 2
#define GBW 3
#define YAW 3
#define TPRIO GPRIO
#define DPRIO 0
#define DPACE 20
#define GMAP 4
constexpr u32 SPIN_CAP = 1u << 16;

struct Args {
  const u16 *x, *lre, *lim, *bre, *bim, *cre, *cim, *ldt, *dsk, *w1, *w2;
  float* out;
  char* ws;
};

__device__ __forceinline__ float u2f(u32 v) { return __builtin_bit_cast(float, v); }
__device__ __forceinline__ u32 f2u(float v) { return __builtin_bit_cast(u32, v); }
__device__ __forceinline__ float bf2f(u32 v) { return __builtin_bit_cast(float, v << 16); }
__device__ __forceinline__ float bfhi(u32 v) { return __builtin_bit_cast(float, v & 0xffff0000u); }
__device__ __forceinline__ u32 pk2(float a, float b) { return __builtin_bit_cast(u32, __builtin_convertvector(f32x2{a, b}, bf16x2)); }
__device__ __forceinline__ float rbf(float a) { return bf2f(pk2(a, a)); }
__device__ __forceinline__ f32x4 unpk4(u32x2 v) { return f32x4{bf2f(v.x), bfhi(v.x), bf2f(v.y), bfhi(v.y)}; }
__device__ __forceinline__ f32x4 rbf4(f32x4 v) { return unpk4(u32x2{pk2(v.x, v.y), pk2(v.z, v.w)}); }
__device__ __forceinline__ u32 relu2(u32 v) {
  return __builtin_bit_cast(u32, __builtin_elementwise_max(__builtin_bit_cast(s16x2, v), s16x2{0, 0}));
}
__device__ __forceinline__ f32x2 cmul(f32x2 a, f32x2 b) {
  float x, y;
  asm("v_mul_f32 %0, %2, %4\n\tv_mul_f32 %1, %2, %5\n\tv_fma_f32 %0, -%3, %5, %0\n\tv_fma_f32 %1, %3, %4, %1"
      : "=&v"(x), "=&v"(y) : "v"(a.x), "v"(a.y), "v"(b.x), "v"(b.y));
  return f32x2{x, y};
}
__device__ __forceinline__ f32x2 cfma(f32x2 a, f32x2 b, f32x2 c) {
  float x, y;
  asm("v_fma_f32 %0, %2, %4, %6\n\tv_fma_f32 %1, %2, %5, %7\n\tv_fma_f32 %0, -%3, %5, %0\n\tv_fma_f32 %1, %3, %4, %1"
      : "=&v"(x), "=&v"(y) : "v"(a.x), "v"(a.y), "v"(b.x), "v"(b.y), "v"(c.x), "v"(c.y));
  return f32x2{x, y};
}
__device__ __forceinline__ rsrc_t mk_rsrc(const void* p, u32 bytes) { return __builtin_amdgcn_make_buffer_rsrc((void*)p, 0, bytes, 0x00020000); }
__device__ __forceinline__ f32x4 mfma(bf16x8 a, bf16x8 b, f32x4 c) { return __builtin_amdgcn_mfma_f32_16x16x32_bf16(a, b, c, 0, 0, 0); }
__device__ __forceinline__ void mfma4(f32x4& c0, f32x4& c1, f32x4& c2, f32x4& c3, u32x4 a, const u32x4 (&b)[4]) {
  asm volatile("s_nop 1\n\tv_mfma_f32_16x16x32_bf16 %0, %4, %5, %0\n\tv_mfma_f32_16x16x32_bf16 %1, %4, %6, %1\n\t"
               "v_mfma_f32_16x16x32_bf16 %2, %4, %7, %2\n\tv_mfma_f32_16x16x32_bf16 %3, %4, %8, %3"
               : "+v"(c0), "+v"(c1), "+v"(c2), "+v"(c3) : "v"(a), "v"(b[0]), "v"(b[1]), "v"(b[2]), "v"(b[3]));
}
__device__ __forceinline__ void mfma_done() {
  __builtin_amdgcn_sched_barrier(0);
  asm volatile("s_nop 7\n\ts_nop 3" ::: "memory");
  __builtin_amdgcn_sched_barrier(0);
}
__device__ __forceinline__ bf16x8 asfrag(u32x4 v) { return __builtin_bit_cast(bf16x8, v); }
__device__ __forceinline__ void dma16(rsrc_t rs, u32 dst, u32 voff, u32 soff) {
  asm volatile("s_mov_b32 m0, %0\n\ts_nop 0\n\tbuffer_load_dwordx4 %1, %2, %3 offen lds" ::"s"(dst), "v"(voff), "s"(rs), "s"(soff) : "memory");
}
__device__ __forceinline__ void dma16_sc1(rsrc_t rs, u32 dst, u32 voff, u32 soff) {
  asm volatile("s_mov_b32 m0, %0\n\ts_nop 0\n\tbuffer_load_dwordx4 %1, %2, %3 offen sc1 lds" ::"s"(dst), "v"(voff), "s"(rs), "s"(soff) : "memory");
}
template <int DOFF> __device__ __forceinline__ void gdma(const char* base, u32 dst, u32 voff) {
  asm volatile("s_add_i32 m0, %0, %1\n\ts_nop 0\n\tglobal_load_lds_dwordx4 %2, %3 sc1" ::"s"(dst), "i"(DOFF), "v"(voff), "s"(base) : "memory", "scc");
}
__device__ __forceinline__ void dma4_sc1(rsrc_t rs, u32 dst, u32 voff, u32 soff) {
  asm volatile("s_mov_b32 m0, %0\n\ts_nop 0\n\tbuffer_load_dword %1, %2, %3 offen sc1 lds" ::"s"(dst), "v"(voff), "s"(rs), "s"(soff) : "memory");
}
__device__ __forceinline__ void dma4_plain(rsrc_t rs, u32 dst, u32 voff, u32 soff) {
  asm volatile("s_mov_b32 m0, %0\n\ts_nop 0\n\tbuffer_load_dword %1, %2, %3 offen lds" ::"s"(dst), "v"(voff), "s"(rs), "s"(soff) : "memory");
}
__device__ __forceinline__ lptr L(u32 addr) { return (lptr)(size_t)addr; }
__device__ __forceinline__ bf16x8 ld128(u32 p) { return *(LAS bf16x8*)L(p); }
__device__ __forceinline__ f32x2 ldc(u32 p) { return *(LAS f32x2*)L(p); }
__device__ __forceinline__ f32x4 ld4(u32 p) { return *(LAS f32x4*)L(p); }
__device__ __forceinline__ bf16x8 ldtr(u32 p0, u32 p1) {
  typedef short v4i16 __attribute__((ext_vector_type(4)));
  v4i16 a = __builtin_amdgcn_ds_read_tr16_b64_v4i16((LAS v4i16*)L(p0));
  v4i16 b = __builtin_amdgcn_ds_read_tr16_b64_v4i16((LAS v4i16*)L(p1));
  return __builtin_shufflevector(a, b, 0, 1, 2, 3, 4, 5, 6, 7);
}
__device__ __forceinline__ u32x4 gld(rsrc_t rs, u32 voff, u32 soff) { return __builtin_amdgcn_raw_buffer_load_b128(rs, voff, soff, SC1); }
__device__ __forceinline__ void gst(u32x4 v, rsrc_t rs, u32 voff) { __builtin_amdgcn_raw_buffer_store_b128(v, rs, voff, 0, SC1); }
template <int N> __device__ __forceinline__ void wait_vm() { asm volatile("s_waitcnt vmcnt(%0)" ::"i"(N) : "memory"); }
__device__ __forceinline__ void wait_lgkm0() { __builtin_amdgcn_s_waitcnt(0xc07f); asm volatile("" ::: "memory"); }
__device__ __forceinline__ void wg_barrier() {
  asm volatile("" ::: "memory");
  __builtin_amdgcn_s_barrier();
  asm volatile("" ::: "memory");
}
#define PIN(x) asm volatile("" : "+v"(x))
#define SCHED0() __builtin_amdgcn_sched_barrier(0)
__device__ __forceinline__ u32 cnt_add(int idx, int lane) {
  u32 old;
  const u32 addr = LD_CNT + idx * 4 + (lane == 0 ? 0 : 64), one = 1;
  asm volatile("s_waitcnt lgkmcnt(0)\n\ts_mov_b64 exec, 1\n\tds_add_rtn_u32 %0, %1, %2\n\ts_mov_b64 exec, -1\n\ts_waitcnt lgkmcnt(0)"
               : "=&v"(old) : "v"(addr), "v"(one) : "memory");
  return __builtin_amdgcn_readfirstlane(old);
}
__device__ __forceinline__ void cnt_wait(int idx, u32 want) {
  const u32 addr = LD_CNT + idx * 4;
#pragma unroll 1
  for (u32 s = 0; s < (SPIN_CAP << 4); s++) {
    u32 v;
    asm volatile("ds_read_b32 %0, %1\n\ts_waitcnt lgkmcnt(0)" : "=&v"(v) : "v"(addr) : "memory");
    if (__builtin_amdgcn_readfirstlane(v) >= want) break;
    __builtin_amdgcn_s_sleep(1);
  }
}
__device__ __forceinline__ void polln(rsrc_t wsr, u32 off, int mask, u32 ta, u32 tb, int lane) {
  const u32 vo = off + lane * 128;
#pragma unroll 1
  for (u32 s = 0; s < SPIN_CAP; s++) {
    asm volatile("" ::: "memory");
    u32x2 v = {ta, tb};
    if (lane <= mask) v = __builtin_amdgcn_raw_buffer_load_b64(wsr, vo, 0, SC1);
    if (__all((v.x == ta) & (v.y == tb))) break;
    __builtin_amdgcn_s_sleep(PSLEEP);
  }
  __builtin_amdgcn_fence(__ATOMIC_ACQUIRE, "wavefront");
  asm volatile("" ::: "memory");
}
__device__ __forceinline__ void put_flag(rsrc_t wsr, u32 off, u32 ta, u32 tb, int lane) {
  __builtin_amdgcn_raw_buffer_store_b64(u32x2{ta, tb}, wsr, lane == 0 ? off : WS_END, 0, SC1);
}
template <int CTRL> __device__ __forceinline__ float dpp(float x) {
  return __builtin_bit_cast(float, __builtin_amdgcn_mov_dpp(__builtin_bit_cast(int, x), CTRL, 0xf, 0xf, true));
}
__device__ __forceinline__ void rows4(float x, float (&r)[4]) {
  auto s = __builtin_amdgcn_permlane16_swap(f2u(x), f2u(x), false, false);
  const u32 e = s[0], o = s[1];
  auto te = __builtin_amdgcn_permlane32_swap(e, e, false, false);
  auto to = __builtin_amdgcn_permlane32_swap(o, o, false, false);
  const u32 r0 = te[0], r2 = te[1], r1 = to[0], r3 = to[1];
  r[0] = u2f(r0); r[1] = u2f(r1); r[2] = u2f(r2); r[3] = u2f(r3);
}
__device__ __forceinline__ float rows_sum(float x) {
  auto s = __builtin_amdgcn_permlane16_swap(f2u(x), f2u(x), false, false);
  const u32 a = s[0], b = s[1];
  x = u2f(a) + u2f(b);
  auto t = __builtin_amdgcn_permlane32_swap(f2u(x), f2u(x), false, false);
  const u32 c = t[0], d = t[1];
  return u2f(c) + u2f(d);
}
__device__ __forceinline__ float wave_sum(float v) {
  v += dpp<0xB1>(v); v += dpp<0x4E>(v); v += dpp<0x124>(v); v += dpp<0x128>(v);
  return rows_sum(v);
}
__device__ __forceinline__ float fexp(float x) {
  const float hi = x * 1.4426950408889634f;
  const float lo = __builtin_fmaf(x, 1.4426950408889634f, -hi) + x * 1.925963033500011e-8f;
  return __builtin_amdgcn_exp2f(hi) * (1.0f + lo * 0.6931471805599453f);
}
__device__ __forceinline__ void fsincos(float x, float& sn, float& cs) {
  if (__builtin_expect(__any(!(__builtin_fabsf(x) < 2000.f)), 0)) { sincosf(x, &sn, &cs); return; }
  const float n = __builtin_rintf(x * 0.6366197723675814f);
  float r = __builtin_fmaf(n, -1.5703125f, x);
  r = __builtin_fmaf(n, -4.837512969970703125e-4f, r);
  r = __builtin_fmaf(n, -7.54978995489188e-8f, r);
  const float r2 = r * r;
  const float s = __builtin_fmaf(r * r2, __builtin_fmaf(r2, __builtin_fmaf(r2, -1.9515295891e-4f, 8.3321608736e-3f), -1.6666654611e-1f), r);
  const float c = __builtin_fmaf(r2 * r2, __builtin_fmaf(r2, __builtin_fmaf(r2, 2.443315711809948e-5f, -1.388731625493765e-3f), 4.166664568298827e-2f),
                                 __builtin_fmaf(r2, -0.5f, 1.0f));
  const int k = (int)n;
  const float s1 = (k & 1) ? c : s, c1 = (k & 1) ? s : c;
  sn = (k & 2) ? -s1 : s1;
  cs = ((k + 1) & 2) ? -c1 : c1;
}
#define STAMP(id) asm volatile("; MARK " #id)
#define STAMPI(id)

struct Pre {
  u64 epoch;
  const char* gb[5];
  u32 dvo[5], dstep[4];
  u32 dd0, dd1;
  int kc;
};
__device__ __forceinline__ void head(const Args& a, Pre& P, u64 pc, int c, int j, int wave, int lane, u32& ta, u32& tb) {
  const int li = lane & 15, r8 = lane >> 3, sl = lane & 7, i2 = j >> 1, tB = j & 1;
  const int nS = 3 - tB, nM = 3 + tB;
  const int s0 = 5 * i2 + 3 * tB;
  const int m0 = 7 * i2 + ((i2 & 1) ? 3 * tB : 4 - 4 * tB);
  int kc = c + j;
  kc -= kc >= 20 ? 20 : kc >= 10 ? 10 : 0;
  kc = kc == 10 ? 0 : kc;
  u64 pbre = (u64)a.bre, pbim = (u64)a.bim, pw1 = (u64)a.w1;
  asm volatile("" : "+s"(pbre), "+s"(pbim), "+s"(pw1) : "s"(a.x), "s"(a.ws), "s"(a.lre), "s"(a.lim), "s"(a.ldt));
  u32x2 ep;
  u32x4 xr0, xr1;
  u32 plr, pli, pdt;
  const GAS char* xrow = (const GAS char*)a.x + (size_t)(c * 128 + j * 8 + wave) * 1280;
  asm volatile("global_load_dwordx2 %0, %1, %2 sc1" : "=v"(ep) : "v"(0), "s"(a.ws) : "memory");
  asm volatile("global_load_dwordx4 %0, %1, %2" : "=v"(xr0) : "v"(lane * 16), "s"(xrow) : "memory");
  asm volatile("global_load_dwordx4 %0, %1, %2 offset:1024" : "=v"(xr1) : "v"(li * 16), "s"(xrow) : "memory");
  const int ch = (s0 * 16 + (lane < 16 * nS ? lane : li)) * 2;
  asm volatile("global_load_ushort %0, %1, %2" : "=v"(plr) : "v"(ch), "s"(a.lre) : "memory");
  asm volatile("global_load_ushort %0, %1, %2" : "=v"(pli) : "v"(ch), "s"(a.lim) : "memory");
  asm volatile("global_load_ushort %0, %1, %2" : "=v"(pdt) : "v"(ch), "s"(a.ldt) : "memory");
  P.dd0 = wave * 2048;
  P.dd1 = 16384 + wave * 3072;
  P.dstep[0] = 128;
#pragma unroll
  for (int b = 0; b < 2; b++) {
    P.gb[b] = (const char*)a.x + (c * 128 + (wave >> 1) * 32 + b * 16 + (wave & 1) * 4) * 1280;
    P.dvo[b] = (8 * (r8 >> 2) + (r8 & 3)) * 1280 + ((sl ^ (b * 4 + (r8 >> 1))) << 4);
  }
  gdma<0>(P.gb[0] + kc * 128, P.dd0, P.dvo[0]);
  gdma<1024>(P.gb[1] + kc * 128, P.dd0, P.dvo[1]);
#pragma unroll
  for (int t = 0; t < 3; t++) {
    const int k = wave * 3 + t, b = 2 + t;
    const bool isw = k >= 4 * nS;
    const int sub = k & 3;
    const u32 bvo = r8 * 1280 + ((sl ^ ((sub & 1) * 4 + (r8 >> 1))) << 4);
    const u32 idx = (k - 4 * nS) * 64 + lane;
    const u32 kr = (idx * (tB ? 32768u : 43691u)) >> 19, cpos = idx - kr * (4 * nM);
    const u32 sw = (kr & (3 * tB)) | (((kr >> 3) & 1) << (2 * tB));
    u32 col16 = m0 * 4 + (((cpos >> 1) ^ sw) << 1) + (cpos & 1);
    col16 -= col16 >= 216 ? 8 : 0;
    P.gb[b] = (const char*)(isw ? pw1 : sub < 2 ? pbre : pbim) + (isw ? 0 : ((s0 + (k >> 2)) * 16 + (sub & 1) * 8) * 1280);
    P.dvo[b] = isw ? kr * 3456 + col16 * 16 : bvo;
    P.dstep[b - 1] = isw ? 64 * 3456 : 128;
    if (t == 0) gdma<0>(P.gb[b] + kc * P.dstep[b - 1], P.dd1, P.dvo[b]);
    if (t == 1) gdma<1024>(P.gb[b] + kc * P.dstep[b - 1], P.dd1, P.dvo[b]);
    if (t == 2) gdma<2048>(P.gb[b] + kc * P.dstep[b - 1], P.dd1, P.dvo[b]);
  }
  kc = kc == 9 ? 0 : kc + 1;
  {
    const int part = (blockIdx.x >> 3) & 15;
    dma4_plain(mk_rsrc((const void*)pc, CODE_BYTES), LD_TRB + 6144, wave == 7 ? (part * 64 + lane) * 128 : CODE_BYTES, 0);
  }
  *(LAS u32*)L(LD_CNT + li * 4) = 0;
  const rsrc_t wsr = mk_rsrc(a.ws, WS_END);
  STAMP(1);
  auto issue1 = [&]() {
    gdma<CHUNK>(P.gb[0] + kc * 128, P.dd0, P.dvo[0]);
    gdma<CHUNK + 1024>(P.gb[1] + kc * 128, P.dd0, P.dvo[1]);
    gdma<CHUNK>(P.gb[2] + kc * P.dstep[1], P.dd1, P.dvo[2]);
    gdma<CHUNK + 1024>(P.gb[3] + kc * P.dstep[2], P.dd1, P.dvo[3]);
    gdma<CHUNK + 2048>(P.gb[4] + kc * P.dstep[3], P.dd1, P.dvo[4]);
  };
  asm volatile("s_waitcnt vmcnt(6)" : "+v"(ep), "+v"(xr0), "+v"(xr1), "+v"(plr), "+v"(pli), "+v"(pdt)::"memory");
  P.kc = kc == 9 ? 0 : kc + 1;
  {
    P.epoch = (u64)__builtin_amdgcn_readfirstlane(ep.y) << 32 | __builtin_amdgcn_readfirstlane(ep.x);
    u64 z = P.epoch + 0x9E3779B97F4A7C15ull;
    z = (z ^ (z >> 30)) * 0xBF58476D1CE4E5B9ull;
    z = (z ^ (z >> 27)) * 0x94D049BB133111EBull;
    z ^= z >> 31;
    ta = (u32)z;
    tb = (u32)(z >> 32);
    float v[16];
#pragma unroll
    for (int e = 0; e < 4; e++) {
      v[2 * e] = bf2f(xr0[e]); v[2 * e + 1] = bfhi(xr0[e]);
      v[8 + 2 * e] = bf2f(xr1[e]); v[8 + 2 * e + 1] = bfhi(xr1[e]);
    }
    float s0_ = 0.f, s1_ = 0.f;
#pragma unroll
    for (int e = 0; e < 8; e++) { s0_ += v[e]; s1_ += v[8 + e]; }
    const float mean = wave_sum(lane < 16 ? s0_ + s1_ : s0_) * (1.0f / 640.0f);
    s0_ = 0.f; s1_ = 0.f;
#pragma unroll
    for (int e = 0; e < 8; e++) {
      const float d0 = v[e] - mean, d1 = v[8 + e] - mean;
      s0_ += d0 * d0; s1_ += d1 * d1;
    }
    const float var = wave_sum(lane < 16 ? s0_ + s1_ : s0_) * (1.0f / 640.0f);
    const float mb = rbf(mean), den = rbf(__builtin_amdgcn_sqrtf(rbf(rbf(var) + 1e-5f)));
    gst(u32x4{f2u(mb), ta, f2u(__builtin_amdgcn_rcpf(den)), tb}, wsr, lane == 0 ? WS_STATS + (c * 128 + j * 8 + wave) * 16 : WS_END);
  }
  issue1();
  {
    if (wave == 0) {
      const float lr = bf2f(plr), lim = bf2f(pli);
      const float dt = rbf(fexp(bf2f(pdt)));
      const float er = fexp(dt * lr);
      float sn, cn;
      fsincos(dt * lim, sn, cn);
      const f32x2 a1 = cmul(f32x2{er, 0.f}, f32x2{cn, sn});
      const float nr = a1.x - 1.0f, dn = 1.0f / (lr * lr + lim * lim);
      const u32 tp = LD_TAB + (lane < 48 ? lane : 47) * TABP;
      if (lane < 16 * nS) {
        const f32x2 cf_ = cmul(f32x2{nr, a1.y}, f32x2{lr * dn, -lim * dn});
        *(LAS f32x4*)L(tp) = f32x4{a1.x, a1.y, cf_.x, cf_.y};
        const f32x2 a2 = cmul(a1, a1), a4 = cmul(a2, a2);
        f32x2 b = cmul(a4, a4);
#pragma unroll
        for (int i = 0; i < 4; i++) {
          const f32x2 b2 = cmul(b, b), b3 = cmul(b2, b);
          *(LAS f32x4*)L(tp + 16 + i * 32) = f32x4{1.f, 0.f, b.x, b.y};
          *(LAS f32x4*)L(tp + 32 + i * 32) = f32x4{b2.x, b2.y, b3.x, b3.y};
          b = cmul(b2, b2);
        }
      }
    }
  }
}

template <bool IS_M, int NP>
__device__ __forceinline__ void phase1(const Args& a, Pre& P, int c, int j, int wave, int lane, u32 ta, u32 tb, u32x4 (&cf)[5][5]) {
  constexpr int NU = 2 * NP;
  constexpr bool TYPE_B = IS_M ? (NP == 4) : (NP == 2);
  constexpr int nS = TYPE_B ? 2 : 3, nM = 6 - nS, MP = nM * 64;
  const int wm = wave & 3;
  const int i2 = j >> 1;
  const int s0 = 5 * i2 + (TYPE_B ? 3 : 0);
  const int m0 = 7 * i2 + ((i2 & 1) ? (TYPE_B ? 3 : 0) : (TYPE_B ? 0 : 4));
  const int li = lane & 15, q = lane >> 4;
  const rsrc_t wsr = mk_rsrc(a.ws, WS_END);
  u32 kc = P.kc;
  auto dma1 = [&](int bufoff, int b) {
    const char* src = P.gb[b] + kc * P.dstep[b < 2 ? 0 : b - 1];
    const u32 d = b < 2 ? P.dd0 : P.dd1;
#define GD(B, O) if (bufoff == B && (b < 2 ? b : b - 2) == O) gdma<B + O * 1024>(src, d, P.dvo[b]);
    GD(0, 0) GD(0, 1) GD(0, 2) GD(CHUNK, 0) GD(CHUNK, 1) GD(CHUNK, 2) GD(2 * CHUNK, 0) GD(2 * CHUNK, 1) GD(2 * CHUNK, 2)
#undef GD
  };
  const int rid = c * 2 + (j & 1);

  int pr[NP];
#pragma unroll
  for (int p = 0; p < NP; p++) pr[p] = (p + wm) % NP;
  u32 xb[2][2], wb[2][NU];
  {
    const u32 xo = li * 128 + ((q ^ (li >> 1)) << 4);
    const int qq = li >> 2;
    const u32 rowb = 16384 + nS * 4096 + (8 * q + qq) * MP + (lane & 3) * 8;
    const int f = TYPE_B ? (qq | ((q & 1) << 2)) : (q & 1);
#pragma unroll
    for (int h = 0; h < 2; h++) {
#pragma unroll
      for (int kk = 0; kk < 2; kk++) {
        xb[h][kk] = h * 2 * CHUNK + wm * 4096 + (xo ^ (kk << 6));
        PIN(xb[h][kk]);
      }
#pragma unroll
      for (int u = 0; u < NU; u++) {
        wb[h][u] = h * 2 * CHUNK + (IS_M ? rowb + (((2 * pr[u >> 1] + (u & 1)) ^ f) << 5) : 16384 + pr[u >> 1] * 4096 + (xo ^ ((u & 1) << 6)));
        PIN(wb[h][u]);
      }
    }
  }
  bf16x8 xf[2][2], wf[2][NU];
  auto read1 = [&](int set, int bufoff, int kk, int f) {
    const int h = bufoff == 2 * CHUNK, bo = h ? 0 : bufoff;
    if (f < 2) {
      xf[set][f] = ld128(xb[h][kk] + (bo + f * 2048));
    } else if (f < NU + 2) {
      const int u = f - 2;
      if (!IS_M) wf[set][u] = ld128(wb[h][(u & ~1) + kk] + (bo + (u & 1) * 2048));
      else wf[set][u] = ldtr(wb[h][u] + (bo + kk * 32 * MP), wb[h][u] + (bo + kk * 32 * MP + 4 * MP));
    }
  };
  f32x4 acc[2][NU], csum[2];
#pragma unroll
  for (int m = 0; m < 2; m++) {
#pragma unroll
    for (int u = 0; u < NU; u++) { acc[m][u] = f32x4{0.f, 0.f, 0.f, 0.f}; PIN(acc[m][u]); }
    csum[m] = f32x4{0.f, 0.f, 0.f, 0.f};
    PIN(csum[m]);
  }
  u32x4 ones4 = {0x3f803f80u, 0x3f803f80u, 0x3f803f80u, 0x3f803f80u};
  PIN(ones4);
  constexpr int G = NU + 1;
  constexpr int NDB = IS_M ? 2 : 3;
  auto dpos = [](int d) { return (2 * G * d + (IS_M ? G : 0)) / 5; };
  static_assert((2 * G * (NDB - 1) + (IS_M ? G : 0)) / 5 < G && (2 * G * NDB + (IS_M ? G : 0)) / 5 >= G, "split");
  auto step = [&](int set, int rbuf, int rkk, int dbuf, bool tail) {
#pragma unroll
    for (int g = 0; g < G; g++) {
      if (g > 0) {
        const int u = g - 1;
#pragma unroll
        for (int m = 0; m < 2; m++)
          acc[m][u] = IS_M ? mfma(wf[set][u], xf[set][m], acc[m][u]) : mfma(xf[set][m], wf[set][u], acc[m][u]);
      } else {
        csum[0] = mfma(asfrag(ones4), wf[set][0], csum[0]);
        csum[1] = mfma(asfrag(ones4), wf[set][1], csum[1]);
      }
      if (rbuf >= 0) {
        if (g == 0) { read1(set ^ 1, rbuf, rkk, 2); read1(set ^ 1, rbuf, rkk, 3); }
        else if (g == 1) { read1(set ^ 1, rbuf, rkk, 0); read1(set ^ 1, rbuf, rkk, 1); }
        else read1(set ^ 1, rbuf, rkk, g + 2);
      } else if (g == 3) {
        LAS float* csp = (LAS float*)L(LD_CS + (IS_M ? 512 : 0) + (wm < NP ? wm : 3) * 128 + (lane & 31) * 4);
        float e0 = csum[0].x, e1 = csum[1].x;
        PIN(e0); PIN(e1);
        if (wm < NP) *csp = (q & 1) ? e1 : e0;
      }
      if (!IS_M && tail) {
#pragma unroll
        for (int f = 0; f < 5 * CTAIL; f++)
          if (f * 2 * G / (5 * CTAIL) == (set == 0 ? g : g + G))
            cf[f / 5][f % 5] = gld(wsr, lane * 16, WS_CT + (i2 * 200 + wm * 50 + f) * 1024);
      }
#pragma unroll
      for (int d = 0; d < 5; d++)
        if ((set == 1 ? g : g + G) == dpos(d)) dma1(dbuf, d);
      SCHED0();
    }
  };

#pragma unroll
  for (int d = 0; d < NDB; d++) dma1(2 * CHUNK, d);
  u32x4 rt[2];
  const int rks = min(rid * 2 + (wm & 1), 53);
  if (!IS_M) {
#pragma unroll
    for (int i = 0; i < 2; i++) {
      const int f = min(rid * 8 + wm * 2 + i, 199);
      const int wk = f / 5, n = f - wk * 5, w = wk / 10, k = wk - w * 10;
      const GAS char* src = (const GAS char*)(k < 5 ? a.cre : a.cim) + (i2 * 80 + n * 16) * 1280 + (160 * w + 32 * (k < 5 ? k : k - 5)) * 2;
      asm volatile("global_load_dwordx4 %0, %1, %2" : "=v"(rt[i]) : "v"(li * 1280 + q * 16), "s"(src) : "memory");
    }
    wait_vm<7 + NDB + 2>();
  } else {
    wait_vm<7 + NDB>();
  }
  wait_lgkm0();
  wg_barrier();
  STAMP(2);
  __builtin_amdgcn_s_setprio(IS_M ? MPRIO : SPRIO);
#pragma unroll
  for (int f = 0; f < NU + 2; f++) read1(0, 0, 0, f);
  int it = 0;
  auto body = [&](int prv, int cur, int nxt) {
    step(0, cur, 1, prv, nxt < 0);
    kc = kc == 9 ? 0 : kc + 1;
    if (nxt < 0) {
      const int r = wave * 16 + li;
      u32x4 g = *(LAS u32x4*)L(LD_STG + r * 16);
      if (!__all((g.y == ta) & (g.w == tb))) {
#pragma unroll 1
        for (u32 s = 0; s < SPIN_CAP; s++) {
          asm volatile("" ::: "memory");
          g = gld(wsr, WS_STATS + (c * 128 + r) * 16, 0);
          if (__all((g.y == ta) & (g.w == tb))) break;
        }
      }
      const int t5 = r & 31;
      *(LAS u32*)L(LD_ST2 + (q & 1) * 512 + ((r & ~31) + (((t5 >> 2) & 1) << 4) + ((t5 >> 3) << 2) + (t5 & 3)) * 4) = (q & 1) ? g.z : g.x;
    } else if (!IS_M && it == 0) wait_vm<7>();
    else if (IS_M && it == 1 && wm < 2) wait_vm<10>();
    else wait_vm<5>();
    wait_lgkm0();
    wg_barrier();
    STAMPI(3 + it);
    if (IS_M && it == 0 && wm < 2) {
      const rsrc_t w2r = mk_rsrc(a.w2, 1728 * 1280);
#pragma unroll
      for (int b = 0; b < 5; b++) {
        const int idx = b * 64 + lane, kr = idx / 10, cpos = idx - kr * 10;
        dma16(w2r, 3 * CHUNK + wm * 5120 + b * 1024, kr * 1280 + i2 * 160 + cpos * 16, rks * (32 * 1280));
      }
    }
    if (it == 2) {
      if (!IS_M) {
        asm volatile("" : "+v"(rt[0]), "+v"(rt[1]));
#pragma unroll
        for (int i = 0; i < 2; i++) {
          const int f = min(rid * 8 + wm * 2 + i, 199);
          gst(rt[i] ^ (f % 50 >= 25 ? 0x80008000u : 0u), wsr, WS_CT + (i2 * 200 + f) * 1024 + lane * 16);
        }
      } else if (wm < 2) {
        const u32 p = 3 * CHUNK + wm * 5120 + (4 * q + (li >> 2)) * 160 + (lane & 3) * 8;
#pragma unroll
        for (int n = 0; n < 5; n++)
          gst(__builtin_bit_cast(u32x4, ldtr(p + n * 32, p + n * 32 + 2560)), wsr, WS_WT + ((i2 * 54 + rks) * 5 + n) * 1024 + lane * 16);
      }
    }
    if (IS_M && it == 3 && wm == 3) put_flag(wsr, WS_F3 + (i2 * 32 + rid) * 128, ta, tb, lane);
    if (it >= 7) {
      if (it == 7) {
        const u32 f = *(LAS u32*)L(LD_TRB + wave * 256 + lane * 4);
        if (!__all(f == ((lane & 1) ? tb : ta))) polln(wsr, WS_F3 + i2 * 32 * 128, 31, ta, tb, lane);
#pragma unroll
        for (int b = 0; b < 5; b++) {
          P.gb[b] = a.ws + b * 1024;
          P.dvo[b] = lane * 16;
          if (b < 4) P.dstep[b] = 1024;
        }
        P.dd0 = wave * 5120;
        P.dd1 = wave * 5120 + 2048;
      }
      kc = (WS_WT >> 10) + (i2 * 54 + wm * 14 + (cur / CHUNK) * 2 + (IS_M ? 1 : 0)) * 5;
    }
    SCHED0();
    step(1, nxt, 0, cur, nxt < 0);
    if (IS_M && it == 8) {
#pragma unroll
      for (int b = 0; b < 5; b++) dma16_sc1(wsr, (24 + wm) * 5120 + b * 1024, lane * 16, WS_WT + ((i2 * 54 + wm * 14 + 6) * 5 + b) * 1024);
    }
    if (it == 5) {
      dma4_sc1(wsr, LD_TRB + wave * 256, (lane >> 1) * 128 + (lane & 1) * 4, WS_F3 + i2 * 32 * 128);
      dma4_sc1(wsr, LD_STG + wave * 256, lane * 4, WS_STATS + c * 2048 + wave * 256);
    }
    it++;
  };
#pragma unroll 1
  for (int r = 0; r < 3; r++) {
    body(2 * CHUNK, 0, CHUNK);
    body(0, CHUNK, 2 * CHUNK);
    body(CHUNK, 2 * CHUNK, 0);
  }
  body(2 * CHUNK, 0, -1);
#pragma unroll
  for (int d = NDB; d < 5; d++) dma1(0, d);
  __builtin_amdgcn_s_setprio(IS_M ? MPRIO2 : SPRIO2);
  STAMP(3);
  float mb[2];
  f32x4 mu[2], rs[2];
  f32x2 ca[NP], cc[NP], aq[NP], a8r[NP];
  if (IS_M) {
#pragma unroll
    for (int m = 0; m < 2; m++) mb[m] = *(LAS float*)L(LD_ST2 + (wm * 32 + m * 16 + li) * 4);
  } else {
#pragma unroll
    for (int m = 0; m < 2; m++) {
      mu[m] = ld4(LD_ST2 + (wm * 32 + m * 16 + 4 * q) * 4);
      rs[m] = ld4(LD_ST2 + 512 + (wm * 32 + m * 16 + 4 * q) * 4);
    }
#pragma unroll
    for (int p = 0; p < NP; p++) {
      const u32 tp = LD_TAB + (pr[p] * 16 + li) * TABP;
      const f32x4 t0 = ld4(tp);
      ca[p] = f32x2{t0.x, t0.y};
      cc[p] = f32x2{t0.z, t0.w};
      aq[p] = cmul(ldc(tp + (T_8 + q) * 8), ldc(tp + (T_32 + wm) * 8));
      a8r[p] = ldc(tp + (T_8 + 3 - q) * 8);
    }
  }
  wait_lgkm0();
  wg_barrier();
  STAMP(4);

  if (IS_M) {
#pragma unroll
    for (int p = 0; p < NP; p++) {
      const f32x4 c0 = ld4(LD_CS + 512 + pr[p] * 128 + q * 16), c1 = ld4(LD_CS + 512 + pr[p] * 128 + 64 + q * 16);
      const u32 off = (m0 + pr[p] < 54) ? WS_H1 + ((c * 8 + 2 * wm) * 54 + m0 + pr[p]) * 1024 : WS_END;
#pragma unroll
      for (int m = 0; m < 2; m++) {
        const f32x4 v0 = acc[m][2 * p] - mb[m] * c0, v1 = acc[m][2 * p + 1] - mb[m] * c1;
        gst(u32x4{relu2(pk2(v0.x, v0.y)), relu2(pk2(v0.z, v0.w)), relu2(pk2(v1.x, v1.y)), relu2(pk2(v1.z, v1.w))}, wsr,
            off + m * 54 * 1024 + lane * 16);
      }
    }
  } else {
    const u32 sg = LD_TRB + ((wm * 4 + q) * 48 + li) * 8;
#pragma unroll
    for (int p = 0; p < NP; p++) {
      const float Sr = *(LAS float*)L(LD_CS + pr[p] * 128 + li * 4), Si = *(LAS float*)L(LD_CS + pr[p] * 128 + 64 + li * 4);
      f32x2 h;
#pragma unroll
      for (int m = 0; m < 2; m++) {
        acc[m][2 * p] = (acc[m][2 * p] - mu[m] * Sr) * rs[m];
        acc[m][2 * p + 1] = (acc[m][2 * p + 1] - mu[m] * Si) * rs[m];
#pragma unroll
        for (int r = 0; r < 4; r++) {
          const f32x2 u = {acc[m][2 * p][r], acc[m][2 * p + 1][r]};
          h = (m | r) ? cfma(ca[p], h, u) : u;
        }
      }
      asm volatile("" ::"v"(Sr), "v"(Si));
      *(LAS f32x2*)L(sg + pr[p] * 128) = h;
      h = cmul(a8r[p], h);
      *(LAS f32x2*)L(LD_AGW + ((wm * 3 + pr[p]) * 16 + li) * 8) = f32x2{rows_sum(h.x), rows_sum(h.y)};
    }
    const int ch = lane < 48 ? lane : 47;
    const u32 tc = LD_TAB + ch * TABP;
    if (cnt_add(C_S1, lane) == 3) {
      const f32x2 a32 = ldc(tc + (T_32 + 1) * 8), a8 = ldc(tc + (T_8 + 1) * 8);
      f32x2 h = ldc(LD_AGW + ch * 8);
#pragma unroll
      for (int w = 1; w < 4; w++) h = cfma(a32, h, ldc(LD_AGW + (w * 48 + ch) * 8));
      gst(u32x4{f2u(h.x), ta, f2u(h.y), tb}, wsr, q < NP ? WS_AGG + (c * 640 + s0 * 16 + lane) * 16 : WS_END);
      SCHED0();
      f32x2 g[15];
#pragma unroll
      for (int i = 0; i < 15; i++) g[i] = ldc(LD_TRB + (i * 48 + ch) * 8);
      *(LAS f32x2*)L(LD_TRB + ch * 8) = f32x2{0.f, 0.f};
#pragma unroll
      for (int i = 0; i < 15; i++) {
        h = i ? cfma(a8, h, g[i]) : g[0];
        *(LAS f32x2*)L(LD_TRB + ((i + 1) * 48 + ch) * 8) = h;
      }
      cnt_add(C_PRE, lane);
    }
    STAMP(5);
    __builtin_amdgcn_s_setprio(SPRIO3);
    {
      const u32 co = WS_CT + (i2 * 200 + wm * 50) * 1024 + lane * 16;
#pragma unroll
      for (int k = CTAIL; k < CPRE; k++)
#pragma unroll
        for (int n = 0; n < 5; n++) cf[k][n] = gld(wsr, co, (k * 5 + n) * 1024);
      SCHED0();
    }
    wait_vm<5 * (CPRE - CTAIL)>();
    cnt_add(C_B2, lane);
#pragma unroll
    for (int p = 0; p < NP; p++) {
      f32x2 cr2 = {cc[p].x, cc[p].x}, ci2 = {cc[p].y, cc[p].y};
      PIN(cr2); PIN(ci2);
      const f32x4 cr4 = {cr2.x, cr2.y, cr2.x, cr2.y}, ci4 = {ci2.x, ci2.y, ci2.x, ci2.y};
#pragma unroll
      for (int m = 0; m < 2; m++) {
        const f32x4 ar = acc[m][2 * p], ai = acc[m][2 * p + 1];
        acc[m][2 * p] = cr4 * ar - ci4 * ai;
        acc[m][2 * p + 1] = cr4 * ai + ci4 * ar;
        PIN(acc[m][2 * p]); PIN(acc[m][2 * p + 1]);
      }
      aq[p] = cmul(cc[p], aq[p]);
    }
    {
      const int nv = min(max(c - 4 * wm, 0), 4), e = max(c - 4 * wm - nv, 0);
      const f32x2 a128 = ldc(tc + (T_128 + 1) * 8), pw = cmul(ldc(tc + (T_128 + (e & 3)) * 8), ldc(tc + (T_512 + (e >> 2)) * 8));
      const u32 go = WS_AGG + (4 * wm * 640 + s0 * 16 + min(lane, 16 * NP - 1)) * 16;
      u32x4 lg[4];
#pragma unroll 1
      for (u32 s = 0; s < SPIN_CAP; s++) {
        asm volatile("" ::: "memory");
        bool ok = true;
#pragma unroll
        for (int k = 0; k < 4; k++) lg[k] = gld(wsr, go, k * 640 * 16);
#pragma unroll
        for (int k = 0; k < 4; k++) ok = ok & (((lg[k].y == ta) & (lg[k].w == tb)) | (k >= nv));
        if (__all(ok)) break;
      }
      f32x2 h = {0.f, 0.f};
#pragma unroll
      for (int k = 0; k < 4; k++)
        if (k < nv) h = cfma(a128, h, f32x2{u2f(lg[k].x), u2f(lg[k].z)});
      *(LAS f32x2*)L(LD_CIN + (wm * 48 + ch) * 8) = cmul(pw, h);
    }
    f32x2 pre[NP];
    u32 hso[NP];
    cnt_wait(C_PRE, 1);
#pragma unroll
    for (int p = 0; p < NP; p++) {
      pre[p] = cmul(cc[p], ldc(sg + pr[p] * 128));
      const int cu = s0 + pr[p];
      hso[p] = WS_HS + ((c * 8 + 2 * wm) * 40 + (cu / 10) * 10 + ((cu % 10) >> 1) + (lane >> 5) * 5) * 1024 + (cu & 1) * 512 + (lane & 31) * 16;
    }
    STAMP(12);
    cnt_add(C_S2, lane);
    const u32 tw = LD_TRB + wm * 2048 + li * 32 + ((q ^ (li >> 2)) & 3) * 8;
    const u32 tr0 = LD_TRB + wm * 2048 + (q >> 1) * 1024 + ((q & 1) * 8 + (li >> 2)) * 32;
    const u32 tra = tr0 + (((lane & 3) ^ (2 * (q & 1))) * 8), trb = tr0 + 128 + (((lane & 3) ^ (2 * (q & 1) + 1)) * 8);
    cnt_wait(C_S2, 4);
    STAMP(6);
    bf16x8 o0, o1;
#pragma unroll
    for (int p = 0; p <= NP; p++) {
      f32x2 hs[8];
      if (p < NP) {
        const u32 ci = LD_CIN + (pr[p] * 16 + li) * 8;
        f32x2 h = cfma(aq[p], (ldc(ci) + ldc(ci + 384)) + (ldc(ci + 768) + ldc(ci + 1152)), pre[p]);
#pragma unroll
        for (int t = 0; t < 8; t++) hs[t] = h = cfma(ca[p], h, f32x2{acc[t >> 2][2 * p][t & 3], acc[t >> 2][2 * p + 1][t & 3]});
      }
      SCHED0();
      if (p > 0) {
        gst(__builtin_bit_cast(u32x4, o0), wsr, hso[p - 1]);
        gst(__builtin_bit_cast(u32x4, o1), wsr, hso[p - 1] + 40 * 1024);
      }
      if (p < NP) {
        asm volatile("" ::: "memory");
#pragma unroll
        for (int m = 0; m < 2; m++) {
          *(LAS u32x2*)L(tw + m * 512) = u32x2{pk2(hs[4 * m].x, hs[4 * m + 1].x), pk2(hs[4 * m + 2].x, hs[4 * m + 3].x)};
          *(LAS u32x2*)L(tw + m * 512 + 1024) = u32x2{pk2(hs[4 * m].y, hs[4 * m + 1].y), pk2(hs[4 * m + 2].y, hs[4 * m + 3].y)};
        }
        asm volatile("" ::: "memory");
        o0 = ldtr(tra, trb);
        o1 = ldtr(tra + 512, trb + 512);
      }
      SCHED0();
    }
    if (c == 15 && j == 0 && wm == 0) {
      const u64 e = P.epoch + 1;
      __builtin_amdgcn_raw_buffer_store_b64(u32x2{(u32)e, (u32)(e >> 32)}, wsr, lane == 0 ? 0 : WS_END, 0, SC1);
    }
  }
}

__device__ __forceinline__ void phase2_m(const Args& a, int c, int j, int wave, int lane, u32 ta, u32 tb, u32x4 (&w)[5][5]) {
  const int wm = wave & 3, li = lane & 15, q = lane >> 4;
  const rsrc_t wsr = mk_rsrc(a.ws, WS_END);
  const int rh = j & 1, i2 = j >> 1, dcol0 = i2 * 80;
  STAMP(6);
  const u32 wo = WS_WT + (i2 * 54 + wm * 14) * 5120;
  auto ldw = [&](int set, int k) {
#pragma unroll
    for (int n = 0; n < 5; n++) w[set][n] = gld(wsr, lane * 16, wo + (((k < 12 || wm < 3) ? k : 0) * 5 + n) * 1024);
  };
  asm volatile("" ::: "memory");
  ldw(0, 7);
  ldw(1, 8);
  SCHED0();
  wait_vm<10>();
  if (cnt_add(C_F1, lane) == 3) put_flag(wsr, WS_F1 + (c * 16 + j) * 128, ta, tb, lane);
  cnt_add(C_B2, lane);
  __builtin_amdgcn_s_setprio(GPRIO);
  STAMP(5);
  const int tt = 4 * rh + wm, r = lane >> 2;
  u32 oo = (c * 128 + (tt >> 1) * 32 + 8 * (r >> 2) + 4 * (tt & 1) + (r & 3)) * 640 + dcol0 + (lane & 3) * 4;
  u32x2 xv[5], sk[5];
  {
#pragma unroll
    for (int n = 0; n < 5; n++) {
      xv[n] = *(const GAS u32x2*)((const GAS u16*)a.x + oo + 16 * n);
      sk[n] = *(const GAS u32x2*)((const GAS u16*)a.dsk + dcol0 + (lane & 3) * 4 + 16 * n);
    }
    const f32x2 sr = {*(LAS float*)L(LD_ST2 + (tt * 16 + r) * 4), *(LAS float*)L(LD_ST2 + 512 + (tt * 16 + r) * 4)};
#pragma unroll
    for (int n = 0; n < 5; n++) {
      const f32x4 t = rbf4(rbf4(unpk4(xv[n]) - sr.x) * sr.y) * unpk4(sk[n]);
      sk[n] = u32x2{pk2(t.x, t.y), pk2(t.z, t.w)};
      PIN(sk[n]); PIN(xv[n]);
    }
    PIN(oo);
    asm volatile("" ::"v"(sr));
  }
  polln(wsr, WS_F1 + (c * 16 + 4 * wm) * 128, 3, ta, tb, lane);
  STAMP(7);
  f32x4 g[4][5];
  {
    u32 wb0 = wm * 5120 + lane * 16, wb1 = wb0 + 61440, wb2 = wb0 + 122880;
    PIN(wb0); PIN(wb1); PIN(wb2);
    constexpr int KO[14] = {3, 4, 7, 5, 6, 8, 0, 9, 1, 2, 10, 11, 12, 13};
    constexpr int RS[14] = {2, 2, 0, 2, 2, 1, 2, 0, 2, 2, 1, 0, 2, 1};
    auto rd1 = [&](int k, int n) {
      w[2][n] = __builtin_bit_cast(u32x4, ld128((k < 3 ? wb0 : k < 6 ? wb1 : wb2) + ((k % 3) * 20480 + n * 1024)));
    };
    constexpr int AW = GAW;
    u32x4 hf[AW][4];
    const u32 hb = WS_H1 + ((c * 8 + 4 * rh) * 54 + wm * 14) * 1024 + lane * 16;
    auto lda = [&](int s) {
#pragma unroll
      for (int m = 0; m < 4; m++) hf[s % AW][m] = gld(wsr, hb, (((m + wm) & 3) * 54 + ((s < 12 || wm < 3) ? KO[s] : 0)) * 1024);
    };
#pragma unroll
    for (int s = 0; s < AW; s++) lda(s);
    SCHED0();
#pragma unroll
    for (int m = 0; m < 4; m++)
#pragma unroll
      for (int n = 0; n < 5; n++) g[m][n] = f32x4{0.f, 0.f, 0.f, 0.f};
    cnt_wait(C_B2, 8);
#pragma unroll
    for (int n = 0; n < 5; n++) rd1(KO[0], n);
    STAMPJ(1);
#pragma unroll
    for (int s = 0; s < 14; s++) {
      int nx = -1;
#pragma unroll
      for (int t = 13; t > s; t--)
        if (RS[t] == RS[s]) nx = KO[t];
      if (s < 12 || wm < 3) {
#pragma unroll
        for (int n = 0; n < 5; n++) {
          mfma4(g[0][n], g[1][n], g[2][n], g[3][n], w[RS[s]][n], hf[s % AW]);
          if (nx >= 0 && nx < 7) { SCHED0(); rd1(nx, n); SCHED0(); }
        }
        SCHED0();
        if (s >= 3) STAMPJ(s - 1);
        if (s + AW < 14) lda(s + AW);
        if (nx >= 7) ldw(RS[s], nx);
        if (s == 3) cnt_add(C_G2 + wm, lane);
        SCHED0();
      }
    }
  }
  mfma_done();
  __builtin_amdgcn_s_setprio(TPRIO);
  STAMP(8);
#pragma unroll
  for (int m = 1; m < 4; m++)
#pragma unroll
    for (int n = 0; n < 5; n++) *(LAS f32x4*)L(((m - 1) * 4 + wm) * 5120 + n * 1024 + lane * 16) = g[m][n];
  cnt_add(C_G, lane);
  const float rstd = *(LAS float*)L(LD_ST2 + 512 + (tt * 16 + li) * 4);
  cnt_wait(C_G, 4);
  STAMP(9);
  STAMP(10);
  const u32 gq = ((wm + 3) & 3) * 5120;
#pragma unroll
  for (int n = 0; n < 5; n++) {
    const u32 gp = n * 1024 + lane * 16;
    g[0][n] = (g[0][n] + ld4(gp + gq)) + (ld4(gp + (4 + ((wm + 2) & 3)) * 5120) + ld4(gp + (8 + ((wm + 1) & 3)) * 5120));
  }
  asm volatile("" ::: "memory");
#pragma unroll
  for (int n = 0; n < 5; n++) {
    const f32x4 gb = rbf4(g[0][n] * rstd);
    f32x4 sg;
#pragma unroll
    for (int e2 = 0; e2 < 4; e2++) sg[e2] = gb[e2] * __builtin_amdgcn_rcpf(1.0f + __builtin_amdgcn_exp2f(gb[e2] * -1.4426950408889634f));
    *(LAS u32x2*)L(gq + n * 512 + li * 32 + q * 8) = u32x2{pk2(sg.x, sg.y), pk2(sg.z, sg.w)};
  }
  asm volatile("" ::"v"(rstd) : "memory");
  f32x4 fa[5], fb[5];
#pragma unroll
  for (int n = 0; n < 5; n++) {
    const f32x4 gt = unpk4(*(LAS u32x2*)L(gq + n * 512 + lane * 8));
    fa[n] = unpk4(xv[n]) + unpk4(sk[n]) * gt;
    fb[n] = gt + gt;
    PIN(fa[n]); PIN(fb[n]);
  }
  STAMP(11);
  cnt_wait(C_Y, 4);
  STAMP(12);
  const rsrc_t outr = mk_rsrc(a.out, 2048 * 640 * 4);
#pragma unroll
  for (int n = 0; n < 5; n++) {
    const u32 yp = (3 + wm) * 20480 + n * 1024 + (lane >> 2) * 64 + ((lane ^ (lane >> 3)) & 3) * 16;
    const f32x4 y = (ld4(yp) + ld4(yp + 5120)) + (ld4(yp + 10240) + ld4(yp + 15360));
    __builtin_amdgcn_raw_buffer_store_b128(__builtin_bit_cast(u32x4, fa[n] + fb[n] * y), outr, oo * 4 + n * 64, 0, OAUX);
  }
  STAMP(13);
}

__device__ __forceinline__ void phase2_s(const Args& a, int c, int j, int wave, int lane, u32 ta, u32 tb, u32x4 (&cf)[5][5]) {
  const int wm = wave & 3;
  const rsrc_t wsr = mk_rsrc(a.ws, WS_END);
  const int rh = j & 1;
  STAMP(7);
  asm volatile("" ::: "memory");
#pragma unroll
  for (int k = CPRE; k < 5; k++)
#pragma unroll
    for (int n = 0; n < 5; n++) cf[k][n] = gld(wsr, lane * 16, WS_CT + ((j >> 1) * 200 + wm * 50 + k * 5 + n) * 1024);
  SCHED0();
  wait_vm<25 - 5 * CPRE>();
  if (cnt_add(C_F2, lane) == 3) put_flag(wsr, WS_F2 + (c * 16 + j) * 128, ta, tb, lane);
  STAMP(8);
  f32x4 y[4][5];
  {
    u32 cb0 = (12 + wm) * 5120 + lane * 16, cb1 = cb0 + 40960;
    PIN(cb0); PIN(cb1);
    __builtin_amdgcn_s_setprio(DPRIO);
    cnt_wait(C_G2 + wm, 1);
#pragma unroll
    for (int k = 0; k < 4; k++) {
#pragma unroll
      for (int b = 0; b < 5; b++)
        dma16_sc1(wsr, ((3 + k) * 4 + wm) * 5120 + b * 1024, lane * 16, WS_CT + ((j >> 1) * 200 + wm * 50 + 25 + k * 5 + b) * 1024);
      if (k < 3) wait_vm<DPACE>();
    }
    polln(wsr, WS_F2 + (c * 16 + 4 * wm) * 128, 3, ta, tb, lane);
    __builtin_amdgcn_s_setprio(YPRIO);
    STAMP(9);
    STAMPJ(1);
    constexpr int AW = YAW;
    u32x4 hf[AW][4];
    const u32 hb = WS_HS + ((c * 8 + 4 * rh) * 40 + wm * 10) * 1024 + lane * 16;
#pragma unroll
    for (int k = 0; k < AW; k++)
#pragma unroll
      for (int m = 0; m < 4; m++) hf[k][m] = gld(wsr, hb, (m * 40 + k) * 1024);
    SCHED0();
#pragma unroll
    for (int m = 0; m < 4; m++)
#pragma unroll
      for (int n = 0; n < 5; n++) y[m][n] = f32x4{0.f, 0.f, 0.f, 0.f};
#pragma unroll
    for (int k = 0; k < 10; k++) {
      const int r = k < 5 ? k : k < 9 ? k - 4 : 0;
#pragma unroll
      for (int n = 0; n < 5; n++) mfma4(y[0][n], y[1][n], y[2][n], y[3][n], cf[r][n], hf[k % AW]);
      SCHED0();
      STAMPJ(2 + k);
      if (k + AW < 10) {
#pragma unroll
        for (int m = 0; m < 4; m++) hf[k % AW][m] = gld(wsr, hb, (m * 40 + k + AW) * 1024);
      }
#pragma unroll
      for (int n = 0; n < 5; n++) {
        if (k == 0) cf[0][n] = gld(wsr, lane * 16, WS_CT + ((j >> 1) * 200 + wm * 50 + 45 + n) * 1024);
        else if (k < 5) cf[k][n] = __builtin_bit_cast(u32x4, ld128((k < 3 ? cb0 : cb1) + (((k - 1) & 1) * 20480 + n * 1024)));
      }
      SCHED0();
    }
  }
  mfma_done();
  STAMP(10);
#pragma unroll
  for (int m = 0; m < 4; m++)
#pragma unroll
    for (int n = 0; n < 5; n++) *(LAS f32x4*)L(((3 + m) * 4 + wm) * 5120 + n * 1024 + (lane & 15) * 64 + (((lane >> 4) ^ (lane >> 1)) & 3) * 16) = y[m][n];
  cnt_add(C_Y, lane);
  STAMP(11);
}

__global__ __launch_bounds__(512, 2) void s5_fused(Args a) {
  extern __shared__ __attribute__((aligned(16))) char smem[];
  const int b = blockIdx.x, xcd = b & 7, r = b >> 3;
  const int c = (xcd & 3) * 4 + (r >> 3), j = (xcd >> 2) * 8 + (r & 7);
  const int wave = __builtin_amdgcn_readfirstlane(threadIdx.x >> 6), lane = threadIdx.x & 63;
  if ((u32)(size_t)(lptr)smem != 0) return;
  const u64 pc = __builtin_amdgcn_s_getpc() & ~(u64)127;
  STAMP(0);
  Pre P;
  u32 ta, tb;
  head(a, P, pc, c, j, wave, lane, ta, tb);
  u32x4 cf[5][5];
  if (wave >= 4) {
    if (j & 1) phase1<true, 4>(a, P, c, j, wave, lane, ta, tb, cf);
    else phase1<true, 3>(a, P, c, j, wave, lane, ta, tb, cf);
    phase2_m(a, c, j, wave, lane, ta, tb, cf);
  } else {
    if (j & 1) phase1<false, 2>(a, P, c, j, wave, lane, ta, tb, cf);
    else phase1<false, 3>(a, P, c, j, wave, lane, ta, tb, cf);
    phase2_s(a, c, j, wave, lane, ta, tb, cf);
  }
}
}

extern "C" void kernel_launch(void* const* d_in, const int* in_sizes, int n_in, void* d_out, int out_size, void* d_ws,
                              size_t ws_size, hipStream_t stream) {
  using namespace s5k;
  Args a;
  a.x = (const u16*)d_in[0];
  a.lre = (const u16*)d_in[1];
  a.lim = (const u16*)d_in[2];
  a.bre = (const u16*)d_in[3];
  a.bim = (const u16*)d_in[4];
  a.cre = (const u16*)d_in[5];
  a.cim = (const u16*)d_in[6];
  a.ldt = (const u16*)d_in[7];
  a.dsk = (const u16*)d_in[8];
  a.w1 = (const u16*)d_in[10];
  a.w2 = (const u16*)d_in[11];
  a.out = (float*)d_out;
  a.ws = (char*)d_ws;
  (void)hipFuncSetAttribute((const void*)s5_fused, hipFuncAttributeMaxDynamicSharedMemorySize, LDS_BYTES);
  s5_fused<<<256, 512, LDS_BYTES, stream>>>(a);
}
```

```cpp
#include <hip/hip_runtime.h>

namespace s5k {
typedef unsigned short u16;
typedef unsigned int u32;
typedef unsigned long long u64;
typedef short bf16x8 __attribute__((ext_vector_type(8)));
typedef short s16x2 __attribute__((ext_vector_type(2)));
typedef float f32x4 __attribute__((ext_vector_type(4)));
typedef float f32x2 __attribute__((ext_vector_type(2)));
typedef u32 u32x4 __attribute__((ext_vector_type(4)));
typedef u32 u32x2 __attribute__((ext_vector_type(2)));
typedef __bf16 bf16x2 __attribute__((ext_vector_type(2)));
#define LAS __attribute__((address_space(3)))
#define GAS __attribute__((address_space(1)))
typedef LAS char* lptr;
typedef __amdgpu_buffer_rsrc_t rsrc_t;

constexpr u32 WS_STATS = 4096;
constexpr u32 WS_AGG = 65536;
constexpr u32 WS_F1 = 262144, WS_F2 = WS_F1 + 32768, WS_F3 = WS_F2 + 32768;
constexpr u32 WS_H1 = 1u << 20;
constexpr u32 WS_HS = 9u << 20;
constexpr u32 WS_CT = 16u << 20;
constexpr u32 WS_WT = 18u << 20;
constexpr u32 WS_END = 21u << 20;
constexpr int CHUNK = 40960;
constexpr int LD_TRB = 143360, LD_STG = LD_TRB + 4096, LD_ST2 = LD_TRB + 8192, LD_CS = LD_ST2 + 1024, LD_AGW = LD_CS + 1024,
              LD_CIN = LD_AGW + 1536, LD_CNT = LD_CIN + 1536, LD_TAB = LD_CNT + 256, TABP = 144, LD_END = LD_TAB + 48 * TABP;
constexpr int LDS_BYTES = 163840;
static_assert(LD_END <= LDS_BYTES, "lds");
enum { T_A = 0, T_C = 1, T_8 = 2, T_32 = 6, T_128 = 10, T_512 = 14 };
enum { C_F1 = 0, C_B2 = 1, C_S1 = 2, C_S2 = 3, C_F2 = 4, C_G = 5, C_PRE = 6, C_Y = 7, C_G2 = 8 };
constexpr int SC1 = 16;
constexpr int CTAIL = 2;
constexpr u32 SPIN_CAP = 1u << 16;

struct Args {
  const u16 *x, *lre, *lim, *bre, *bim, *cre, *cim, *ldt, *dsk, *w1, *w2;
  float* out;
  char* ws;
};

__device__ __forceinline__ float u2f(u32 v) { return __builtin_bit_cast(float, v); }
__device__ __forceinline__ u32 f2u(float v) { return __builtin_bit_cast(u32, v); }
__device__ __forceinline__ float bf2f(u32 v) { return __builtin_bit_cast(float, v << 16); }
__device__ __forceinline__ float bfhi(u32 v) { return __builtin_bit_cast(float, v & 0xffff0000u); }
__device__ __forceinline__ u32 pk2(float a, float b) { return __builtin_bit_cast(u32, __builtin_convertvector(f32x2{a, b}, bf16x2)); }
__device__ __forceinline__ float rbf(float a) { return bf2f(pk2(a, a)); }
__device__ __forceinline__ f32x4 unpk4(u32x2 v) { return f32x4{bf2f(v.x), bfhi(v.x), bf2f(v.y), bfhi(v.y)}; }
__device__ __forceinline__ f32x4 rbf4(f32x4 v) { return unpk4(u32x2{pk2(v.x, v.y), pk2(v.z, v.w)}); }
__device__ __forceinline__ u32 relu2(u32 v) {
  return __builtin_bit_cast(u32, __builtin_elementwise_max(__builtin_bit_cast(s16x2, v), s16x2{0, 0}));
}
__device__ __forceinline__ f32x2 cmul(f32x2 a, f32x2 b) {
  float x, y;
  asm("v_mul_f32 %0, %2, %4\n\tv_mul_f32 %1, %2, %5\n\tv_fma_f32 %0, -%3, %5, %0\n\tv_fma_f32 %1, %3, %4, %1"
      : "=&v"(x), "=&v"(y) : "v"(a.x), "v"(a.y), "v"(b.x), "v"(b.y));
  return f32x2{x, y};
}
__device__ __forceinline__ f32x2 cfma(f32x2 a, f32x2 b, f32x2 c) {
  float x, y;
  asm("v_fma_f32 %0, %2, %4, %6\n\tv_fma_f32 %1, %2, %5, %7\n\tv_fma_f32 %0, -%3, %5, %0\n\tv_fma_f32 %1, %3, %4, %1"
      : "=&v"(x), "=&v"(y) : "v"(a.x), "v"(a.y), "v"(b.x), "v"(b.y), "v"(c.x), "v"(c.y));
  return f32x2{x, y};
}
__device__ __forceinline__ rsrc_t mk_rsrc(const void* p, u32 bytes) { return __builtin_amdgcn_make_buffer_rsrc((void*)p, 0, bytes, 0x00020000); }
__device__ __forceinline__ f32x4 mfma(bf16x8 a, bf16x8 b, f32x4 c) { return __builtin_amdgcn_mfma_f32_16x16x32_bf16(a, b, c, 0, 0, 0); }
__device__ __forceinline__ void mfma4(f32x4& c0, f32x4& c1, f32x4& c2, f32x4& c3, u32x4 a, const u32x4 (&b)[4]) {
  asm volatile("s_nop 1\n\tv_mfma_f32_16x16x32_bf16 %0, %4, %5, %0\n\tv_mfma_f32_16x16x32_bf16 %1, %4, %6, %1\n\t"
               "v_mfma_f32_16x16x32_bf16 %2, %4, %7, %2\n\tv_mfma_f32_16x16x32_bf16 %3, %4, %8, %3"
               : "+v"(c0), "+v"(c1), "+v"(c2), "+v"(c3) : "v"(a), "v"(b[0]), "v"(b[1]), "v"(b[2]), "v"(b[3]));
}
__device__ __forceinline__ void mfma_done() {
  __builtin_amdgcn_sched_barrier(0);
  asm volatile("s_nop 7\n\ts_nop 3" ::: "memory");
  __builtin_amdgcn_sched_barrier(0);
}
__device__ __forceinline__ bf16x8 asfrag(u32x4 v) { return __builtin_bit_cast(bf16x8, v); }
__device__ __forceinline__ void dma16(rsrc_t rs, u32 dst, u32 voff, u32 soff) {
  asm volatile("s_mov_b32 m0, %0\n\ts_nop 0\n\tbuffer_load_dwordx4 %1, %2, %3 offen lds" ::"s"(dst), "v"(voff), "s"(rs), "s"(soff) : "memory");
}
__device__ __forceinline__ void dma16_sc1(rsrc_t rs, u32 dst, u32 voff, u32 soff) {
  asm volatile("s_mov_b32 m0, %0\n\ts_nop 0\n\tbuffer_load_dwordx4 %1, %2, %3 offen sc1 lds" ::"s"(dst), "v"(voff), "s"(rs), "s"(soff) : "memory");
}
template <int DOFF> __device__ __forceinline__ void gdma(const char* base, u32 dst, u32 voff) {
  asm volatile("s_add_i32 m0, %0, %1\n\ts_nop 0\n\tglobal_load_lds_dwordx4 %2, %3 sc1" ::"s"(dst), "i"(DOFF), "v"(voff), "s"(base) : "memory", "scc");
}
__device__ __forceinline__ void dma4_sc1(rsrc_t rs, u32 dst, u32 voff, u32 soff) {
  asm volatile("s_mov_b32 m0, %0\n\ts_nop 0\n\tbuffer_load_dword %1, %2, %3 offen sc1 lds" ::"s"(dst), "v"(voff), "s"(rs), "s"(soff) : "memory");
}
__device__ __forceinline__ void dma4_plain(rsrc_t rs, u32 dst, u32 voff, u32 soff) {
  asm volatile("s_mov_b32 m0, %0\n\ts_nop 0\n\tbuffer_load_dword %1, %2, %3 offen lds" ::"s"(dst), "v"(voff), "s"(rs), "s"(soff) : "memory");
}
__device__ __forceinline__ lptr L(u32 addr) { return (lptr)(size_t)addr; }
__device__ __forceinline__ bf16x8 ld128(u32 p) { return *(LAS bf16x8*)L(p); }
__device__ __forceinline__ f32x2 ldc(u32 p) { return *(LAS f32x2*)L(p); }
__device__ __forceinline__ f32x4 ld4(u32 p) { return *(LAS f32x4*)L(p); }
__device__ __forceinline__ bf16x8 ldtr(u32 p0, u32 p1) {
  typedef short v4i16 __attribute__((ext_vector_type(4)));
  v4i16 a = __builtin_amdgcn_ds_read_tr16_b64_v4i16((LAS v4i16*)L(p0));
  v4i16 b = __builtin_amdgcn_ds_read_tr16_b64_v4i16((LAS v4i16*)L(p1));
  return __builtin_shufflevector(a, b, 0, 1, 2, 3, 4, 5, 6, 7);
}
__device__ __forceinline__ u32x4 gld(rsrc_t rs, u32 voff, u32 soff) { return __builtin_amdgcn_raw_buffer_load_b128(rs, voff, soff, SC1); }
__device__ __forceinline__ void gst(u32x4 v, rsrc_t rs, u32 voff) { __builtin_amdgcn_raw_buffer_store_b128(v, rs, voff, 0, SC1); }
template <int N> __device__ __forceinline__ void wait_vm() { asm volatile("s_waitcnt vmcnt(%0)" ::"i"(N) : "memory"); }
__device__ __forceinline__ void wait_lgkm0() { __builtin_amdgcn_s_waitcnt(0xc07f); asm volatile("" ::: "memory"); }
__device__ __forceinline__ void wg_barrier() {
  asm volatile("" ::: "memory");
  __builtin_amdgcn_s_barrier();
  asm volatile("" ::: "memory");
}
#define PIN(x) asm volatile("" : "+v"(x))
#define SCHED0() __builtin_amdgcn_sched_barrier(0)
__device__ __forceinline__ u32 cnt_add(int idx, int lane) {
  u32 old;
  const u32 addr = LD_CNT + idx * 4 + (lane == 0 ? 0 : 64), one = 1;
  asm volatile("s_waitcnt lgkmcnt(0)\n\ts_mov_b64 exec, 1\n\tds_add_rtn_u32 %0, %1, %2\n\ts_mov_b64 exec, -1\n\ts_waitcnt lgkmcnt(0)"
               : "=&v"(old) : "v"(addr), "v"(one) : "memory");
  return __builtin_amdgcn_readfirstlane(old);
}
__device__ __forceinline__ void cnt_wait(int idx, u32 want) {
  const u32 addr = LD_CNT + idx * 4;
#pragma unroll 1
  for (u32 s = 0; s < (SPIN_CAP << 4); s++) {
    u32 v;
    asm volatile("ds_read_b32 %0, %1\n\ts_waitcnt lgkmcnt(0)" : "=&v"(v) : "v"(addr) : "memory");
    if (__builtin_amdgcn_readfirstlane(v) >= want) break;
    __builtin_amdgcn_s_sleep(1);
  }
}
__device__ __forceinline__ void polln(rsrc_t wsr, u32 off, int mask, u32 ta, u32 tb, int lane) {
  const u32 vo = off + lane * 128;
#pragma unroll 1
  for (u32 s = 0; s < SPIN_CAP; s++) {
    asm volatile("" ::: "memory");
    u32x2 v = {ta, tb};
    if (lane <= mask) v = __builtin_amdgcn_raw_buffer_load_b64(wsr, vo, 0, SC1);
    if (__all((v.x == ta) & (v.y == tb))) break;
    __builtin_amdgcn_s_sleep(2);
  }
  __builtin_amdgcn_fence(__ATOMIC_ACQUIRE, "wavefront");
  asm volatile("" ::: "memory");
}
__device__ __forceinline__ void put_flag(rsrc_t wsr, u32 off, u32 ta, u32 tb, int lane) {
  __builtin_amdgcn_raw_buffer_store_b64(u32x2{ta, tb}, wsr, lane == 0 ? off : WS_END, 0, SC1);
}
template <int CTRL> __device__ __forceinline__ float dpp(float x) {
  return __builtin_bit_cast(float, __builtin_amdgcn_mov_dpp(__builtin_bit_cast(int, x), CTRL, 0xf, 0xf, true));
}
__device__ __forceinline__ float rows_sum(float x) {
  auto s = __builtin_amdgcn_permlane16_swap(f2u(x), f2u(x), false, false);
  const u32 a = s[0], b = s[1];
  x = u2f(a) + u2f(b);
  auto t = __builtin_amdgcn_permlane32_swap(f2u(x), f2u(x), false, false);
  const u32 c = t[0], d = t[1];
  return u2f(c) + u2f(d);
}
__device__ __forceinline__ float wave_sum(float v) {
  v += dpp<0xB1>(v); v += dpp<0x4E>(v); v += dpp<0x124>(v); v += dpp<0x128>(v);
  return rows_sum(v);
}
__device__ __forceinline__ float fexp(float x) {
  const float hi = x * 1.4426950408889634f;
  const float lo = __builtin_fmaf(x, 1.4426950408889634f, -hi) + x * 1.925963033500011e-8f;
  return __builtin_amdgcn_exp2f(hi) * (1.0f + lo * 0.6931471805599453f);
}
__device__ __forceinline__ void fsincos(float x, float& sn, float& cs) {
  if (__builtin_expect(__any(!(__builtin_fabsf(x) < 2000.f)), 0)) { sincosf(x, &sn, &cs); return; }
  const float n = __builtin_rintf(x * 0.6366197723675814f);
  float r = __builtin_fmaf(n, -1.5703125f, x);
  r = __builtin_fmaf(n, -4.837512969970703125e-4f, r);
  r = __builtin_fmaf(n, -7.54978995489188e-8f, r);
  const float r2 = r * r;
  const float s = __builtin_fmaf(r * r2, __builtin_fmaf(r2, __builtin_fmaf(r2, -1.9515295891e-4f, 8.3321608736e-3f), -1.6666654611e-1f), r);
  const float c = __builtin_fmaf(r2 * r2, __builtin_fmaf(r2, __builtin_fmaf(r2, 2.443315711809948e-5f, -1.388731625493765e-3f), 4.166664568298827e-2f),
                                 __builtin_fmaf(r2, -0.5f, 1.0f));
  const int k = (int)n;
  const float s1 = (k & 1) ? c : s, c1 = (k & 1) ? s : c;
  sn = (k & 2) ? -s1 : s1;
  cs = ((k + 1) & 2) ? -c1 : c1;
}

struct Pre {
  u64 epoch;
  const char* gb[5];
  u32 dvo[5], dstep[4];
  u32 dd0, dd1;
  int kc;
};
__device__ __forceinline__ void head(const Args& a, Pre& P, int c, int j, int wave, int lane, u32& ta, u32& tb) {
  const int li = lane & 15, r8 = lane >> 3, sl = lane & 7, i2 = j >> 1, tB = j & 1;
  const int nS = 3 - tB, nM = 3 + tB;
  const int s0 = 5 * i2 + 3 * tB;
  const int m0 = 7 * i2 + ((i2 & 1) ? 3 * tB : 4 - 4 * tB);
  int kc = c + j;
  kc -= kc >= 20 ? 20 : kc >= 10 ? 10 : 0;
  kc = kc == 10 ? 0 : kc;
  u64 pbre = (u64)a.bre, pbim = (u64)a.bim, pw1 = (u64)a.w1;
  asm volatile("" : "+s"(pbre), "+s"(pbim), "+s"(pw1) : "s"(a.x), "s"(a.ws), "s"(a.lre), "s"(a.lim), "s"(a.ldt));
  u32x2 ep;
  u32x4 xr0, xr1;
  u32 plr, pli, pdt;
  const GAS char* xrow = (const GAS char*)a.x + (size_t)(c * 128 + j * 8 + wave) * 1280;
  asm volatile("global_load_dwordx2 %0, %1, %2 sc1" : "=v"(ep) : "v"(0), "s"(a.ws) : "memory");
  asm volatile("global_load_dwordx4 %0, %1, %2" : "=v"(xr0) : "v"(lane * 16), "s"(xrow) : "memory");
  asm volatile("global_load_dwordx4 %0, %1, %2 offset:1024" : "=v"(xr1) : "v"(li * 16), "s"(xrow) : "memory");
  const int ch = (s0 * 16 + (lane < 16 * nS ? lane : li)) * 2;
  asm volatile("global_load_ushort %0, %1, %2" : "=v"(plr) : "v"(ch), "s"(a.lre) : "memory");
  asm volatile("global_load_ushort %0, %1, %2" : "=v"(pli) : "v"(ch), "s"(a.lim) : "memory");
  asm volatile("global_load_ushort %0, %1, %2" : "=v"(pdt) : "v"(ch), "s"(a.ldt) : "memory");
  P.dd0 = wave * 2048;
  P.dd1 = 16384 + wave * 3072;
  P.dstep[0] = 128;
#pragma unroll
  for (int b = 0; b < 2; b++) {
    P.gb[b] = (const char*)a.x + (c * 128 + (wave >> 1) * 32 + b * 16 + (wave & 1) * 4) * 1280;
    P.dvo[b] = (8 * (r8 >> 2) + (r8 & 3)) * 1280 + ((sl ^ (b * 4 + (r8 >> 1))) << 4);
  }
  gdma<0>(P.gb[0] + kc * 128, P.dd0, P.dvo[0]);
  gdma<1024>(P.gb[1] + kc * 128, P.dd0, P.dvo[1]);
#pragma unroll
  for (int t = 0; t < 3; t++) {
    const int k = wave * 3 + t, b = 2 + t;
    const bool isw = k >= 4 * nS;
    const int sub = k & 3;
    const u32 bvo = r8 * 1280 + ((sl ^ ((sub & 1) * 4 + (r8 >> 1))) << 4);
    const u32 idx = (k - 4 * nS) * 64 + lane;
    const u32 kr = (idx * (tB ? 32768u : 43691u)) >> 19, cpos = idx - kr * (4 * nM);
    const u32 sw = (kr & (3 * tB)) | (((kr >> 3) & 1) << (2 * tB));
    u32 col16 = m0 * 4 + (((cpos >> 1) ^ sw) << 1) + (cpos & 1);
    col16 -= col16 >= 216 ? 8 : 0;
    P.gb[b] = (const char*)(isw ? pw1 : sub < 2 ? pbre : pbim) + (isw ? 0 : ((s0 + (k >> 2)) * 16 + (sub & 1) * 8) * 1280);
    P.dvo[b] = isw ? kr * 3456 + col16 * 16 : bvo;
    P.dstep[b - 1] = isw ? 64 * 3456 : 128;
    if (t == 0) gdma<0>(P.gb[b] + kc * P.dstep[b - 1], P.dd1, P.dvo[b]);
    if (t == 1) gdma<1024>(P.gb[b] + kc * P.dstep[b - 1], P.dd1, P.dvo[b]);
    if (t == 2) gdma<2048>(P.gb[b] + kc * P.dstep[b - 1], P.dd1, P.dvo[b]);
  }
  kc = kc == 9 ? 0 : kc + 1;
  {
    u64 pc;
    int len;
    asm volatile("s_getpc_b64 %0\n.Ls5k_here:\n\ts_mov_b32 %1, .Ls5k_tail-.Ls5k_here" : "=s"(pc), "=s"(len));
    const int part = (blockIdx.x >> 3) & 7;
    dma4_plain(mk_rsrc((const void*)pc, wave == 7 ? max(len, 0) : 0), LD_TRB + 6144, (part * 64 + lane) * 128, 0);
  }
  *(LAS u32*)L(LD_CNT + li * 4) = 0;
  const rsrc_t wsr = mk_rsrc(a.ws, WS_END);
  auto issue1 = [&]() {
    gdma<CHUNK>(P.gb[0] + kc * 128, P.dd0, P.dvo[0]);
    gdma<CHUNK + 1024>(P.gb[1] + kc * 128, P.dd0, P.dvo[1]);
    gdma<CHUNK>(P.gb[2] + kc * P.dstep[1], P.dd1, P.dvo[2]);
    gdma<CHUNK + 1024>(P.gb[3] + kc * P.dstep[2], P.dd1, P.dvo[3]);
    gdma<CHUNK + 2048>(P.gb[4] + kc * P.dstep[3], P.dd1, P.dvo[4]);
  };
  asm volatile("s_waitcnt vmcnt(6)" : "+v"(ep), "+v"(xr0), "+v"(xr1), "+v"(plr), "+v"(pli), "+v"(pdt)::"memory");
  P.kc = kc == 9 ? 0 : kc + 1;
  {
    P.epoch = (u64)(u32)__builtin_amdgcn_readfirstlane(ep.y) << 32 | (u32)__builtin_amdgcn_readfirstlane(ep.x);
    u64 z = P.epoch + 0x9E3779B97F4A7C15ull;
    z = (z ^ (z >> 30)) * 0xBF58476D1CE4E5B9ull;
    z = (z ^ (z >> 27)) * 0x94D049BB133111EBull;
    z ^= z >> 31;
    ta = (u32)z;
    tb = (u32)(z >> 32);
    float v[16];
#pragma unroll
    for (int e = 0; e < 4; e++) {
      v[2 * e] = bf2f(xr0[e]); v[2 * e + 1] = bfhi(xr0[e]);
      v[8 + 2 * e] = bf2f(xr1[e]); v[8 + 2 * e + 1] = bfhi(xr1[e]);
    }
    float s0_ = 0.f, s1_ = 0.f;
#pragma unroll
    for (int e = 0; e < 8; e++) { s0_ += v[e]; s1_ += v[8 + e]; }
    const float mean = wave_sum(lane < 16 ? s0_ + s1_ : s0_) * (1.0f / 640.0f);
    s0_ = 0.f; s1_ = 0.f;
#pragma unroll
    for (int e = 0; e < 8; e++) {
      const float d0 = v[e] - mean, d1 = v[8 + e] - mean;
      s0_ += d0 * d0; s1_ += d1 * d1;
    }
    const float var = wave_sum(lane < 16 ? s0_ + s1_ : s0_) * (1.0f / 640.0f);
    const float mb = rbf(mean), den = rbf(__builtin_amdgcn_sqrtf(rbf(rbf(var) + 1e-5f)));
    gst(u32x4{f2u(mb), ta, f2u(__builtin_amdgcn_rcpf(den)), tb}, wsr, lane == 0 ? WS_STATS + (c * 128 + j * 8 + wave) * 16 : WS_END);
  }
  issue1();
  {
    if (wave == 0) {
      const float lr = bf2f(plr), lim = bf2f(pli);
      const float dt = rbf(fexp(bf2f(pdt)));
      const float er = fexp(dt * lr);
      float sn, cn;
      fsincos(dt * lim, sn, cn);
      const f32x2 a1 = cmul(f32x2{er, 0.f}, f32x2{cn, sn});
      const float nr = a1.x - 1.0f, dn = 1.0f / (lr * lr + lim * lim);
      const u32 tp = LD_TAB + (lane < 48 ? lane : 47) * TABP;
      if (lane < 16 * nS) {
        const f32x2 cf_ = cmul(f32x2{nr, a1.y}, f32x2{lr * dn, -lim * dn});
        *(LAS f32x4*)L(tp) = f32x4{a1.x, a1.y, cf_.x, cf_.y};
        const f32x2 a2 = cmul(a1, a1), a4 = cmul(a2, a2);
        f32x2 b = cmul(a4, a4);
#pragma unroll
        for (int i = 0; i < 4; i++) {
          const f32x2 b2 = cmul(b, b), b3 = cmul(b2, b);
          *(LAS f32x4*)L(tp + 16 + i * 32) = f32x4{1.f, 0.f, b.x, b.y};
          *(LAS f32x4*)L(tp + 32 + i * 32) = f32x4{b2.x, b2.y, b3.x, b3.y};
          b = cmul(b2, b2);
        }
      }
    }
  }
}

template <bool IS_M, int NP>
__device__ __forceinline__ void phase1(const Args& a, Pre& P, int c, int j, int wave, int lane, u32 ta, u32 tb, u32x4 (&cf)[5][5]) {
  constexpr int NU = 2 * NP;
  constexpr bool TYPE_B = IS_M ? (NP == 4) : (NP == 2);
  constexpr int nS = TYPE_B ? 2 : 3, nM = 6 - nS, MP = nM * 64;
  const int wm = wave & 3;
  const int i2 = j >> 1;
  const int s0 = 5 * i2 + (TYPE_B ? 3 : 0);
  const int m0 = 7 * i2 + ((i2 & 1) ? (TYPE_B ? 3 : 0) : (TYPE_B ? 0 : 4));
  const int li = lane & 15, q = lane >> 4;
  const rsrc_t wsr = mk_rsrc(a.ws, WS_END);
  u32 kc = P.kc;
  auto dma1 = [&](int bufoff, int b) {
    const char* src = P.gb[b] + kc * P.dstep[b < 2 ? 0 : b - 1];
    const u32 d = b < 2 ? P.dd0 : P.dd1;
#define GD(B, O) if (bufoff == B && (b < 2 ? b : b - 2) == O) gdma<B + O * 1024>(src, d, P.dvo[b]);
    GD(0, 0) GD(0, 1) GD(0, 2) GD(CHUNK, 0) GD(CHUNK, 1) GD(CHUNK, 2) GD(2 * CHUNK, 0) GD(2 * CHUNK, 1) GD(2 * CHUNK, 2)
#undef GD
  };
  const int rid = c * 2 + (j & 1);

  int pr[NP];
#pragma unroll
  for (int p = 0; p < NP; p++) pr[p] = (p + wm) % NP;
  u32 xb[2][2], wb[2][NU];
  {
    const u32 xo = li * 128 + ((q ^ (li >> 1)) << 4);
    const int qq = li >> 2;
    const u32 rowb = 16384 + nS * 4096 + (8 * q + qq) * MP + (lane & 3) * 8;
    const int f = TYPE_B ? (qq | ((q & 1) << 2)) : (q & 1);
#pragma unroll
    for (int h = 0; h < 2; h++) {
#pragma unroll
      for (int kk = 0; kk < 2; kk++) {
        xb[h][kk] = h * 2 * CHUNK + wm * 4096 + (xo ^ (kk << 6));
        PIN(xb[h][kk]);
      }
#pragma unroll
      for (int u = 0; u < NU; u++) {
        wb[h][u] = h * 2 * CHUNK + (IS_M ? rowb + (((2 * pr[u >> 1] + (u & 1)) ^ f) << 5) : 16384 + pr[u >> 1] * 4096 + (xo ^ ((u & 1) << 6)));
        PIN(wb[h][u]);
      }
    }
  }
  bf16x8 xf[2][2], wf[2][NU];
  auto read1 = [&](int set, int bufoff, int kk, int f) {
    const int h = bufoff == 2 * CHUNK, bo = h ? 0 : bufoff;
    if (f < 2) {
      xf[set][f] = ld128(xb[h][kk] + (bo + f * 2048));
    } else if (f < NU + 2) {
      const int u = f - 2;
      if (!IS_M) wf[set][u] = ld128(wb[h][(u & ~1) + kk] + (bo + (u & 1) * 2048));
      else wf[set][u] = ldtr(wb[h][u] + (bo + kk * 32 * MP), wb[h][u] + (bo + kk * 32 * MP + 4 * MP));
    }
  };
  f32x4 acc[2][NU], csum[2];
#pragma unroll
  for (int m = 0; m < 2; m++) {
#pragma unroll
    for (int u = 0; u < NU; u++) { acc[m][u] = f32x4{0.f, 0.f, 0.f, 0.f}; PIN(acc[m][u]); }
    csum[m] = f32x4{0.f, 0.f, 0.f, 0.f};
    PIN(csum[m]);
  }
  u32x4 ones4 = {0x3f803f80u, 0x3f803f80u, 0x3f803f80u, 0x3f803f80u};
  PIN(ones4);
  constexpr int G = NU + 1;
  constexpr int NDB = IS_M ? 2 : 3;
  auto dpos = [](int d) { return (2 * G * d + (IS_M ? G : 0)) / 5; };
  static_assert((2 * G * (NDB - 1) + (IS_M ? G : 0)) / 5 < G && (2 * G * NDB + (IS_M ? G : 0)) / 5 >= G, "split");
  auto step = [&](int set, int rbuf, int rkk, int dbuf, bool tail) {
#pragma unroll
    for (int g = 0; g < G; g++) {
      if (g > 0) {
        const int u = g - 1;
#pragma unroll
        for (int m = 0; m < 2; m++)
          acc[m][u] = IS_M ? mfma(wf[set][u], xf[set][m], acc[m][u]) : mfma(xf[set][m], wf[set][u], acc[m][u]);
      } else {
        csum[0] = mfma(asfrag(ones4), wf[set][0], csum[0]);
        csum[1] = mfma(asfrag(ones4), wf[set][1], csum[1]);
      }
      if (rbuf >= 0) {
        if (g == 0) { read1(set ^ 1, rbuf, rkk, 2); read1(set ^ 1, rbuf, rkk, 3); }
        else if (g == 1) { read1(set ^ 1, rbuf, rkk, 0); read1(set ^ 1, rbuf, rkk, 1); }
        else read1(set ^ 1, rbuf, rkk, g + 2);
      } else if (g == 3) {
        LAS float* csp = (LAS float*)L(LD_CS + (IS_M ? 512 : 0) + (wm < NP ? wm : 3) * 128 + (lane & 31) * 4);
        float e0 = csum[0].x, e1 = csum[1].x;
        PIN(e0); PIN(e1);
        if (wm < NP) *csp = (q & 1) ? e1 : e0;
      }
      if (!IS_M && tail) {
#pragma unroll
        for (int f = 0; f < 5 * CTAIL; f++)
          if (f * 2 * G / (5 * CTAIL) == (set == 0 ? g : g + G))
            cf[f / 5][f % 5] = gld(wsr, lane * 16, WS_CT + (i2 * 200 + wm * 50 + f) * 1024);
      }
#pragma unroll
      for (int d = 0; d < 5; d++)
        if ((set == 1 ? g : g + G) == dpos(d)) dma1(dbuf, d);
      SCHED0();
    }
  };

#pragma unroll
  for (int d = 0; d < NDB; d++) dma1(2 * CHUNK, d);
  u32x4 rt[2];
  const int rks = min(rid * 2 + (wm & 1), 53);
  if (!IS_M) {
#pragma unroll
    for (int i = 0; i < 2; i++) {
      const int f = min(rid * 8 + wm * 2 + i, 199);
      const int wk = f / 5, n = f - wk * 5, w = wk / 10, k = wk - w * 10;
      const GAS char* src = (const GAS char*)(k < 5 ? a.cre : a.cim) + (i2 * 80 + n * 16) * 1280 + (160 * w + 32 * (k < 5 ? k : k - 5)) * 2;
      asm volatile("global_load_dwordx4 %0, %1, %2" : "=v"(rt[i]) : "v"(li * 1280 + q * 16), "s"(src) : "memory");
    }
    wait_vm<7 + NDB + 2>();
  } else {
    wait_vm<7 + NDB>();
  }
  wait_lgkm0();
  wg_barrier();
  __builtin_amdgcn_s_setprio(IS_M ? 1 : 0);
#pragma unroll
  for (int f = 0; f < NU + 2; f++) read1(0, 0, 0, f);
  int it = 0;
  auto body = [&](int prv, int cur, int nxt) {
    step(0, cur, 1, prv, nxt < 0);
    kc = kc == 9 ? 0 : kc + 1;
    if (nxt < 0) {
      const int r = wave * 16 + li;
      u32x4 g = *(LAS u32x4*)L(LD_STG + r * 16);
      if (!__all((g.y == ta) & (g.w == tb))) {
#pragma unroll 1
        for (u32 s = 0; s < SPIN_CAP; s++) {
          asm volatile("" ::: "memory");
          g = gld(wsr, WS_STATS + (c * 128 + r) * 16, 0);
          if (__all((g.y == ta) & (g.w == tb))) break;
        }
      }
      const int t5 = r & 31;
      *(LAS u32*)L(LD_ST2 + (q & 1) * 512 + ((r & ~31) + (((t5 >> 2) & 1) << 4) + ((t5 >> 3) << 2) + (t5 & 3)) * 4) = (q & 1) ? g.z : g.x;
    } else if (!IS_M && it == 0) wait_vm<7>();
    else if (IS_M && it == 1 && wm < 2) wait_vm<10>();
    else wait_vm<5>();
    wait_lgkm0();
    wg_barrier();
    if (IS_M && it == 0 && wm < 2) {
      const rsrc_t w2r = mk_rsrc(a.w2, 1728 * 1280);
#pragma unroll
      for (int b = 0; b < 5; b++) {
        const int idx = b * 64 + lane, kr = idx / 10, cpos = idx - kr * 10;
        dma16(w2r, 3 * CHUNK + wm * 5120 + b * 1024, kr * 1280 + i2 * 160 + cpos * 16, rks * (32 * 1280));
      }
    }
    if (it == 2) {
      if (!IS_M) {
        asm volatile("" : "+v"(rt[0]), "+v"(rt[1]));
#pragma unroll
        for (int i = 0; i < 2; i++) {
          const int f = min(rid * 8 + wm * 2 + i, 199);
          gst(rt[i] ^ (f % 50 >= 25 ? 0x80008000u : 0u), wsr, WS_CT + (i2 * 200 + f) * 1024 + lane * 16);
        }
      } else if (wm < 2) {
        const u32 p = 3 * CHUNK + wm * 5120 + (4 * q + (li >> 2)) * 160 + (lane & 3) * 8;
#pragma unroll
        for (int n = 0; n < 5; n++)
          gst(__builtin_bit_cast(u32x4, ldtr(p + n * 32, p + n * 32 + 2560)), wsr, WS_WT + ((i2 * 54 + rks) * 5 + n) * 1024 + lane * 16);
      }
    }
    if (IS_M && it == 3 && wm == 3) put_flag(wsr, WS_F3 + (i2 * 32 + rid) * 128, ta, tb, lane);
    if (it >= 7) {
      if (it == 7) {
        const u32 f = *(LAS u32*)L(LD_TRB + wave * 256 + lane * 4);
        if (!__all(f == ((lane & 1) ? tb : ta))) polln(wsr, WS_F3 + i2 * 32 * 128, 31, ta, tb, lane);
#pragma unroll
        for (int b = 0; b < 5; b++) {
          P.gb[b] = a.ws + b * 1024;
          P.dvo[b] = lane * 16;
          if (b < 4) P.dstep[b] = 1024;
        }
        P.dd0 = wave * 5120;
        P.dd1 = wave * 5120 + 2048;
      }
      kc = (WS_WT >> 10) + (i2 * 54 + wm * 14 + (cur / CHUNK) * 2 + (IS_M ? 1 : 0)) * 5;
    }
    SCHED0();
    step(1, nxt, 0, cur, nxt < 0);
    if (IS_M && it == 8) {
#pragma unroll
      for (int b = 0; b < 5; b++) dma16_sc1(wsr, (24 + wm) * 5120 + b * 1024, lane * 16, WS_WT + ((i2 * 54 + wm * 14 + 6) * 5 + b) * 1024);
    }
    if (it == 5) {
      dma4_sc1(wsr, LD_TRB + wave * 256, (lane >> 1) * 128 + (lane & 1) * 4, WS_F3 + i2 * 32 * 128);
      dma4_sc1(wsr, LD_STG + wave * 256, lane * 4, WS_STATS + c * 2048 + wave * 256);
    }
    it++;
  };
#pragma unroll 1
  for (int r = 0; r < 3; r++) {
    body(2 * CHUNK, 0, CHUNK);
    body(0, CHUNK, 2 * CHUNK);
    body(CHUNK, 2 * CHUNK, 0);
  }
  body(2 * CHUNK, 0, -1);
#pragma unroll
  for (int d = NDB; d < 5; d++) dma1(0, d);
  __builtin_amdgcn_s_setprio(IS_M ? 2 : 1);
  float mb[2];
  f32x4 mu[2], rs[2];
  f32x2 ca[NP], cc[NP], aq[NP], a8r[NP];
  if (IS_M) {
#pragma unroll
    for (int m = 0; m < 2; m++) mb[m] = *(LAS float*)L(LD_ST2 + (wm * 32 + m * 16 + li) * 4);
  } else {
#pragma unroll
    for (int m = 0; m < 2; m++) {
      mu[m] = ld4(LD_ST2 + (wm * 32 + m * 16 + 4 * q) * 4);
      rs[m] = ld4(LD_ST2 + 512 + (wm * 32 + m * 16 + 4 * q) * 4);
    }
#pragma unroll
    for (int p = 0; p < NP; p++) {
      const u32 tp = LD_TAB + (pr[p] * 16 + li) * TABP;
      const f32x4 t0 = ld4(tp);
      ca[p] = f32x2{t0.x, t0.y};
      cc[p] = f32x2{t0.z, t0.w};
      aq[p] = cmul(ldc(tp + (T_8 + q) * 8), ldc(tp + (T_32 + wm) * 8));
      a8r[p] = ldc(tp + (T_8 + 3 - q) * 8);
    }
  }
  wait_lgkm0();
  wg_barrier();

  if (IS_M) {
#pragma unroll
    for (int p = 0; p < NP; p++) {
      const f32x4 c0 = ld4(LD_CS + 512 + pr[p] * 128 + q * 16), c1 = ld4(LD_CS + 512 + pr[p] * 128 + 64 + q * 16);
      const u32 off = (m0 + pr[p] < 54) ? WS_H1 + ((c * 8 + 2 * wm) * 54 + m0 + pr[p]) * 1024 : WS_END;
#pragma unroll
      for (int m = 0; m < 2; m++) {
        const f32x4 v0 = acc[m][2 * p] - mb[m] * c0, v1 = acc[m][2 * p + 1] - mb[m] * c1;
        gst(u32x4{relu2(pk2(v0.x, v0.y)), relu2(pk2(v0.z, v0.w)), relu2(pk2(v1.x, v1.y)), relu2(pk2(v1.z, v1.w))}, wsr,
            off + m * 54 * 1024 + lane * 16);
      }
    }
  } else {
    const u32 sg = LD_TRB + ((wm * 4 + q) * 48 + li) * 8;
#pragma unroll
    for (int p = 0; p < NP; p++) {
      const float Sr = *(LAS float*)L(LD_CS + pr[p] * 128 + li * 4), Si = *(LAS float*)L(LD_CS + pr[p] * 128 + 64 + li * 4);
      f32x2 h;
#pragma unroll
      for (int m = 0; m < 2; m++) {
        acc[m][2 * p] = (acc[m][2 * p] - mu[m] * Sr) * rs[m];
        acc[m][2 * p + 1] = (acc[m][2 * p + 1] - mu[m] * Si) * rs[m];
#pragma unroll
        for (int r = 0; r < 4; r++) {
          const f32x2 u = {acc[m][2 * p][r], acc[m][2 * p + 1][r]};
          h = (m | r) ? cfma(ca[p], h, u) : u;
        }
      }
      asm volatile("" ::"v"(Sr), "v"(Si));
      *(LAS f32x2*)L(sg + pr[p] * 128) = h;
      h = cmul(a8r[p], h);
      *(LAS f32x2*)L(LD_AGW + ((wm * 3 + pr[p]) * 16 + li) * 8) = f32x2{rows_sum(h.x), rows_sum(h.y)};
    }
    const int ch = lane < 48 ? lane : 47;
    const u32 tc = LD_TAB + ch * TABP;
    if (cnt_add(C_S1, lane) == 3) {
      const f32x2 a32 = ldc(tc + (T_32 + 1) * 8), a8 = ldc(tc + (T_8 + 1) * 8);
      f32x2 h = ldc(LD_AGW + ch * 8);
#pragma unroll
      for (int w = 1; w < 4; w++) h = cfma(a32, h, ldc(LD_AGW + (w * 48 + ch) * 8));
      gst(u32x4{f2u(h.x), ta, f2u(h.y), tb}, wsr, q < NP ? WS_AGG + (c * 640 + s0 * 16 + lane) * 16 : WS_END);
      SCHED0();
      f32x2 g[15];
#pragma unroll
      for (int i = 0; i < 15; i++) g[i] = ldc(LD_TRB + (i * 48 + ch) * 8);
      *(LAS f32x2*)L(LD_TRB + ch * 8) = f32x2{0.f, 0.f};
#pragma unroll
      for (int i = 0; i < 15; i++) {
        h = i ? cfma(a8, h, g[i]) : g[0];
        *(LAS f32x2*)L(LD_TRB + ((i + 1) * 48 + ch) * 8) = h;
      }
      cnt_add(C_PRE, lane);
    }
    __builtin_amdgcn_s_setprio(2);
    {
      const u32 co = WS_CT + (i2 * 200 + wm * 50) * 1024 + lane * 16;
#pragma unroll
      for (int k = CTAIL; k < 5; k++)
#pragma unroll
        for (int n = 0; n < 5; n++) cf[k][n] = gld(wsr, co, (k * 5 + n) * 1024);
      SCHED0();
    }
    wait_vm<5 * (5 - CTAIL)>();
    cnt_add(C_B2, lane);
#pragma unroll
    for (int p = 0; p < NP; p++) {
      f32x2 cr2 = {cc[p].x, cc[p].x}, ci2 = {cc[p].y, cc[p].y};
      PIN(cr2); PIN(ci2);
      const f32x4 cr4 = {cr2.x, cr2.y, cr2.x, cr2.y}, ci4 = {ci2.x, ci2.y, ci2.x, ci2.y};
#pragma unroll
      for (int m = 0; m < 2; m++) {
        const f32x4 ar = acc[m][2 * p], ai = acc[m][2 * p + 1];
        acc[m][2 * p] = cr4 * ar - ci4 * ai;
        acc[m][2 * p + 1] = cr4 * ai + ci4 * ar;
        PIN(acc[m][2 * p]); PIN(acc[m][2 * p + 1]);
      }
      aq[p] = cmul(cc[p], aq[p]);
    }
    {
      const int nv = min(max(c - 4 * wm, 0), 4), e = max(c - 4 * wm - nv, 0);
      const f32x2 a128 = ldc(tc + (T_128 + 1) * 8), pw = cmul(ldc(tc + (T_128 + (e & 3)) * 8), ldc(tc + (T_512 + (e >> 2)) * 8));
      const u32 go = WS_AGG + (4 * wm * 640 + s0 * 16 + min(lane, 16 * NP - 1)) * 16;
      u32x4 lg[4];
#pragma unroll 1
      for (u32 s = 0; s < SPIN_CAP; s++) {
        asm volatile("" ::: "memory");
        bool ok = true;
#pragma unroll
        for (int k = 0; k < 4; k++) lg[k] = gld(wsr, go, k * 640 * 16);
#pragma unroll
        for (int k = 0; k < 4; k++) ok = ok & (((lg[k].y == ta) & (lg[k].w == tb)) | (k >= nv));
        if (__all(ok)) break;
      }
      f32x2 h = {0.f, 0.f};
#pragma unroll
      for (int k = 0; k < 4; k++)
        if (k < nv) h = cfma(a128, h, f32x2{u2f(lg[k].x), u2f(lg[k].z)});
      *(LAS f32x2*)L(LD_CIN + (wm * 48 + ch) * 8) = cmul(pw, h);
    }
    f32x2 pre[NP];
    u32 hso[NP];
    cnt_wait(C_PRE, 1);
#pragma unroll
    for (int p = 0; p < NP; p++) {
      pre[p] = cmul(cc[p], ldc(sg + pr[p] * 128));
      const int cu = s0 + pr[p];
      hso[p] = WS_HS + ((c * 8 + 2 * wm) * 40 + (cu / 10) * 10 + ((cu % 10) >> 1) + (lane >> 5) * 5) * 1024 + (cu & 1) * 512 + (lane & 31) * 16;
    }
    cnt_add(C_S2, lane);
    const u32 tw = LD_TRB + wm * 2048 + li * 32 + ((q ^ (li >> 2)) & 3) * 8;
    const u32 tr0 = LD_TRB + wm * 2048 + (q >> 1) * 1024 + ((q & 1) * 8 + (li >> 2)) * 32;
    const u32 tra = tr0 + (((lane & 3) ^ (2 * (q & 1))) * 8), trb = tr0 + 128 + (((lane & 3) ^ (2 * (q & 1) + 1)) * 8);
    cnt_wait(C_S2, 4);
    bf16x8 o0, o1;
#pragma unroll
    for (int p = 0; p <= NP; p++) {
      f32x2 hs[8];
      if (p < NP) {
        const u32 ci = LD_CIN + (pr[p] * 16 + li) * 8;
        f32x2 h = cfma(aq[p], (ldc(ci) + ldc(ci + 384)) + (ldc(ci + 768) + ldc(ci + 1152)), pre[p]);
#pragma unroll
        for (int t = 0; t < 8; t++) hs[t] = h = cfma(ca[p], h, f32x2{acc[t >> 2][2 * p][t & 3], acc[t >> 2][2 * p + 1][t & 3]});
      }
      SCHED0();
      if (p > 0) {
        gst(__builtin_bit_cast(u32x4, o0), wsr, hso[p - 1]);
        gst(__builtin_bit_cast(u32x4, o1), wsr, hso[p - 1] + 40 * 1024);
      }
      if (p < NP) {
        asm volatile("" ::: "memory");
#pragma unroll
        for (int m = 0; m < 2; m++) {
          *(LAS u32x2*)L(tw + m * 512) = u32x2{pk2(hs[4 * m].x, hs[4 * m + 1].x), pk2(hs[4 * m + 2].x, hs[4 * m + 3].x)};
          *(LAS u32x2*)L(tw + m * 512 + 1024) = u32x2{pk2(hs[4 * m].y, hs[4 * m + 1].y), pk2(hs[4 * m + 2].y, hs[4 * m + 3].y)};
        }
        asm volatile("" ::: "memory");
        o0 = ldtr(tra, trb);
        o1 = ldtr(tra + 512, trb + 512);
      }
      SCHED0();
    }
    if (c == 15 && j == 0 && wm == 0) {
      const u64 e = P.epoch + 1;
      __builtin_amdgcn_raw_buffer_store_b64(u32x2{(u32)e, (u32)(e >> 32)}, wsr, lane == 0 ? 0 : WS_END, 0, SC1);
    }
  }
}

__device__ __forceinline__ void phase2_m(const Args& a, int c, int j, int wave, int lane, u32 ta, u32 tb, u32x4 (&w)[5][5]) {
  const int wm = wave & 3, li = lane & 15, q = lane >> 4;
  const rsrc_t wsr = mk_rsrc(a.ws, WS_END);
  const int rh = j & 1, i2 = j >> 1, dcol0 = i2 * 80;
  const u32 wo = WS_WT + (i2 * 54 + wm * 14) * 5120;
  auto ldw = [&](int set, int k) {
#pragma unroll
    for (int n = 0; n < 5; n++) w[set][n] = gld(wsr, lane * 16, wo + (((k < 12 || wm < 3) ? k : 0) * 5 + n) * 1024);
  };
  const int tt = 4 * rh + wm, r = lane >> 2;
  u32 oo = (c * 128 + (tt >> 1) * 32 + 8 * (r >> 2) + 4 * (tt & 1) + (r & 3)) * 640 + dcol0 + (lane & 3) * 4;
  u32x2 xv[5], sk[5];
  asm volatile("" ::: "memory");
  wait_vm<0>();
  if (cnt_add(C_F1, lane) == 3) put_flag(wsr, WS_F1 + (c * 16 + j) * 128, ta, tb, lane);
  cnt_add(C_B2, lane);
  __builtin_amdgcn_s_setprio(1);
  ldw(0, 7);
  ldw(1, 8);
#pragma unroll
  for (int n = 0; n < 5; n++) {
    xv[n] = *(const GAS u32x2*)((const GAS u16*)a.x + oo + 16 * n);
    sk[n] = *(const GAS u32x2*)((const GAS u16*)a.dsk + dcol0 + (lane & 3) * 4 + 16 * n);
  }
  SCHED0();
  polln(wsr, WS_F1 + (c * 16 + 4 * wm) * 128, 3, ta, tb, lane);
  f32x4 g[4][5];
  {
    u32 wb0 = wm * 5120 + lane * 16, wb1 = wb0 + 61440, wb2 = wb0 + 122880;
    PIN(wb0); PIN(wb1); PIN(wb2);
    constexpr int KO[14] = {3, 4, 7, 5, 6, 8, 0, 9, 1, 2, 10, 11, 12, 13};
    constexpr int RS[14] = {2, 2, 0, 2, 2, 1, 2, 0, 2, 2, 1, 0, 2, 1};
    auto rd1 = [&](int k, int n) {
      w[2][n] = __builtin_bit_cast(u32x4, ld128((k < 3 ? wb0 : k < 6 ? wb1 : wb2) + ((k % 3) * 20480 + n * 1024)));
    };
    constexpr int AW = 3;
    u32x4 hf[AW][4];
    const u32 hb = WS_H1 + ((c * 8 + 4 * rh) * 54 + wm * 14) * 1024 + lane * 16;
    auto lda = [&](int s) {
#pragma unroll
      for (int m = 0; m < 4; m++) hf[s % AW][m] = gld(wsr, hb, (((m + wm) & 3) * 54 + ((s < 12 || wm < 3) ? KO[s] : 0)) * 1024);
    };
#pragma unroll
    for (int s = 0; s < AW; s++) lda(s);
    SCHED0();
  {
    const f32x2 sr = {*(LAS float*)L(LD_ST2 + (tt * 16 + r) * 4), *(LAS float*)L(LD_ST2 + 512 + (tt * 16 + r) * 4)};
#pragma unroll
    for (int n = 0; n < 5; n++) {
      const f32x4 t = rbf4(rbf4(unpk4(xv[n]) - sr.x) * sr.y) * unpk4(sk[n]);
      sk[n] = u32x2{pk2(t.x, t.y), pk2(t.z, t.w)};
      PIN(sk[n]); PIN(xv[n]);
    }
    PIN(oo);
    asm volatile("" ::"v"(sr));
  }
    SCHED0();
#pragma unroll
    for (int m = 0; m < 4; m++)
#pragma unroll
      for (int n = 0; n < 5; n++) g[m][n] = f32x4{0.f, 0.f, 0.f, 0.f};
    cnt_wait(C_B2, 8);
#pragma unroll
    for (int n = 0; n < 5; n++) rd1(KO[0], n);
#pragma unroll
    for (int s = 0; s < 14; s++) {
      int nx = -1;
#pragma unroll
      for (int t = 13; t > s; t--)
        if (RS[t] == RS[s]) nx = KO[t];
      if (s < 12 || wm < 3) {
#pragma unroll
        for (int n = 0; n < 5; n++) {
          mfma4(g[0][n], g[1][n], g[2][n], g[3][n], w[RS[s]][n], hf[s % AW]);
          if (nx >= 0 && nx < 7) { SCHED0(); rd1(nx, n); SCHED0(); }
        }
        SCHED0();
        if (s + AW < 14) lda(s + AW);
        if (nx >= 7) ldw(RS[s], nx);
        if (s == 3) cnt_add(C_G2 + wm, lane);
        SCHED0();
      }
    }
  }
  mfma_done();
#pragma unroll
  for (int m = 1; m < 4; m++)
#pragma unroll
    for (int n = 0; n < 5; n++) *(LAS f32x4*)L(((m - 1) * 4 + wm) * 5120 + n * 1024 + lane * 16) = g[m][n];
  cnt_add(C_G, lane);
  const float rstd = *(LAS float*)L(LD_ST2 + 512 + (tt * 16 + li) * 4);
  cnt_wait(C_G, 4);
  const u32 gq = ((wm + 3) & 3) * 5120;
#pragma unroll
  for (int n = 0; n < 5; n++) {
    const u32 gp = n * 1024 + lane * 16;
    g[0][n] = (g[0][n] + ld4(gp + gq)) + (ld4(gp + (4 + ((wm + 2) & 3)) * 5120) + ld4(gp + (8 + ((wm + 1) & 3)) * 5120));
  }
  asm volatile("" ::: "memory");
#pragma unroll
  for (int n = 0; n < 5; n++) {
    const f32x4 gb = rbf4(g[0][n] * rstd);
    f32x4 sg;
#pragma unroll
    for (int e2 = 0; e2 < 4; e2++) sg[e2] = gb[e2] * __builtin_amdgcn_rcpf(1.0f + __builtin_amdgcn_exp2f(gb[e2] * -1.4426950408889634f));
    *(LAS u32x2*)L(gq + n * 512 + li * 32 + q * 8) = u32x2{pk2(sg.x, sg.y), pk2(sg.z, sg.w)};
  }
  asm volatile("" ::"v"(rstd) : "memory");
  f32x4 fa[5], fb[5];
#pragma unroll
  for (int n = 0; n < 5; n++) {
    const f32x4 gt = unpk4(*(LAS u32x2*)L(gq + n * 512 + lane * 8));
    fa[n] = unpk4(xv[n]) + unpk4(sk[n]) * gt;
    fb[n] = gt + gt;
    PIN(fa[n]); PIN(fb[n]);
  }
  cnt_wait(C_Y, 4);
  const rsrc_t outr = mk_rsrc(a.out, 2048 * 640 * 4);
#pragma unroll
  for (int n = 0; n < 5; n++) {
    const u32 yp = (3 + wm) * 20480 + n * 1024 + (lane >> 2) * 64 + ((lane ^ (lane >> 3)) & 3) * 16;
    const f32x4 y = (ld4(yp) + ld4(yp + 5120)) + (ld4(yp + 10240) + ld4(yp + 15360));
    __builtin_amdgcn_raw_buffer_store_b128(__builtin_bit_cast(u32x4, fa[n] + fb[n] * y), outr, oo * 4 + n * 64, 0, 2);
  }
}

__device__ __forceinline__ void phase2_s(const Args& a, int c, int j, int wave, int lane, u32 ta, u32 tb, u32x4 (&cf)[5][5]) {
  const int wm = wave & 3;
  const rsrc_t wsr = mk_rsrc(a.ws, WS_END);
  const int rh = j & 1;
  wait_vm<0>();
  if (cnt_add(C_F2, lane) == 3) put_flag(wsr, WS_F2 + (c * 16 + j) * 128, ta, tb, lane);
  f32x4 y[4][5];
  {
    u32 cb0 = (12 + wm) * 5120 + lane * 16, cb1 = cb0 + 40960;
    PIN(cb0); PIN(cb1);
    __builtin_amdgcn_s_setprio(0);
    cnt_wait(C_G2 + wm, 1);
#pragma unroll
    for (int k = 0; k < 4; k++) {
#pragma unroll
      for (int b = 0; b < 5; b++)
        dma16_sc1(wsr, ((3 + k) * 4 + wm) * 5120 + b * 1024, lane * 16, WS_CT + ((j >> 1) * 200 + wm * 50 + 25 + k * 5 + b) * 1024);
    }
    polln(wsr, WS_F2 + (c * 16 + 4 * wm) * 128, 3, ta, tb, lane);
    constexpr int AW = 3;
    u32x4 hf[AW][4];
    const u32 hb = WS_HS + ((c * 8 + 4 * rh) * 40 + wm * 10) * 1024 + lane * 16;
#pragma unroll
    for (int k = 0; k < AW; k++)
#pragma unroll
      for (int m = 0; m < 4; m++) hf[k][m] = gld(wsr, hb, (m * 40 + k) * 1024);
    SCHED0();
#pragma unroll
    for (int m = 0; m < 4; m++)
#pragma unroll
      for (int n = 0; n < 5; n++) y[m][n] = f32x4{0.f, 0.f, 0.f, 0.f};
#pragma unroll
    for (int k = 0; k < 10; k++) {
      const int r = k < 5 ? k : k < 9 ? k - 4 : 0;
#pragma unroll
      for (int n = 0; n < 5; n++) mfma4(y[0][n], y[1][n], y[2][n], y[3][n], cf[r][n], hf[k % AW]);
      SCHED0();
      if (k + AW < 10) {
#pragma unroll
        for (int m = 0; m < 4; m++) hf[k % AW][m] = gld(wsr, hb, (m * 40 + k + AW) * 1024);
      }
#pragma unroll
      for (int n = 0; n < 5; n++) {
        if (k == 0) cf[0][n] = gld(wsr, lane * 16, WS_CT + ((j >> 1) * 200 + wm * 50 + 45 + n) * 1024);
        else if (k < 5) cf[k][n] = __builtin_bit_cast(u32x4, ld128((k < 3 ? cb0 : cb1) + (((k - 1) & 1) * 20480 + n * 1024)));
      }
      SCHED0();
    }
  }
  mfma_done();
#pragma unroll
  for (int m = 0; m < 4; m++)
#pragma unroll
    for (int n = 0; n < 5; n++) *(LAS f32x4*)L(((3 + m) * 4 + wm) * 5120 + n * 1024 + (lane & 15) * 64 + (((lane >> 4) ^ (lane >> 1)) & 3) * 16) = y[m][n];
  cnt_add(C_Y, lane);
}

__global__ __launch_bounds__(512, 2) void s5_fused(Args a) {
  extern __shared__ __attribute__((aligned(16))) char smem[];
  const int b = blockIdx.x, xcd = b & 7, r = b >> 3;
  const int c = (xcd & 3) * 4 + (r >> 3), j = (xcd >> 2) * 8 + (r & 7);
  const int wave = __builtin_amdgcn_readfirstlane(threadIdx.x >> 6), lane = threadIdx.x & 63;
  if ((u32)(size_t)(lptr)smem != 0) return;
  Pre P;
  u32 ta, tb;
  head(a, P, c, j, wave, lane, ta, tb);
  u32x4 cf[5][5];
  if (wave >= 4) {
    if (j & 1) phase1<true, 4>(a, P, c, j, wave, lane, ta, tb, cf);
    else phase1<true, 3>(a, P, c, j, wave, lane, ta, tb, cf);
    phase2_m(a, c, j, wave, lane, ta, tb, cf);
    asm volatile(".Ls5k_tail:");
  } else {
    if (j & 1) phase1<false, 2>(a, P, c, j, wave, lane, ta, tb, cf);
    else phase1<false, 3>(a, P, c, j, wave, lane, ta, tb, cf);
    phase2_s(a, c, j, wave, lane, ta, tb, cf);
  }
}
}

extern "C" void kernel_launch(void* const* d_in, const int* in_sizes, int n_in, void* d_out, int out_size, void* d_ws,
                              size_t ws_size, hipStream_t stream) {
  using namespace s5k;
  Args a;
  a.x = (const u16*)d_in[0];
  a.lre = (const u16*)d_in[1];
  a.lim = (const u16*)d_in[2];
  a.bre = (const u16*)d_in[3];
  a.bim = (const u16*)d_in[4];
  a.cre = (const u16*)d_in[5];
  a.cim = (const u16*)d_in[6];
  a.ldt = (const u16*)d_in[7];
  a.dsk = (const u16*)d_in[8];
  a.w1 = (const u16*)d_in[10];
  a.w2 = (const u16*)d_in[11];
  a.out = (float*)d_out;
  a.ws = (char*)d_ws;
  (void)hipFuncSetAttribute((const void*)s5_fused, hipFuncAttributeMaxDynamicSharedMemorySize, LDS_BYTES);
  s5_fused<<<256, 512, LDS_BYTES, stream>>>(a);
}
```
